```python
import torch

def _softmax(x, axis=-1):
    return torch.softmax(x, dim=axis)
def _gelu(x):
    return torch.nn.functional.gelu(x, approximate="tanh")
def _silu(x):
    return torch.nn.functional.silu(x)
def _sigmoid(x):
    return torch.sigmoid(x)
def _layernorm(x, axis=-1, eps=1e-5):
    m = x.mean(dim=axis, keepdim=True)
    v = x.var(dim=axis, keepdim=True, unbiased=False)
    return (x - m) / torch.sqrt(v + eps)
def _scatter_add(tgt, idx, src, axis=0):
    return tgt.clone().index_add_(axis, idx, src)
def _segment_sum(src, seg, n):
    out = torch.zeros((n,) + src.shape[1:], dtype=src.dtype)
    return out.index_add_(0, seg, src)
def _pad(x, pw):
    flat = tuple(v for pair in reversed(pw) for v in pair)
    return torch.nn.functional.pad(x, flat)

kernel_name = 'standalone__mha_softmax__near_ridge'

def setup_inputs():

    _w = (lambda a: (lambda t: t.to(torch.bfloat16) if t.is_floating_point() else t)(torch.as_tensor(a)))
    B, H, Sq, Sk, D = 16, 16, 736, 512, 256
    return {
        "q": _w((lambda *s: torch.randn(*s, dtype=torch.bfloat16) * 0.8)(B, H, Sq, D)),
        "k": _w((lambda *s: torch.randn(*s, dtype=torch.bfloat16) * 0.8)(B, H, Sk, D)),
        "v": _w((lambda *s: torch.randn(*s, dtype=torch.bfloat16) * 0.05)(B, H, Sk, D)),
    }

def reference(q, k, v):
    scale = q.shape[-1] ** -0.5
    scores = torch.einsum("bhqd,bhkd->bhqk", q, k) * scale
    p = _softmax(scores, axis=-1)
    return torch.einsum("bhqk,bhkd->bhqd", p, v)
```

```cpp
#include <hip/hip_runtime.h>
#include <hip/hip_bf16.h>

constexpr int NB = 16, NH = 16, SQ = 736, SK = 512, D = 256;
using bf16x8 = __attribute__((ext_vector_type(8))) __bf16;
using s16x4 = __attribute__((ext_vector_type(4))) short;
using s16x8 = __attribute__((ext_vector_type(8))) short;
using f32x16 = __attribute__((ext_vector_type(16))) float;
using u32x4 = __attribute__((ext_vector_type(4))) unsigned;
using u32x2 = __attribute__((ext_vector_type(2))) unsigned;
typedef float f32x2 __attribute__((ext_vector_type(2)));
typedef __bf16 bf16x2 __attribute__((ext_vector_type(2)));

__device__ __forceinline__ unsigned cvtpk(float lo, float hi) {
  f32x2 v = {lo, hi}; bf16x2 b = __builtin_convertvector(v, bf16x2); return __builtin_bit_cast(unsigned, b);
}
__device__ __forceinline__ unsigned koff(unsigned row, unsigned ch) {
  return 4096u * (row >> 3) + 512u * (ch >> 2) + 64u * (row & 7) + 16u * ((ch & 3) ^ ((row >> 2) & 3));
}
__device__ __forceinline__ unsigned voff(unsigned key, unsigned ch) {
  return 512u * ((key >> 3) * 8 + (ch >> 2)) + 64u * (key & 7) + 16u * (ch & 3);
}

__global__ __launch_bounds__(256, 1) void attn0(const unsigned short* __restrict__ Q, const unsigned short* __restrict__ K,
                                                const unsigned short* __restrict__ V, unsigned short* __restrict__ O) {
  __shared__ __attribute__((aligned(16))) char lds[32768];
  char* Kl = lds; char* Vl = lds + 16384;
  const int tid = threadIdx.x, lane = tid & 63, wid = tid >> 6, r32 = lane & 31, hi = lane >> 5;
  const int head = blockIdx.y, qb = blockIdx.x;
  int row = qb * 128 + wid * 32 + r32;
  const bool valid = (qb * 128 + wid * 32) < SQ;
  if (!valid) row = r32;
  const unsigned short* Qp = Q + ((size_t)head * SQ + row) * D + hi * 8;
  bf16x8 qr[16];
#pragma unroll
  for (int s = 0; s < 16; ++s) qr[s] = *(const bf16x8*)(Qp + s * 16);
  f32x16 o[8];
#pragma unroll
  for (int i = 0; i < 8; ++i) o[i] = f32x16{};
  float m = -1e30f, l = 0.f;
  const float C = 0.0625f * 1.4426950408889634f;
  const unsigned short* Kh = K + (size_t)head * SK * D;
  const unsigned short* Vh = V + (size_t)head * SK * D;
  const unsigned vbase = (unsigned)(size_t)Vl + (hi << 8) + (((lane >> 2) & 3) << 6) + (((lane >> 4) & 1) << 5) + ((lane & 3) << 3);
  for (int t = 0; t < SK / 32; ++t) {
    __syncthreads();
#pragma unroll
    for (int i = 0; i < 4; ++i) {
      int n = tid + 256 * i; int rr = n >> 5, ch = n & 31;
      u32x4 kv = *(const u32x4*)(Kh + (size_t)(t * 32 + rr) * D + ch * 8);
      u32x4 vv = *(const u32x4*)(Vh + (size_t)(t * 32 + rr) * D + ch * 8);
      *(u32x4*)(Kl + koff(rr, ch)) = kv;
      *(u32x4*)(Vl + voff(rr, ch)) = vv;
    }
    __syncthreads();
    f32x16 s = f32x16{};
#pragma unroll
    for (int ks = 0; ks < 16; ++ks) {
      bf16x8 kf = *(const bf16x8*)(Kl + koff(r32, 2 * ks + hi));
      s = __builtin_amdgcn_mfma_f32_32x32x16_bf16(kf, qr[ks], s, 0, 0, 0);
    }
    float pm = s[0];
#pragma unroll
    for (int r = 1; r < 16; ++r) pm = fmaxf(pm, s[r]);
    pm = fmaxf(pm, __shfl_xor(pm, 32));
    float mn = fmaxf(m, pm);
    float alpha = __builtin_amdgcn_exp2f((m - mn) * C);
    m = mn;
    float ps = 0.f;
#pragma unroll
    for (int r = 0; r < 16; ++r) { s[r] = __builtin_amdgcn_exp2f((s[r] - mn) * C); ps += s[r]; }
    ps += __shfl_xor(ps, 32);
    l = l * alpha + ps;
#pragma unroll
    for (int i = 0; i < 8; ++i)
#pragma unroll
      for (int r = 0; r < 16; ++r) o[i][r] *= alpha;
    bf16x8 pf[2];
#pragma unroll
    for (int ks = 0; ks < 2; ++ks) {
      u32x4 w;
#pragma unroll
      for (int p = 0; p < 4; ++p) w[p] = cvtpk(s[8 * ks + 2 * p], s[8 * ks + 2 * p + 1]);
      pf[ks] = __builtin_bit_cast(bf16x8, w);
    }
#pragma unroll
    for (int db = 0; db < 8; ++db) {
#pragma unroll
      for (int ks = 0; ks < 2; ++ks) {
        s16x4 lo, hi4;
        asm volatile("ds_read_b64_tr_b16 %0, %2 offset:%3\n\tds_read_b64_tr_b16 %1, %2 offset:%4\n\ts_waitcnt lgkmcnt(0)"
                     : "=&v"(lo), "=&v"(hi4) : "v"(vbase), "i"(512 * ((2 * ks) * 8 + db)), "i"(512 * ((2 * ks + 1) * 8 + db)) : "memory");
        s16x8 vf = __builtin_shufflevector(lo, hi4, 0, 1, 2, 3, 4, 5, 6, 7);
        o[db] = __builtin_amdgcn_mfma_f32_32x32x16_bf16(__builtin_bit_cast(bf16x8, vf), pf[ks], o[db], 0, 0, 0);
      }
    }
  }
  if (valid) {
    float il = 1.f / l;
    unsigned short* Op = O + ((size_t)head * SQ + row) * D + hi * 4;
#pragma unroll
    for (int db = 0; db < 8; ++db)
#pragma unroll
      for (int g = 0; g < 4; ++g) {
        u32x2 w = {cvtpk(o[db][4 * g] * il, o[db][4 * g + 1] * il), cvtpk(o[db][4 * g + 2] * il, o[db][4 * g + 3] * il)};
        *(u32x2*)(Op + db * 32 + g * 8) = w;
      }
  }
}

extern "C" void kernel_launch(void* const* d_in, const int* in_sizes, int n_in, void* d_out, int out_size, void* d_ws, size_t ws_size,
                              hipStream_t stream) {
  attn0<<<dim3(6, NB * NH), 256, 0, stream>>>((const unsigned short*)d_in[0], (const unsigned short*)d_in[1], (const unsigned short*)d_in[2],
                                               (unsigned short*)d_out);
}
```

```cpp
#include <hip/hip_runtime.h>
#include <hip/hip_bf16.h>

constexpr int SQ = 736, SK = 512, D = 256;
using bf16x8 = __attribute__((ext_vector_type(8))) __bf16;
using s16x4 = __attribute__((ext_vector_type(4))) short;
using s16x8 = __attribute__((ext_vector_type(8))) short;
using f32x16 = __attribute__((ext_vector_type(16))) float;
using u32x4 = __attribute__((ext_vector_type(4))) unsigned;
using u32x8 = __attribute__((ext_vector_type(8))) unsigned;
using u32x2 = __attribute__((ext_vector_type(2))) unsigned;
typedef float f32x2 __attribute__((ext_vector_type(2)));
typedef __bf16 bf16x2 __attribute__((ext_vector_type(2)));

__device__ __forceinline__ unsigned cvtpk(float lo, float hi) {
  f32x2 v = {lo, hi}; bf16x2 b = __builtin_convertvector(v, bf16x2); return __builtin_bit_cast(unsigned, b);
}
__device__ __forceinline__ unsigned koff(unsigned row, unsigned ch) {
  return 4096u * (row >> 3) + 512u * (ch >> 2) + 64u * (row & 7) + 16u * ((ch & 3) ^ ((row >> 2) & 3));
}
__device__ __forceinline__ unsigned voff(unsigned key, unsigned ch) {
  return 512u * ((key >> 3) * 8 + (ch >> 2)) + 64u * (key & 7) + 16u * (ch & 3);
}

__device__ __forceinline__ void safe_rows(const unsigned short* __restrict__ Q, const unsigned short* __restrict__ K,
                                          const unsigned short* __restrict__ V, unsigned short* __restrict__ O, int head, int row0, char* lds, int tid) {
  char* Kl = lds; char* Vl = lds + 16384;
  const int lane = tid & 63, wid = tid >> 6, r32 = lane & 31, hi = lane >> 5;
  int row = row0 + wid * 32 + r32;
  const bool valid = (row0 + wid * 32) < SQ;
  if (!valid) row = r32;
  const unsigned short* Qp = Q + ((size_t)head * SQ + row) * D + hi * 8;
  bf16x8 qr[16];
#pragma unroll
  for (int s = 0; s < 16; ++s) qr[s] = *(const bf16x8*)(Qp + s * 16);
  f32x16 o[8];
#pragma unroll
  for (int i = 0; i < 8; ++i) o[i] = f32x16{};
  float m = -__builtin_inff(), l = 0.f;
  const float C = 0.0625f * 1.4426950408889634f;
  const unsigned short* Kh = K + (size_t)head * SK * D;
  const unsigned short* Vh = V + (size_t)head * SK * D;
  const unsigned vbase = (unsigned)(size_t)Vl + (hi << 8) + (((lane >> 2) & 3) << 6) + (((lane >> 4) & 1) << 5) + ((lane & 3) << 3);
  for (int t = 0; t < SK / 32; ++t) {
    __syncthreads();
#pragma unroll
    for (int i = 0; i < 4; ++i) {
      int n = tid + 256 * i; int rr = n >> 5, ch = n & 31;
      u32x4 kv = *(const u32x4*)(Kh + (size_t)(t * 32 + rr) * D + ch * 8);
      u32x4 vv = *(const u32x4*)(Vh + (size_t)(t * 32 + rr) * D + ch * 8);
      *(u32x4*)(Kl + koff(rr, ch)) = kv;
      *(u32x4*)(Vl + voff(rr, ch)) = vv;
    }
    __syncthreads();
    f32x16 s = f32x16{};
#pragma unroll
    for (int ks = 0; ks < 16; ++ks) {
      bf16x8 kf = *(const bf16x8*)(Kl + koff(r32, 2 * ks + hi));
      s = __builtin_amdgcn_mfma_f32_32x32x16_bf16(kf, qr[ks], s, 0, 0, 0);
    }
    float pm = s[0];
#pragma unroll
    for (int r = 1; r < 16; ++r) pm = fmaxf(pm, s[r]);
    pm = fmaxf(pm, __shfl_xor(pm, 32));
    float mn = fmaxf(m, pm);
    float mref = (mn == -__builtin_inff()) ? 0.f : mn;
    float alpha = __builtin_amdgcn_exp2f((m - mref) * C);
    m = mn;
    float ps = 0.f;
#pragma unroll
    for (int r = 0; r < 16; ++r) { s[r] = __builtin_amdgcn_exp2f((s[r] - mref) * C); ps += s[r]; }
    ps += __shfl_xor(ps, 32);
    l = l * alpha + ps;
#pragma unroll
    for (int i = 0; i < 8; ++i)
#pragma unroll
      for (int r = 0; r < 16; ++r) o[i][r] *= alpha;
    bf16x8 pf[2];
#pragma unroll
    for (int ks = 0; ks < 2; ++ks) {
      u32x4 w;
#pragma unroll
      for (int p = 0; p < 4; ++p) w[p] = cvtpk(s[8 * ks + 2 * p], s[8 * ks + 2 * p + 1]);
      pf[ks] = __builtin_bit_cast(bf16x8, w);
    }
#pragma unroll
    for (int db = 0; db < 8; ++db) {
#pragma unroll
      for (int ks = 0; ks < 2; ++ks) {
        s16x4 lo, hi4;
        asm volatile("ds_read_b64_tr_b16 %0, %2 offset:%3\n\tds_read_b64_tr_b16 %1, %2 offset:%4\n\ts_waitcnt lgkmcnt(0)"
                     : "=&v"(lo), "=&v"(hi4) : "v"(vbase), "i"(512 * ((2 * ks) * 8 + db)), "i"(512 * ((2 * ks + 1) * 8 + db)) : "memory");
        s16x8 vf = __builtin_shufflevector(lo, hi4, 0, 1, 2, 3, 4, 5, 6, 7);
        o[db] = __builtin_amdgcn_mfma_f32_32x32x16_bf16(__builtin_bit_cast(bf16x8, vf), pf[ks], o[db], 0, 0, 0);
      }
    }
  }
  if (valid) {
    float il = 1.f / l;
    unsigned short* Op = O + ((size_t)head * SQ + row) * D + hi * 4;
#pragma unroll
    for (int db = 0; db < 8; ++db)
#pragma unroll
      for (int g = 0; g < 4; ++g) {
        u32x2 w = {cvtpk(o[db][4 * g] * il, o[db][4 * g + 1] * il), cvtpk(o[db][4 * g + 2] * il, o[db][4 * g + 3] * il)};
        *(u32x2*)(Op + db * 32 + g * 8) = w;
      }
  }
}

__global__ __launch_bounds__(256, 1) void mha_fwd(const unsigned short* __restrict__ Q, const unsigned short* __restrict__ K,
                                                  const unsigned short* __restrict__ V, unsigned short* __restrict__ O) {
  __shared__ __attribute__((aligned(1024))) char lds[163840];
  u32x8 ka;
  asm volatile("s_load_dwordx8 %0, %1, 0x0" : "={s[64:71]}"(ka) : "s"(__builtin_amdgcn_kernarg_segment_ptr()));
  __builtin_amdgcn_sched_barrier(0);
  const unsigned wid = __builtin_amdgcn_readfirstlane(threadIdx.x >> 6);
  const unsigned id0 = 96u * (blockIdx.x & 7u) + (blockIdx.x >> 3);
  unsigned kv[3], qo[3], lim[3];
#pragma unroll
  for (int r = 0; r < 3; ++r) {
    unsigned id = id0 + 32u * r, head = id / 3u, j = id - 3u * head;
    kv[r] = head * (SK * 512u) + (head & 3u) * 128u; qo[r] = (head * SQ + j * 256u) * 512u; lim[r] = (head + 1u) * (SQ * 512u);
  }
  const unsigned id2 = id0 + 64u, part = (id2 % 3u) * 4u + wid;
  const unsigned vpf = (id2 / 3u) * (SK * 512u) + min(part * 24576u, SK * 512u - 24576u) + 256u * (1u ^ (((id2 / 3u) >> 1) & 1u));
  unsigned fail;
  asm volatile(
    "v_mbcnt_lo_u32_b32 v16, -1, 0\n"
    "v_mbcnt_hi_u32_b32 v16, -1, v16\n"
    "s_getpc_b64 s[48:49]\n"
    "L_pc_%=:\n"
    "s_and_b32 s49, s49, 0xffff\n"
    "s_mov_b32 s50, L_end_%=-L_pc_%=\n"
    "s_mov_b32 s51, 0x20000\n"
    "v_lshlrev_b32 v17, 7, v16\n"
    "s_lshl_b32 s93, %5, 13\n"
    "buffer_load_dword v160, v17, s[48:51], s93 offen\n"
    "s_add_u32 s85, s93, 0x8000\n"
    "buffer_load_dword v161, v17, s[48:51], s85 offen\n"
    "s_add_u32 s96, s48, L_blk_%=-L_pc_%=\n"
    "s_addc_u32 s97, s49, 0\n"
    "s_mov_b32 s98, L_end_%=-L_blk_%=\n"
    "s_mov_b32 s99, 0x20000\n"
    "s_mov_b32 s35, L_tail_%=-L_blk_%=\n"
    "s_mov_b32 s47, 0x20000\n"
    "s_mov_b32 s39, 0x20000\n"
    "s_mov_b32 s43, 0x20000\n"
    "s_mov_b32 s38, 0x4000000\n"
    "s_mov_b32 s42, 0x4000000\n"
    "s_mov_b32 s50, 0\n"
    "s_mov_b32 s46, %13\n"
    "s_mov_b32 s84, %5\n"
    "s_mov_b32 s89, %6\n"
    "s_mov_b32 s72, %7\n"
    "s_mov_b32 s76, %10\n"
    "s_mov_b32 s80, %13\n"
    "s_mov_b32 s73, %8\n"
    "s_mov_b32 s77, %11\n"
    "s_mov_b32 s81, %14\n"
    "s_mov_b32 s74, %9\n"
    "s_mov_b32 s78, %12\n"
    "s_mov_b32 s82, %15\n"
    "s_mov_b32 s75, 0\n"
    "s_mov_b32 s79, 0\n"
    "s_mov_b32 s83, 0\n"
    "s_lshl_b32 s90, s84, 12\n"
    "s_lshl_b32 s91, s84, 14\n"
    "s_lshr_b32 s85, s84, 1\n"
    "s_lshl_b32 s92, s85, 15\n"
    "s_and_b32 s85, s84, 1\n"
    "s_lshl_b32 s85, s85, 12\n"
    "s_add_u32 s92, s92, s85\n"
    "s_mov_b32 s56, s90\n"
    "s_add_u32 s53, s72, s90\n"
    "s_andn2_b32 s53, s53, 0x180\n"
    "v_bfe_u32 v17, v16, 2, 3\n"
    "v_lshlrev_b32 v3, 9, v17\n"
    "v_lshrrev_b32 v17, 5, v16\n"
    "v_lshl_add_u32 v3, v17, 6, v3\n"
    "v_and_b32 v17, 3, v16\n"
    "v_lshl_add_u32 v4, v17, 4, v3\n"
    "s_and_b32 s85, s84, 1\n"
    "s_lshl_b32 s85, s85, 5\n"
    "v_xor_b32 v3, s85, v4\n"
    "v_and_b32 v17, 16, v16\n"
    "v_xor_b32 v4, v17, v4\n"
    "v_and_b32 v17, 15, v16\n"
    "v_lshlrev_b32 v5, 9, v17\n"
    "v_lshrrev_b32 v18, 4, v16\n"
    "v_lshl_add_u32 v5, v18, 4, v5\n"
    "s_lshl_b32 s85, s93, 2\n"
    "s_add_u32 s60, s76, s85\n"
    "s_add_u32 s61, s60, 0x2000\n"
    "s_add_u32 s62, s60, 0x4000\n"
    "s_add_u32 s63, s60, 0x6000\n"
    "s_waitcnt lgkmcnt(0)\n"
    "s_mov_b32 s36, s66\n"
    "s_and_b32 s37, s67, 0xffff\n"
    "s_mov_b32 s44, s64\n"
    "s_and_b32 s45, s65, 0xffff\n"
    "s_mov_b32 s40, s68\n"
    "s_and_b32 s41, s69, 0xffff\n"
    "s_mov_b32 s48, s70\n"
    "s_and_b32 s49, s71, 0xffff\n"
    "s_add_u32 m0, s56, 0x10000\n"
    "s_add_u32 s57, s53, 0x0\n"
    "buffer_load_dwordx4 v3, s[36:39], s57 offen offset:0 lds\n"
    "s_add_u32 m0, s56, 0x10380\n"
    "s_add_u32 s57, s53, 0x0\n"
    "buffer_load_dwordx4 v3, s[36:39], s57 offen offset:128 lds\n"
    "s_add_u32 m0, s56, 0x10700\n"
    "s_add_u32 s57, s53, 0x0\n"
    "buffer_load_dwordx4 v3, s[36:39], s57 offen offset:256 lds\n"
    "s_add_u32 m0, s56, 0x10a80\n"
    "s_add_u32 s57, s53, 0x0\n"
    "buffer_load_dwordx4 v3, s[36:39], s57 offen offset:384 lds\n"
    "buffer_load_dwordx4 a[0:3], v5, s[44:47], s60 offen offset:0\n"
    "buffer_load_dwordx4 a[4:7], v5, s[44:47], s60 offen offset:64\n"
    "s_add_u32 m0, s56, 0x14000\n"
    "s_add_u32 s57, s53, 0x4000\n"
    "buffer_load_dwordx4 v3, s[36:39], s57 offen offset:0 lds\n"
    "s_add_u32 m0, s56, 0x14380\n"
    "s_add_u32 s57, s53, 0x4000\n"
    "buffer_load_dwordx4 v3, s[36:39], s57 offen offset:128 lds\n"
    "buffer_load_dwordx4 a[8:11], v5, s[44:47], s60 offen offset:128\n"
    "buffer_load_dwordx4 a[12:15], v5, s[44:47], s60 offen offset:192\n"
    "v_bfe_u32 v17, v16, 3, 1\n"
    "v_lshlrev_b32 v0, 12, v17\n"
    "v_lshlrev_b32 v17, 1, v17\n"
    "v_xor_b32 v17, v17, v18\n"
    "v_lshl_add_u32 v0, v17, 4, v0\n"
    "v_and_b32 v17, 7, v16\n"
    "v_lshl_add_u32 v0, v17, 6, v0\n"
    "v_lshrrev_b32 v17, 1, v18\n"
    "v_lshlrev_b32 v1, 10, v17\n"
    "v_and_b32 v17, 1, v18\n"
    "v_mul_u32_u24 v17, 272, v17\n"
    "v_add_u32 v1, v17, v1\n"
    "v_bfe_u32 v17, v16, 1, 3\n"
    "v_lshl_add_u32 v1, v17, 5, v1\n"
    "v_and_b32 v17, 1, v16\n"
    "v_lshl_add_u32 v1, v17, 3, v1\n"
    "v_xor_b32 v2, 16, v1\n"
    "v_mov_b32 v12, 0x3f803f80\n"
    "v_mov_b32 v13, 0x3f803f80\n"
    "v_mov_b32 v14, 0x3f803f80\n"
    "v_mov_b32 v15, 0x3f803f80\n"
    "s_mov_b32 s64, 0\n"
    "s_mov_b32 s65, 0\n"
    "s_mov_b32 s66, 0\n"
    "s_mov_b32 s67, 0\n"
    "s_mov_b32 s68, 0\n"
    "s_mov_b32 s69, 0\n"
    "s_mov_b32 s70, 0\n"
    "s_mov_b32 s71, 0\n"
    "v_and_b32 v17, 7, v16\n"
    "v_lshlrev_b32 v6, 9, v17\n"
    "v_bfe_u32 v17, v16, 3, 1\n"
    "v_lshl_add_u32 v6, v17, 6, v6\n"
    "v_lshl_add_u32 v6, v18, 4, v6\n"
    "s_mov_b32 s52, 0x3db8aa3b\n"
    "s_mov_b32 s86, 0x27800000\n"
    "s_mov_b32 s87, 0x1d000000\n"
    "s_mov_b32 s59, 0\n"
    "s_mov_b32 s95, 1\n"
    "s_mov_b32 s58, 3\n"
    "v_add_u32 v232, 0x10000, v0\n"
    "v_add_u32 v233, 0x20000, v0\n"
    "s_branch L_p0_%=\n"
    ".p2align 12\n"
    "L_p0_%=:\n"
    "s_waitcnt vmcnt(6)\n"
    "s_barrier\n"
    "ds_read_b128 a[208:211], v232 offset:0\n"
    "ds_read_b128 a[212:215], v232 offset:512\n"
    "ds_read_b128 a[216:219], v232 offset:1024\n"
    "ds_read_b128 a[220:223], v232 offset:1536\n"
    "ds_read_b128 a[224:227], v232 offset:2048\n"
    "ds_read_b128 a[228:231], v232 offset:2560\n"
    "ds_read_b128 a[232:235], v232 offset:3072\n"
    "ds_read_b128 a[236:239], v232 offset:3584\n"
    "ds_read_b128 a[176:179], v232 offset:8192\n"
    "ds_read_b128 a[180:183], v232 offset:8704\n"
    "ds_read_b128 a[184:187], v232 offset:9216\n"
    "ds_read_b128 a[188:191], v232 offset:9728\n"
    "ds_read_b128 v[96:99], v232 offset:10240\n"
    "ds_read_b128 v[100:103], v232 offset:10752\n"
    "ds_read_b128 v[104:107], v232 offset:11264\n"
    "ds_read_b128 v[108:111], v232 offset:11776\n"
    "s_waitcnt vmcnt(9)\n"
    "s_add_u32 m0, s56, 0x14700\n"
    "s_add_u32 s57, s53, 0x4000\n"
    "buffer_load_dwordx4 v3, s[36:39], s57 offen offset:256 lds\n"
    "s_waitcnt vmcnt(9)\n"
    "s_add_u32 m0, s56, 0x14a80\n"
    "s_add_u32 s57, s53, 0x4000\n"
    "buffer_load_dwordx4 v3, s[36:39], s57 offen offset:384 lds\n"
    "s_mov_b64 exec, 0\n"
    "s_cmp_eq_u32 s84, 0\n"
    "s_cbranch_scc1 L_pad0_%=\n"
    "s_cmp_eq_u32 s84, 1\n"
    "s_cbranch_scc1 L_pad1_%=\n"
    "s_cmp_eq_u32 s84, 2\n"
    "s_cbranch_scc1 L_pad2_%=\n"
    "s_branch L_pad3_%=\n"
    "L_ret0_%=:\n"
    "s_mov_b64 exec, -1\n"
    "s_waitcnt vmcnt(9)\n"
    "buffer_load_dwordx4 a[16:19], v5, s[44:47], s60 offen offset:256\n"
    "s_waitcnt vmcnt(9)\n"
    "buffer_load_dwordx4 a[20:23], v5, s[44:47], s60 offen offset:320\n"
    "s_waitcnt vmcnt(9)\n"
    "s_add_u32 m0, s56, 0x18000\n"
    "s_add_u32 s57, s53, 0x8000\n"
    "buffer_load_dwordx4 v3, s[36:39], s57 offen offset:0 lds\n"
    "s_mov_b64 exec, 0\n"
    "s_cmp_eq_u32 s84, 0\n"
    "s_cbranch_scc1 L_pad4_%=\n"
    "s_cmp_eq_u32 s84, 1\n"
    "s_cbranch_scc1 L_pad5_%=\n"
    "s_cmp_eq_u32 s84, 2\n"
    "s_cbranch_scc1 L_pad6_%=\n"
    "s_branch L_pad7_%=\n"
    "L_ret1_%=:\n"
    "s_mov_b64 exec, -1\n"
    "s_waitcnt vmcnt(9)\n"
    "s_add_u32 m0, s56, 0x18380\n"
    "s_add_u32 s57, s53, 0x8000\n"
    "buffer_load_dwordx4 v3, s[36:39], s57 offen offset:128 lds\n"
    "s_waitcnt vmcnt(9)\n"
    "buffer_load_dwordx4 a[24:27], v5, s[44:47], s60 offen offset:384\n"
    "s_waitcnt vmcnt(9)\n"
    "buffer_load_dwordx4 a[28:31], v5, s[44:47], s60 offen offset:448\n"
    "s_mov_b64 exec, 0\n"
    "s_cmp_eq_u32 s84, 0\n"
    "s_cbranch_scc1 L_pad8_%=\n"
    "s_cmp_eq_u32 s84, 1\n"
    "s_cbranch_scc1 L_pad9_%=\n"
    "s_cmp_eq_u32 s84, 2\n"
    "s_cbranch_scc1 L_pad10_%=\n"
    "s_branch L_pad11_%=\n"
    "L_ret2_%=:\n"
    "s_mov_b64 exec, -1\n"
    "s_waitcnt vmcnt(9)\n"
    "s_add_u32 m0, s56, 0x18700\n"
    "s_add_u32 s57, s53, 0x8000\n"
    "buffer_load_dwordx4 v3, s[36:39], s57 offen offset:256 lds\n"
    "s_waitcnt vmcnt(9)\n"
    "s_add_u32 m0, s56, 0x18a80\n"
    "s_add_u32 s57, s53, 0x8000\n"
    "buffer_load_dwordx4 v3, s[36:39], s57 offen offset:384 lds\n"
    "s_waitcnt vmcnt(9)\n"
    "buffer_load_dwordx4 a[32:35], v5, s[44:47], s61 offen offset:0\n"
    "s_mov_b64 exec, 0\n"
    "s_cmp_eq_u32 s84, 0\n"
    "s_cbranch_scc1 L_pad12_%=\n"
    "s_cmp_eq_u32 s84, 1\n"
    "s_cbranch_scc1 L_pad13_%=\n"
    "s_cmp_eq_u32 s84, 2\n"
    "s_cbranch_scc1 L_pad14_%=\n"
    "s_branch L_ret3_%=\n"
    "L_ret3_%=:\n"
    "s_mov_b64 exec, -1\n"
    "s_waitcnt vmcnt(9)\n"
    "buffer_load_dwordx4 a[36:39], v5, s[44:47], s61 offen offset:64\n"
    "s_waitcnt vmcnt(9)\n"
    "s_add_u32 m0, s56, 0x1c700\n"
    "s_add_u32 s57, s53, 0xc000\n"
    "buffer_load_dwordx4 v3, s[36:39], s57 offen offset:256 lds\n"
    "s_waitcnt vmcnt(9)\n"
    "s_add_u32 m0, s56, 0x1ca80\n"
    "s_add_u32 s57, s53, 0xc000\n"
    "buffer_load_dwordx4 v3, s[36:39], s57 offen offset:384 lds\n"
    "s_waitcnt vmcnt(9)\n"
    "buffer_load_dwordx4 a[40:43], v5, s[44:47], s61 offen offset:128\n"
    "s_waitcnt vmcnt(9)\n"
    "buffer_load_dwordx4 a[44:47], v5, s[44:47], s61 offen offset:192\n"
    "s_waitcnt vmcnt(9)\n"
    "s_add_u32 m0, s56, 0x1c000\n"
    "s_add_u32 s57, s53, 0xc000\n"
    "buffer_load_dwordx4 v3, s[36:39], s57 offen offset:0 lds\n"
    "s_waitcnt vmcnt(9) lgkmcnt(8)\n"
    "s_barrier\n"
    "v_mfma_f32_16x16x32_bf16 v[208:211], a[208:211], a[0:3], 0\n"
    "ds_read_b128 v[112:115], v232 offset:16384\n"
    "v_mfma_f32_16x16x32_bf16 v[208:211], a[212:215], a[4:7], v[208:211]\n"
    "ds_read_b128 v[116:119], v232 offset:16896\n"
    "s_add_u32 m0, s56, 0x1c380\n"
    "s_add_u32 s57, s53, 0xc000\n"
    "buffer_load_dwordx4 v3, s[36:39], s57 offen offset:128 lds\n"
    "v_mfma_f32_16x16x32_bf16 v[208:211], a[216:219], a[8:11], v[208:211]\n"
    "ds_read_b128 v[120:123], v232 offset:17408\n"
    "v_mfma_f32_16x16x32_bf16 v[208:211], a[220:223], a[12:15], v[208:211]\n"
    "ds_read_b128 v[124:127], v232 offset:17920\n"
    "v_mfma_f32_16x16x32_bf16 v[208:211], a[224:227], a[16:19], v[208:211]\n"
    "ds_read_b128 v[128:131], v232 offset:18432\n"
    "v_mfma_f32_16x16x32_bf16 v[208:211], a[228:231], a[20:23], v[208:211]\n"
    "ds_read_b128 v[132:135], v232 offset:18944\n"
    "v_mfma_f32_16x16x32_bf16 v[208:211], a[232:235], a[24:27], v[208:211]\n"
    "ds_read_b128 v[136:139], v232 offset:19456\n"
    "v_mfma_f32_16x16x32_bf16 v[208:211], a[236:239], a[28:31], v[208:211]\n"
    "ds_read_b128 v[140:143], v232 offset:19968\n"
    "s_waitcnt lgkmcnt(8)\n"
    "v_mfma_f32_16x16x32_bf16 v[212:215], a[176:179], a[0:3], 0\n"
    "ds_read_b128 v[144:147], v232 offset:24576\n"
    "v_mfma_f32_16x16x32_bf16 v[212:215], a[180:183], a[4:7], v[212:215]\n"
    "ds_read_b128 v[148:151], v232 offset:25088\n"
    "v_mfma_f32_16x16x32_bf16 v[212:215], a[184:187], a[8:11], v[212:215]\n"
    "ds_read_b128 v[152:155], v232 offset:25600\n"
    "v_mul_f32 v208, s52, v208\n"
    "v_mfma_f32_16x16x32_bf16 v[212:215], a[188:191], a[12:15], v[212:215]\n"
    "ds_read_b128 v[156:159], v232 offset:26112\n"
    "buffer_load_dwordx4 a[48:51], v5, s[44:47], s61 offen offset:256\n"
    "v_mfma_f32_16x16x32_bf16 v[212:215], v[96:99], a[16:19], v[212:215]\n"
    "ds_read_b128 a[208:211], v232 offset:26624\n"
    "v_mul_f32 v209, s52, v209\n"
    "v_mfma_f32_16x16x32_bf16 v[212:215], v[100:103], a[20:23], v[212:215]\n"
    "ds_read_b128 a[212:215], v232 offset:27136\n"
    "v_mul_f32 v210, s52, v210\n"
    "v_mfma_f32_16x16x32_bf16 v[212:215], v[104:107], a[24:27], v[212:215]\n"
    "ds_read_b128 a[216:219], v232 offset:27648\n"
    "v_exp_f32 v208, v208\n"
    "v_mfma_f32_16x16x32_bf16 v[212:215], v[108:111], a[28:31], v[212:215]\n"
    "ds_read_b128 a[220:223], v232 offset:28160\n"
    "v_mul_f32 v211, s52, v211\n"
    "s_waitcnt vmcnt(9) lgkmcnt(8)\n"
    "s_barrier\n"
    "v_mfma_f32_16x16x32_bf16 v[216:219], v[112:115], a[0:3], 0\n"
    "ds_read_b128 a[224:227], v232 offset:32768\n"
    "v_exp_f32 v209, v209\n"
    "v_mfma_f32_16x16x32_bf16 v[216:219], v[116:119], a[4:7], v[216:219]\n"
    "ds_read_b128 a[228:231], v232 offset:33280\n"
    "buffer_load_dwordx4 a[52:55], v5, s[44:47], s61 offen offset:320\n"
    "v_cvt_pk_bf16_f32 v208, v208, v209\n"
    "v_mfma_f32_16x16x32_bf16 v[216:219], v[120:123], a[8:11], v[216:219]\n"
    "ds_read_b128 a[232:235], v232 offset:33792\n"
    "v_exp_f32 v210, v210\n"
    "v_accvgpr_write_b32 a128, v208\n"
    "v_mfma_f32_16x16x32_bf16 v[216:219], v[124:127], a[12:15], v[216:219]\n"
    "ds_read_b128 a[236:239], v232 offset:34304\n"
    "v_mul_f32 v212, s52, v212\n"
    "v_mfma_f32_16x16x32_bf16 v[216:219], v[128:131], a[16:19], v[216:219]\n"
    "ds_read_b128 a[176:179], v232 offset:34816\n"
    "v_exp_f32 v211, v211\n"
    "v_mfma_f32_16x16x32_bf16 v[216:219], v[132:135], a[20:23], v[216:219]\n"
    "ds_read_b128 a[180:183], v232 offset:35328\n"
    "v_cvt_pk_bf16_f32 v210, v210, v211\n"
    "v_mfma_f32_16x16x32_bf16 v[216:219], v[136:139], a[24:27], v[216:219]\n"
    "ds_read_b128 a[184:187], v232 offset:35840\n"
    "v_accvgpr_write_b32 a129, v210\n"
    "v_exp_f32 v212, v212\n"
    "v_mfma_f32_16x16x32_bf16 v[216:219], v[140:143], a[28:31], v[216:219]\n"
    "ds_read_b128 a[188:191], v232 offset:36352\n"
    "v_mul_f32 v213, s52, v213\n"
    "s_waitcnt lgkmcnt(8)\n"
    "v_mfma_f32_16x16x32_bf16 v[220:223], v[144:147], a[0:3], 0\n"
    "ds_read_b128 v[96:99], v232 offset:40960\n"
    "v_exp_f32 v213, v213\n"
    "v_mfma_f32_16x16x32_bf16 v[220:223], v[148:151], a[4:7], v[220:223]\n"
    "ds_read_b128 v[100:103], v232 offset:41472\n"
    "v_mul_f32 v214, s52, v214\n"
    "v_cvt_pk_bf16_f32 v212, v212, v213\n"
    "v_mfma_f32_16x16x32_bf16 v[220:223], v[152:155], a[8:11], v[220:223]\n"
    "ds_read_b128 v[104:107], v232 offset:41984\n"
    "v_exp_f32 v214, v214\n"
    "v_accvgpr_write_b32 a130, v212\n"
    "v_mfma_f32_16x16x32_bf16 v[220:223], v[156:159], a[12:15], v[220:223]\n"
    "ds_read_b128 v[108:111], v232 offset:42496\n"
    "s_add_u32 m0, s56, 0x20000\n"
    "s_add_u32 s57, s53, 0x10000\n"
    "buffer_load_dwordx4 v3, s[36:39], s57 offen offset:0 lds\n"
    "v_mul_f32 v215, s52, v215\n"
    "v_mfma_f32_16x16x32_bf16 v[220:223], a[208:211], a[16:19], v[220:223]\n"
    "ds_read_b128 v[112:115], v232 offset:43008\n"
    "v_exp_f32 v215, v215\n"
    "v_mfma_f32_16x16x32_bf16 v[220:223], a[212:215], a[20:23], v[220:223]\n"
    "ds_read_b128 v[116:119], v232 offset:43520\n"
    "v_cvt_pk_bf16_f32 v214, v214, v215\n"
    "v_mul_f32 v216, s52, v216\n"
    "v_mfma_f32_16x16x32_bf16 v[220:223], a[216:219], a[24:27], v[220:223]\n"
    "ds_read_b128 v[120:123], v232 offset:44032\n"
    "v_accvgpr_write_b32 a131, v214\n"
    "v_exp_f32 v216, v216\n"
    "v_mfma_f32_16x16x32_bf16 v[220:223], a[220:223], a[28:31], v[220:223]\n"
    "ds_read_b128 v[124:127], v232 offset:44544\n"
    "v_mul_f32 v217, s52, v217\n"
    "s_waitcnt lgkmcnt(8)\n"
    "v_mfma_f32_16x16x32_bf16 v[224:227], a[224:227], a[0:3], 0\n"
    "ds_read_b128 v[128:131], v232 offset:0\n"
    "v_exp_f32 v217, v217\n"
    "v_mfma_f32_16x16x32_bf16 v[224:227], a[228:231], a[4:7], v[224:227]\n"
    "ds_read_b128 v[132:135], v232 offset:512\n"
    "v_mul_f32 v218, s52, v218\n"
    "v_cvt_pk_bf16_f32 v216, v216, v217\n"
    "v_mfma_f32_16x16x32_bf16 v[224:227], a[232:235], a[8:11], v[224:227]\n"
    "ds_read_b128 v[136:139], v232 offset:1024\n"
    "v_exp_f32 v218, v218\n"
    "v_accvgpr_write_b32 a136, v216\n"
    "v_mfma_f32_16x16x32_bf16 v[224:227], a[236:239], a[12:15], v[224:227]\n"
    "ds_read_b128 v[140:143], v232 offset:1536\n"
    "v_mul_f32 v219, s52, v219\n"
    "v_mfma_f32_16x16x32_bf16 v[224:227], a[176:179], a[16:19], v[224:227]\n"
    "ds_read_b128 v[144:147], v232 offset:2048\n"
    "v_exp_f32 v219, v219\n"
    "v_mfma_f32_16x16x32_bf16 v[224:227], a[180:183], a[20:23], v[224:227]\n"
    "ds_read_b128 v[148:151], v232 offset:2560\n"
    "s_add_u32 m0, s56, 0x20380\n"
    "s_add_u32 s57, s53, 0x10000\n"
    "buffer_load_dwordx4 v3, s[36:39], s57 offen offset:128 lds\n"
    "v_cvt_pk_bf16_f32 v218, v218, v219\n"
    "v_mfma_f32_16x16x32_bf16 v[224:227], a[184:187], a[24:27], v[224:227]\n"
    "ds_read_b128 v[152:155], v232 offset:3072\n"
    "v_mul_f32 v220, s52, v220\n"
    "v_accvgpr_write_b32 a137, v218\n"
    "v_mfma_f32_16x16x32_bf16 v[224:227], a[188:191], a[28:31], v[224:227]\n"
    "ds_read_b128 v[156:159], v232 offset:3584\n"
    "v_exp_f32 v220, v220\n"
    "v_mul_f32 v221, s52, v221\n"
    "v_mfma_f32_16x16x32_bf16 a[240:243], v[12:15], a[128:131], 0\n"
    "v_exp_f32 v221, v221\n"
    "s_waitcnt lgkmcnt(8)\n"
    "v_mfma_f32_16x16x32_bf16 v[228:231], v[96:99], a[0:3], 0\n"
    "ds_read_b128 a[208:211], v232 offset:8192\n"
    "v_mul_f32 v222, s52, v222\n"
    "v_cvt_pk_bf16_f32 v220, v220, v221\n"
    "v_mfma_f32_16x16x32_bf16 v[228:231], v[100:103], a[4:7], v[228:231]\n"
    "ds_read_b128 a[212:215], v232 offset:8704\n"
    "v_exp_f32 v222, v222\n"
    "v_accvgpr_write_b32 a138, v220\n"
    "v_mfma_f32_16x16x32_bf16 v[228:231], v[104:107], a[8:11], v[228:231]\n"
    "ds_read_b128 a[216:219], v232 offset:9216\n"
    "v_mul_f32 v223, s52, v223\n"
    "v_mfma_f32_16x16x32_bf16 v[228:231], v[108:111], a[12:15], v[228:231]\n"
    "ds_read_b128 a[220:223], v232 offset:9728\n"
    "v_exp_f32 v223, v223\n"
    "v_mfma_f32_16x16x32_bf16 v[228:231], v[112:115], a[16:19], v[228:231]\n"
    "ds_read_b128 a[224:227], v232 offset:10240\n"
    "v_cvt_pk_bf16_f32 v222, v222, v223\n"
    "v_mul_f32 v224, s52, v224\n"
    "v_mfma_f32_16x16x32_bf16 v[228:231], v[116:119], a[20:23], v[228:231]\n"
    "ds_read_b128 a[228:231], v232 offset:10752\n"
    "v_accvgpr_write_b32 a139, v222\n"
    "v_exp_f32 v224, v224\n"
    "v_mfma_f32_16x16x32_bf16 v[228:231], v[120:123], a[24:27], v[228:231]\n"
    "ds_read_b128 a[232:235], v232 offset:11264\n"
    "buffer_load_dwordx4 a[56:59], v5, s[44:47], s61 offen offset:384\n"
    "v_mul_f32 v225, s52, v225\n"
    "v_mfma_f32_16x16x32_bf16 v[228:231], v[124:127], a[28:31], v[228:231]\n"
    "ds_read_b128 a[236:239], v232 offset:11776\n"
    "v_exp_f32 v225, v225\n"
    "s_waitcnt vmcnt(9)\n"
    "buffer_load_dwordx4 a[60:63], v5, s[44:47], s61 offen offset:448\n"
    "s_waitcnt vmcnt(9)\n"
    "s_add_u32 m0, s56, 0x20700\n"
    "s_add_u32 s57, s53, 0x10000\n"
    "buffer_load_dwordx4 v3, s[36:39], s57 offen offset:256 lds\n"
    "s_waitcnt vmcnt(9)\n"
    "s_add_u32 m0, s56, 0x20a80\n"
    "s_add_u32 s57, s53, 0x10000\n"
    "buffer_load_dwordx4 v3, s[36:39], s57 offen offset:384 lds\n"
    "s_waitcnt vmcnt(9)\n"
    "buffer_load_dwordx4 a[64:67], v5, s[44:47], s62 offen offset:0\n"
    "s_waitcnt vmcnt(9)\n"
    "buffer_load_dwordx4 a[68:71], v5, s[44:47], s62 offen offset:64\n"
    "s_waitcnt vmcnt(9)\n"
    "s_add_u32 m0, s56, 0x24000\n"
    "s_add_u32 s57, s53, 0x14000\n"
    "buffer_load_dwordx4 v3, s[36:39], s57 offen offset:0 lds\n"
    "s_waitcnt vmcnt(9)\n"
    "buffer_load_dwordx4 a[72:75], v5, s[44:47], s62 offen offset:128\n"
    "s_waitcnt vmcnt(9)\n"
    "buffer_load_dwordx4 a[76:79], v5, s[44:47], s62 offen offset:192\n"
    "s_waitcnt vmcnt(9)\n"
    "s_add_u32 m0, s56, 0x24380\n"
    "s_add_u32 s57, s53, 0x14000\n"
    "buffer_load_dwordx4 v3, s[36:39], s57 offen offset:128 lds\n"
    "s_waitcnt vmcnt(9)\n"
    "buffer_load_dwordx4 a[80:83], v5, s[44:47], s62 offen offset:256\n"
    "s_waitcnt vmcnt(9) lgkmcnt(8)\n"
    "v_mfma_f32_16x16x32_bf16 v[208:211], v[128:131], a[32:35], 0\n"
    "ds_read_b128 a[176:179], v232 offset:16384\n"
    "v_mul_f32 v226, s52, v226\n"
    "v_cvt_pk_bf16_f32 v224, v224, v225\n"
    "v_mfma_f32_16x16x32_bf16 v[208:211], v[132:135], a[36:39], v[208:211]\n"
    "ds_read_b128 a[180:183], v232 offset:16896\n"
    "buffer_load_dwordx4 a[84:87], v5, s[44:47], s62 offen offset:320\n"
    "v_exp_f32 v226, v226\n"
    "v_mfma_f32_16x16x32_bf16 v[208:211], v[136:139], a[40:43], v[208:211]\n"
    "ds_read_b128 a[184:187], v232 offset:17408\n"
    "v_accvgpr_write_b32 a144, v224\n"
    "v_mul_f32 v227, s52, v227\n"
    "v_mfma_f32_16x16x32_bf16 v[208:211], v[140:143], a[44:47], v[208:211]\n"
    "ds_read_b128 a[188:191], v232 offset:17920\n"
    "v_exp_f32 v227, v227\n"
    "v_mfma_f32_16x16x32_bf16 v[208:211], v[144:147], a[48:51], v[208:211]\n"
    "ds_read_b128 v[96:99], v232 offset:18432\n"
    "v_cvt_pk_bf16_f32 v226, v226, v227\n"
    "v_mfma_f32_16x16x32_bf16 v[208:211], v[148:151], a[52:55], v[208:211]\n"
    "ds_read_b128 v[100:103], v232 offset:18944\n"
    "v_accvgpr_write_b32 a145, v226\n"
    "v_mul_f32 v228, s52, v228\n"
    "v_mfma_f32_16x16x32_bf16 v[208:211], v[152:155], a[56:59], v[208:211]\n"
    "ds_read_b128 v[104:107], v232 offset:19456\n"
    "v_exp_f32 v228, v228\n"
    "v_mfma_f32_16x16x32_bf16 v[208:211], v[156:159], a[60:63], v[208:211]\n"
    "ds_read_b128 v[108:111], v232 offset:19968\n"
    "v_mul_f32 v229, s52, v229\n"
    "v_mfma_f32_16x16x32_bf16 a[240:243], v[12:15], a[136:139], a[240:243]\n"
    "v_exp_f32 v229, v229\n"
    "s_waitcnt lgkmcnt(8)\n"
    "v_mfma_f32_16x16x32_bf16 v[212:215], a[208:211], a[32:35], 0\n"
    "ds_read_b128 v[112:115], v232 offset:24576\n"
    "v_mul_f32 v230, s52, v230\n"
    "v_cvt_pk_bf16_f32 v228, v228, v229\n"
    "v_mfma_f32_16x16x32_bf16 v[212:215], a[212:215], a[36:39], v[212:215]\n"
    "ds_read_b128 v[116:119], v232 offset:25088\n"
    "v_exp_f32 v230, v230\n"
    "v_accvgpr_write_b32 a146, v228\n"
    "v_mfma_f32_16x16x32_bf16 v[212:215], a[216:219], a[40:43], v[212:215]\n"
    "ds_read_b128 v[120:123], v232 offset:25600\n"
    "s_add_u32 m0, s56, 0x24700\n"
    "s_add_u32 s57, s53, 0x14000\n"
    "buffer_load_dwordx4 v3, s[36:39], s57 offen offset:256 lds\n"
    "v_mul_f32 v231, s52, v231\n"
    "v_mfma_f32_16x16x32_bf16 v[212:215], a[220:223], a[44:47], v[212:215]\n"
    "ds_read_b128 v[124:127], v232 offset:26112\n"
    "v_exp_f32 v231, v231\n"
    "v_mfma_f32_16x16x32_bf16 v[212:215], a[224:227], a[48:51], v[212:215]\n"
    "ds_read_b128 v[128:131], v232 offset:26624\n"
    "v_cvt_pk_bf16_f32 v230, v230, v231\n"
    "v_mul_f32 v208, s52, v208\n"
    "v_mfma_f32_16x16x32_bf16 v[212:215], a[228:231], a[52:55], v[212:215]\n"
    "ds_read_b128 v[132:135], v232 offset:27136\n"
    "v_accvgpr_write_b32 a147, v230\n"
    "v_exp_f32 v208, v208\n"
    "v_mfma_f32_16x16x32_bf16 v[212:215], a[232:235], a[56:59], v[212:215]\n"
    "ds_read_b128 v[136:139], v232 offset:27648\n"
    "v_mul_f32 v209, s52, v209\n"
    "v_mfma_f32_16x16x32_bf16 v[212:215], a[236:239], a[60:63], v[212:215]\n"
    "ds_read_b128 v[140:143], v232 offset:28160\n"
    "v_exp_f32 v209, v209\n"
    "s_waitcnt lgkmcnt(8)\n"
    "v_mfma_f32_16x16x32_bf16 v[216:219], a[176:179], a[32:35], 0\n"
    "ds_read_b128 v[144:147], v232 offset:32768\n"
    "v_mul_f32 v210, s52, v210\n"
    "v_cvt_pk_bf16_f32 v160, v208, v209\n"
    "v_mfma_f32_16x16x32_bf16 v[216:219], a[180:183], a[36:39], v[216:219]\n"
    "ds_read_b128 v[148:151], v232 offset:33280\n"
    "v_exp_f32 v210, v210\n"
    "v_mfma_f32_16x16x32_bf16 v[216:219], a[184:187], a[40:43], v[216:219]\n"
    "ds_read_b128 v[152:155], v232 offset:33792\n"
    "v_mul_f32 v211, s52, v211\n"
    "v_mfma_f32_16x16x32_bf16 v[216:219], a[188:191], a[44:47], v[216:219]\n"
    "ds_read_b128 v[156:159], v232 offset:34304\n"
    "v_exp_f32 v211, v211\n"
    "v_mfma_f32_16x16x32_bf16 v[216:219], v[96:99], a[48:51], v[216:219]\n"
    "ds_read_b128 a[208:211], v232 offset:34816\n"
    "buffer_load_dwordx4 a[88:91], v5, s[44:47], s62 offen offset:384\n"
    "v_cvt_pk_bf16_f32 v161, v210, v211\n"
    "v_mfma_f32_16x16x32_bf16 v[216:219], v[100:103], a[52:55], v[216:219]\n"
    "ds_read_b128 a[212:215], v232 offset:35328\n"
    "v_mul_f32 v212, s52, v212\n"
    "v_mfma_f32_16x16x32_bf16 v[216:219], v[104:107], a[56:59], v[216:219]\n"
    "ds_read_b128 a[216:219], v232 offset:35840\n"
    "v_exp_f32 v212, v212\n"
    "v_mfma_f32_16x16x32_bf16 v[216:219], v[108:111], a[60:63], v[216:219]\n"
    "ds_read_b128 a[220:223], v232 offset:36352\n"
    "v_mul_f32 v213, s52, v213\n"
    "v_mfma_f32_16x16x32_bf16 a[240:243], v[12:15], a[144:147], a[240:243]\n"
    "v_exp_f32 v213, v213\n"
    "s_waitcnt lgkmcnt(8)\n"
    "v_mfma_f32_16x16x32_bf16 v[220:223], v[112:115], a[32:35], 0\n"
    "ds_read_b128 a[224:227], v232 offset:40960\n"
    "v_mul_f32 v214, s52, v214\n"
    "v_cvt_pk_bf16_f32 v162, v212, v213\n"
    "v_mfma_f32_16x16x32_bf16 v[220:223], v[116:119], a[36:39], v[220:223]\n"
    "ds_read_b128 a[228:231], v232 offset:41472\n"
    "v_exp_f32 v214, v214\n"
    "v_mfma_f32_16x16x32_bf16 v[220:223], v[120:123], a[40:43], v[220:223]\n"
    "ds_read_b128 a[232:235], v232 offset:41984\n"
    "v_mul_f32 v215, s52, v215\n"
    "v_mfma_f32_16x16x32_bf16 v[220:223], v[124:127], a[44:47], v[220:223]\n"
    "ds_read_b128 a[236:239], v232 offset:42496\n"
    "v_exp_f32 v215, v215\n"
    "v_mfma_f32_16x16x32_bf16 v[220:223], v[128:131], a[48:51], v[220:223]\n"
    "ds_read_b128 a[176:179], v232 offset:43008\n"
    "v_cvt_pk_bf16_f32 v163, v214, v215\n"
    "v_mul_f32 v216, s52, v216\n"
    "v_mfma_f32_16x16x32_bf16 v[220:223], v[132:135], a[52:55], v[220:223]\n"
    "ds_read_b128 a[180:183], v232 offset:43520\n"
    "buffer_load_dwordx4 a[92:95], v5, s[44:47], s62 offen offset:448\n"
    "v_exp_f32 v216, v216\n"
    "v_mfma_f32_16x16x32_bf16 v[220:223], v[136:139], a[56:59], v[220:223]\n"
    "ds_read_b128 a[184:187], v232 offset:44032\n"
    "v_mul_f32 v217, s52, v217\n"
    "v_mfma_f32_16x16x32_bf16 v[220:223], v[140:143], a[60:63], v[220:223]\n"
    "ds_read_b128 a[188:191], v232 offset:44544\n"
    "v_exp_f32 v217, v217\n"
    "s_waitcnt vmcnt(19) lgkmcnt(8)\n"
    "s_barrier\n"
    "v_mfma_f32_16x16x32_bf16 v[224:227], v[144:147], a[32:35], 0\n"
    "ds_read_b128 v[96:99], v232 offset:49152\n"
    "v_mul_f32 v218, s52, v218\n"
    "v_cvt_pk_bf16_f32 v168, v216, v217\n"
    "v_mfma_f32_16x16x32_bf16 v[224:227], v[148:151], a[36:39], v[224:227]\n"
    "ds_read_b128 v[100:103], v232 offset:49664\n"
    "s_add_u32 m0, s56, 0x24a80\n"
    "s_add_u32 s57, s53, 0x14000\n"
    "buffer_load_dwordx4 v3, s[36:39], s57 offen offset:384 lds\n"
    "v_exp_f32 v218, v218\n"
    "v_mfma_f32_16x16x32_bf16 v[224:227], v[152:155], a[40:43], v[224:227]\n"
    "ds_read_b128 v[104:107], v232 offset:50176\n"
    "v_mul_f32 v219, s52, v219\n"
    "v_mfma_f32_16x16x32_bf16 v[224:227], v[156:159], a[44:47], v[224:227]\n"
    "ds_read_b128 v[108:111], v232 offset:50688\n"
    "v_exp_f32 v219, v219\n"
    "v_mfma_f32_16x16x32_bf16 v[224:227], a[208:211], a[48:51], v[224:227]\n"
    "ds_read_b128 v[112:115], v232 offset:51200\n"
    "v_cvt_pk_bf16_f32 v169, v218, v219\n"
    "v_mfma_f32_16x16x32_bf16 v[224:227], a[212:215], a[52:55], v[224:227]\n"
    "ds_read_b128 v[116:119], v232 offset:51712\n"
    "v_mul_f32 v220, s52, v220\n"
    "v_mfma_f32_16x16x32_bf16 v[224:227], a[216:219], a[56:59], v[224:227]\n"
    "ds_read_b128 v[120:123], v232 offset:52224\n"
    "v_exp_f32 v220, v220\n"
    "v_mfma_f32_16x16x32_bf16 v[224:227], a[220:223], a[60:63], v[224:227]\n"
    "ds_read_b128 v[124:127], v232 offset:52736\n"
    "v_mul_f32 v221, s52, v221\n"
    "v_mfma_f32_16x16x32_bf16 a[244:247], v[12:15], v[160:163], 0\n"
    "v_exp_f32 v221, v221\n"
    "s_waitcnt lgkmcnt(8)\n"
    "v_mfma_f32_16x16x32_bf16 v[228:231], a[224:227], a[32:35], 0\n"
    "ds_read_b128 v[128:131], v232 offset:57344\n"
    "v_mul_f32 v222, s52, v222\n"
    "v_cvt_pk_bf16_f32 v170, v220, v221\n"
    "v_mfma_f32_16x16x32_bf16 v[228:231], a[228:231], a[36:39], v[228:231]\n"
    "ds_read_b128 v[132:135], v232 offset:57856\n"
    "v_exp_f32 v222, v222\n"
    "v_mfma_f32_16x16x32_bf16 v[228:231], a[232:235], a[40:43], v[228:231]\n"
    "ds_read_b128 v[136:139], v232 offset:58368\n"
    "buffer_load_dwordx4 a[96:99], v5, s[44:47], s63 offen offset:0\n"
    "v_mul_f32 v223, s52, v223\n"
    "v_mfma_f32_16x16x32_bf16 v[228:231], a[236:239], a[44:47], v[228:231]\n"
    "ds_read_b128 v[140:143], v232 offset:58880\n"
    "v_exp_f32 v223, v223\n"
    "v_mfma_f32_16x16x32_bf16 v[228:231], a[176:179], a[48:51], v[228:231]\n"
    "ds_read_b128 v[144:147], v232 offset:59392\n"
    "v_cvt_pk_bf16_f32 v171, v222, v223\n"
    "v_mul_f32 v224, s52, v224\n"
    "v_mfma_f32_16x16x32_bf16 v[228:231], a[180:183], a[52:55], v[228:231]\n"
    "ds_read_b128 v[148:151], v232 offset:59904\n"
    "v_exp_f32 v224, v224\n"
    "v_mfma_f32_16x16x32_bf16 v[228:231], a[184:187], a[56:59], v[228:231]\n"
    "ds_read_b128 v[152:155], v232 offset:60416\n"
    "v_mul_f32 v225, s52, v225\n"
    "v_mfma_f32_16x16x32_bf16 v[228:231], a[188:191], a[60:63], v[228:231]\n"
    "ds_read_b128 v[156:159], v232 offset:60928\n"
    "v_exp_f32 v225, v225\n"
    "s_waitcnt vmcnt(13) lgkmcnt(8)\n"
    "s_barrier\n"
    "v_mfma_f32_16x16x32_bf16 v[208:211], v[96:99], a[0:3], 0\n"
    "ds_read_b128 a[208:211], v233 offset:0\n"
    "v_mul_f32 v226, s52, v226\n"
    "v_cvt_pk_bf16_f32 v176, v224, v225\n"
    "v_mfma_f32_16x16x32_bf16 v[208:211], v[100:103], a[4:7], v[208:211]\n"
    "buffer_load_dwordx4 a[100:103], v5, s[44:47], s63 offen offset:64\n"
    "v_exp_f32 v226, v226\n"
    "v_mfma_f32_16x16x32_bf16 v[208:211], v[104:107], a[8:11], v[208:211]\n"
    "ds_read_b128 a[212:215], v233 offset:512\n"
    "v_mul_f32 v227, s52, v227\n"
    "v_mfma_f32_16x16x32_bf16 v[208:211], v[108:111], a[12:15], v[208:211]\n"
    "v_exp_f32 v227, v227\n"
    "v_mfma_f32_16x16x32_bf16 v[208:211], v[112:115], a[16:19], v[208:211]\n"
    "ds_read_b128 a[216:219], v233 offset:1024\n"
    "v_cvt_pk_bf16_f32 v177, v226, v227\n"
    "v_mfma_f32_16x16x32_bf16 v[208:211], v[116:119], a[20:23], v[208:211]\n"
    "v_mul_f32 v228, s52, v228\n"
    "v_mul_f32 v229, s52, v229\n"
    "v_mfma_f32_16x16x32_bf16 v[208:211], v[120:123], a[24:27], v[208:211]\n"
    "ds_read_b128 a[220:223], v233 offset:1536\n"
    "v_exp_f32 v228, v228\n"
    "v_mfma_f32_16x16x32_bf16 v[208:211], v[124:127], a[28:31], v[208:211]\n"
    "v_exp_f32 v229, v229\n"
    "v_mfma_f32_16x16x32_bf16 a[244:247], v[12:15], v[168:171], a[244:247]\n"
    "ds_read_b128 a[224:227], v233 offset:2048\n"
    "v_mul_f32 v230, s52, v230\n"
    "v_mfma_f32_16x16x32_bf16 v[212:215], v[96:99], a[32:35], 0\n"
    "v_cvt_pk_bf16_f32 v178, v228, v229\n"
    "v_mul_f32 v231, s52, v231\n"
    "v_mfma_f32_16x16x32_bf16 v[212:215], v[100:103], a[36:39], v[212:215]\n"
    "ds_read_b128 a[228:231], v233 offset:2560\n"
    "v_exp_f32 v230, v230\n"
    "v_mfma_f32_16x16x32_bf16 v[212:215], v[104:107], a[40:43], v[212:215]\n"
    "buffer_load_dwordx4 a[104:107], v5, s[44:47], s63 offen offset:128\n"
    "v_mfma_f32_16x16x32_bf16 v[212:215], v[108:111], a[44:47], v[212:215]\n"
    "ds_read_b128 a[232:235], v233 offset:3072\n"
    "v_exp_f32 v231, v231\n"
    "v_mfma_f32_16x16x32_bf16 v[212:215], v[112:115], a[48:51], v[212:215]\n"
    "v_cvt_pk_bf16_f32 v179, v230, v231\n"
    "v_mul_f32 v208, s52, v208\n"
    "v_mfma_f32_16x16x32_bf16 v[212:215], v[116:119], a[52:55], v[212:215]\n"
    "ds_read_b128 a[236:239], v233 offset:3584\n"
    "v_exp_f32 v208, v208\n"
    "v_mfma_f32_16x16x32_bf16 v[212:215], v[120:123], a[56:59], v[212:215]\n"
    "v_mul_f32 v209, s52, v209\n"
    "v_mul_f32 v210, s52, v210\n"
    "v_mfma_f32_16x16x32_bf16 v[212:215], v[124:127], a[60:63], v[212:215]\n"
    "v_exp_f32 v209, v209\n"
    "s_waitcnt lgkmcnt(8)\n"
    "v_mfma_f32_16x16x32_bf16 v[216:219], v[128:131], a[0:3], 0\n"
    "ds_read_b128 a[176:179], v233 offset:8192\n"
    "v_cvt_pk_bf16_f32 v208, v208, v209\n"
    "v_mfma_f32_16x16x32_bf16 v[216:219], v[132:135], a[4:7], v[216:219]\n"
    "v_exp_f32 v210, v210\n"
    "v_accvgpr_write_b32 a152, v208\n"
    "v_mfma_f32_16x16x32_bf16 v[216:219], v[136:139], a[8:11], v[216:219]\n"
    "ds_read_b128 a[180:183], v233 offset:8704\n"
    "v_mul_f32 v211, s52, v211\n"
    "v_mfma_f32_16x16x32_bf16 v[216:219], v[140:143], a[12:15], v[216:219]\n"
    "v_exp_f32 v211, v211\n"
    "v_mfma_f32_16x16x32_bf16 v[216:219], v[144:147], a[16:19], v[216:219]\n"
    "ds_read_b128 a[184:187], v233 offset:9216\n"
    "buffer_load_dwordx4 a[108:111], v5, s[44:47], s63 offen offset:192\n"
    "v_cvt_pk_bf16_f32 v210, v210, v211\n"
    "v_mfma_f32_16x16x32_bf16 v[216:219], v[148:151], a[20:23], v[216:219]\n"
    "v_accvgpr_write_b32 a153, v210\n"
    "v_mul_f32 v212, s52, v212\n"
    "v_mfma_f32_16x16x32_bf16 v[216:219], v[152:155], a[24:27], v[216:219]\n"
    "ds_read_b128 a[188:191], v233 offset:9728\n"
    "v_exp_f32 v212, v212\n"
    "v_mfma_f32_16x16x32_bf16 v[216:219], v[156:159], a[28:31], v[216:219]\n"
    "v_mul_f32 v213, s52, v213\n"
    "v_mul_f32 v214, s52, v214\n"
    "v_mfma_f32_16x16x32_bf16 a[244:247], v[12:15], v[176:179], a[244:247]\n"
    "ds_read_b128 v[96:99], v233 offset:10240\n"
    "v_exp_f32 v213, v213\n"
    "v_mfma_f32_16x16x32_bf16 v[220:223], v[128:131], a[32:35], 0\n"
    "v_cvt_pk_bf16_f32 v184, v212, v213\n"
    "v_mul_f32 v215, s52, v215\n"
    "v_mfma_f32_16x16x32_bf16 v[220:223], v[132:135], a[36:39], v[220:223]\n"
    "ds_read_b128 v[100:103], v233 offset:10752\n"
    "v_exp_f32 v214, v214\n"
    "v_mfma_f32_16x16x32_bf16 v[220:223], v[136:139], a[40:43], v[220:223]\n"
    "v_exp_f32 v215, v215\n"
    "v_mfma_f32_16x16x32_bf16 v[220:223], v[140:143], a[44:47], v[220:223]\n"
    "ds_read_b128 v[104:107], v233 offset:11264\n"
    "v_cvt_pk_bf16_f32 v185, v214, v215\n"
    "v_mfma_f32_16x16x32_bf16 v[220:223], v[144:147], a[48:51], v[220:223]\n"
    "v_mul_f32 v216, s52, v216\n"
    "v_mul_f32 v217, s52, v217\n"
    "v_mfma_f32_16x16x32_bf16 v[220:223], v[148:151], a[52:55], v[220:223]\n"
    "ds_read_b128 v[108:111], v233 offset:11776\n"
    "buffer_load_dwordx4 a[112:115], v5, s[44:47], s63 offen offset:256\n"
    "v_exp_f32 v216, v216\n"
    "v_mfma_f32_16x16x32_bf16 v[220:223], v[152:155], a[56:59], v[220:223]\n"
    "v_exp_f32 v217, v217\n"
    "v_mfma_f32_16x16x32_bf16 v[220:223], v[156:159], a[60:63], v[220:223]\n"
    "v_mul_f32 v218, s52, v218\n"
    "v_cvt_pk_bf16_f32 v216, v216, v217\n"
    "s_waitcnt lgkmcnt(8)\n"
    "v_mfma_f32_16x16x32_bf16 v[224:227], a[208:211], a[0:3], 0\n"
    "ds_read_b128 v[112:115], v232 offset:0\n"
    "v_accvgpr_write_b32 a154, v216\n"
    "v_mfma_f32_16x16x32_bf16 v[224:227], a[212:215], a[4:7], v[224:227]\n"
    "v_exp_f32 v218, v218\n"
    "v_mfma_f32_16x16x32_bf16 v[224:227], a[216:219], a[8:11], v[224:227]\n"
    "ds_read_b128 v[116:119], v232 offset:512\n"
    "v_mul_f32 v219, s52, v219\n"
    "v_mfma_f32_16x16x32_bf16 v[224:227], a[220:223], a[12:15], v[224:227]\n"
    "v_exp_f32 v219, v219\n"
    "v_mfma_f32_16x16x32_bf16 v[224:227], a[224:227], a[16:19], v[224:227]\n"
    "ds_read_b128 v[120:123], v232 offset:1024\n"
    "v_cvt_pk_bf16_f32 v218, v218, v219\n"
    "v_mfma_f32_16x16x32_bf16 v[224:227], a[228:231], a[20:23], v[224:227]\n"
    "v_accvgpr_write_b32 a155, v218\n"
    "v_mul_f32 v220, s52, v220\n"
    "v_mfma_f32_16x16x32_bf16 v[224:227], a[232:235], a[24:27], v[224:227]\n"
    "ds_read_b128 v[124:127], v232 offset:1536\n"
    "v_exp_f32 v220, v220\n"
    "v_mfma_f32_16x16x32_bf16 v[224:227], a[236:239], a[28:31], v[224:227]\n"
    "buffer_load_dwordx4 a[116:119], v5, s[44:47], s63 offen offset:320\n"
    "v_mul_f32 v221, s52, v221\n"
    "v_mfma_f32_16x16x32_bf16 v[228:231], a[208:211], a[32:35], 0\n"
    "ds_read_b128 v[128:131], v232 offset:2048\n"
    "v_exp_f32 v221, v221\n"
    "v_mfma_f32_16x16x32_bf16 v[228:231], a[212:215], a[36:39], v[228:231]\n"
    "v_mul_f32 v222, s52, v222\n"
    "v_cvt_pk_bf16_f32 v186, v220, v221\n"
    "v_mfma_f32_16x16x32_bf16 v[228:231], a[216:219], a[40:43], v[228:231]\n"
    "ds_read_b128 v[132:135], v232 offset:2560\n"
    "v_exp_f32 v222, v222\n"
    "v_mfma_f32_16x16x32_bf16 v[228:231], a[220:223], a[44:47], v[228:231]\n"
    "L_pad0_%=:\n"
    "s_cbranch_execz L_ret0_%=\n"
    "v_mul_f32 v223, s52, v223\n"
    "v_mul_f32 v224, s52, v224\n"
    "v_mfma_f32_16x16x32_bf16 v[228:231], a[224:227], a[48:51], v[228:231]\n"
    "ds_read_b128 v[136:139], v232 offset:3072\n"
    "v_exp_f32 v223, v223\n"
    "v_mfma_f32_16x16x32_bf16 v[228:231], a[228:231], a[52:55], v[228:231]\n"
    "v_cvt_pk_bf16_f32 v187, v222, v223\n"
    "v_mul_f32 v225, s52, v225\n"
    "v_mfma_f32_16x16x32_bf16 v[228:231], a[232:235], a[56:59], v[228:231]\n"
    "ds_read_b128 v[140:143], v232 offset:3584\n"
    "v_exp_f32 v224, v224\n"
    "v_mfma_f32_16x16x32_bf16 v[228:231], a[236:239], a[60:63], v[228:231]\n"
    "v_exp_f32 v225, v225\n"
    "v_mfma_f32_16x16x32_bf16 a[240:243], v[12:15], a[152:155], a[240:243]\n"
    "v_mul_f32 v226, s52, v226\n"
    "v_cvt_pk_bf16_f32 v32, v224, v225\n"
    "s_waitcnt lgkmcnt(8)\n"
    "v_mfma_f32_16x16x32_bf16 v[208:211], a[176:179], a[0:3], 0\n"
    "ds_read_b128 v[144:147], v232 offset:8192\n"
    "buffer_load_dwordx4 a[120:123], v5, s[44:47], s63 offen offset:384\n"
    "v_mfma_f32_16x16x32_bf16 v[208:211], a[180:183], a[4:7], v[208:211]\n"
    "v_exp_f32 v226, v226\n"
    "v_mfma_f32_16x16x32_bf16 v[208:211], a[184:187], a[8:11], v[208:211]\n"
    "ds_read_b128 v[148:151], v232 offset:8704\n"
    "v_mul_f32 v227, s52, v227\n"
    "v_mfma_f32_16x16x32_bf16 v[208:211], a[188:191], a[12:15], v[208:211]\n"
    "v_exp_f32 v227, v227\n"
    "v_mfma_f32_16x16x32_bf16 v[208:211], v[96:99], a[16:19], v[208:211]\n"
    "ds_read_b128 v[152:155], v232 offset:9216\n"
    "v_cvt_pk_bf16_f32 v33, v226, v227\n"
    "v_mul_f32 v228, s52, v228\n"
    "v_mfma_f32_16x16x32_bf16 v[208:211], v[100:103], a[20:23], v[208:211]\n"
    "v_exp_f32 v228, v228\n"
    "v_mfma_f32_16x16x32_bf16 v[208:211], v[104:107], a[24:27], v[208:211]\n"
    "ds_read_b128 v[156:159], v232 offset:9728\n"
    "v_mul_f32 v229, s52, v229\n"
    "v_mfma_f32_16x16x32_bf16 v[208:211], v[108:111], a[28:31], v[208:211]\n"
    "v_exp_f32 v229, v229\n"
    "v_mfma_f32_16x16x32_bf16 a[244:247], v[12:15], v[184:187], a[244:247]\n"
    "ds_read_b128 a[208:211], v232 offset:10240\n"
    "v_mul_f32 v230, s52, v230\n"
    "v_cvt_pk_bf16_f32 v36, v228, v229\n"
    "v_mfma_f32_16x16x32_bf16 v[212:215], a[176:179], a[32:35], 0\n"
    "v_exp_f32 v230, v230\n"
    "v_mfma_f32_16x16x32_bf16 v[212:215], a[180:183], a[36:39], v[212:215]\n"
    "ds_read_b128 a[212:215], v232 offset:10752\n"
    "buffer_load_dwordx4 a[124:127], v5, s[44:47], s63 offen offset:448\n"
    "v_mul_f32 v231, s52, v231\n"
    "v_mfma_f32_16x16x32_bf16 v[212:215], a[184:187], a[40:43], v[212:215]\n"
    "v_exp_f32 v231, v231\n"
    "v_mfma_f32_16x16x32_bf16 v[212:215], a[188:191], a[44:47], v[212:215]\n"
    "ds_read_b128 a[216:219], v232 offset:11264\n"
    "v_cvt_pk_bf16_f32 v37, v230, v231\n"
    "v_mfma_f32_16x16x32_bf16 v[212:215], v[96:99], a[48:51], v[212:215]\n"
    "v_mul_f32 v208, s52, v208\n"
    "v_mul_f32 v209, s52, v209\n"
    "v_mfma_f32_16x16x32_bf16 v[212:215], v[100:103], a[52:55], v[212:215]\n"
    "ds_read_b128 a[220:223], v232 offset:11776\n"
    "v_exp_f32 v208, v208\n"
    "v_mfma_f32_16x16x32_bf16 v[212:215], v[104:107], a[56:59], v[212:215]\n"
    "v_exp_f32 v209, v209\n"
    "v_mfma_f32_16x16x32_bf16 v[212:215], v[108:111], a[60:63], v[212:215]\n"
    "v_mul_f32 v210, s52, v210\n"
    "v_cvt_pk_bf16_f32 v34, v208, v209\n"
    "s_waitcnt vmcnt(9) lgkmcnt(8)\n"
    "v_mfma_f32_16x16x32_bf16 v[216:219], v[112:115], a[64:67], 0\n"
    "ds_read_b128 a[224:227], v232 offset:16384\n"
    "v_mul_f32 v211, s52, v211\n"
    "v_mfma_f32_16x16x32_bf16 v[216:219], v[116:119], a[68:71], v[216:219]\n"
    "ds_read_b128 a[228:231], v232 offset:16896\n"
    "s_add_u32 m0, s56, 0x8000\n"
    "s_add_u32 s57, s53, 0x18000\n"
    "buffer_load_dwordx4 v3, s[36:39], s57 offen offset:0 lds\n"
    "v_exp_f32 v210, v210\n"
    "v_mfma_f32_16x16x32_bf16 v[216:219], v[120:123], a[72:75], v[216:219]\n"
    "ds_read_b128 a[232:235], v232 offset:17408\n"
    "v_mul_f32 v212, s52, v212\n"
    "v_mfma_f32_16x16x32_bf16 v[216:219], v[124:127], a[76:79], v[216:219]\n"
    "ds_read_b128 a[236:239], v232 offset:17920\n"
    "v_exp_f32 v211, v211\n"
    "v_mfma_f32_16x16x32_bf16 v[216:219], v[128:131], a[80:83], v[216:219]\n"
    "ds_read_b128 a[176:179], v232 offset:18432\n"
    "v_cvt_pk_bf16_f32 v35, v210, v211\n"
    "v_mfma_f32_16x16x32_bf16 v[216:219], v[132:135], a[84:87], v[216:219]\n"
    "ds_read_b128 a[180:183], v232 offset:18944\n"
    "v_mul_f32 v213, s52, v213\n"
    "v_mfma_f32_16x16x32_bf16 v[216:219], v[136:139], a[88:91], v[216:219]\n"
    "ds_read_b128 a[184:187], v232 offset:19456\n"
    "v_exp_f32 v212, v212\n"
    "v_mfma_f32_16x16x32_bf16 v[216:219], v[140:143], a[92:95], v[216:219]\n"
    "ds_read_b128 a[188:191], v232 offset:19968\n"
    "v_mul_f32 v214, s52, v214\n"
    "s_waitcnt lgkmcnt(8)\n"
    "v_mfma_f32_16x16x32_bf16 v[220:223], v[144:147], a[64:67], 0\n"
    "ds_read_b128 v[96:99], v232 offset:24576\n"
    "v_exp_f32 v213, v213\n"
    "v_mfma_f32_16x16x32_bf16 v[220:223], v[148:151], a[68:71], v[220:223]\n"
    "ds_read_b128 v[100:103], v232 offset:25088\n"
    "v_cvt_pk_bf16_f32 v38, v212, v213\n"
    "v_mfma_f32_16x16x32_bf16 v[220:223], v[152:155], a[72:75], v[220:223]\n"
    "ds_read_b128 v[104:107], v232 offset:25600\n"
    "v_exp_f32 v214, v214\n"
    "v_mfma_f32_16x16x32_bf16 v[220:223], v[156:159], a[76:79], v[220:223]\n"
    "ds_read_b128 v[108:111], v232 offset:26112\n"
    "s_add_u32 m0, s56, 0x8380\n"
    "s_add_u32 s57, s53, 0x18000\n"
    "buffer_load_dwordx4 v3, s[36:39], s57 offen offset:128 lds\n"
    "v_mul_f32 v215, s52, v215\n"
    "v_mfma_f32_16x16x32_bf16 v[220:223], a[208:211], a[80:83], v[220:223]\n"
    "ds_read_b128 v[112:115], v232 offset:26624\n"
    "v_exp_f32 v215, v215\n"
    "v_mfma_f32_16x16x32_bf16 v[220:223], a[212:215], a[84:87], v[220:223]\n"
    "ds_read_b128 v[116:119], v232 offset:27136\n"
    "v_cvt_pk_bf16_f32 v39, v214, v215\n"
    "v_mul_f32 v216, s52, v216\n"
    "v_mfma_f32_16x16x32_bf16 v[220:223], a[216:219], a[88:91], v[220:223]\n"
    "ds_read_b128 v[120:123], v232 offset:27648\n"
    "v_exp_f32 v216, v216\n"
    "v_mfma_f32_16x16x32_bf16 v[220:223], a[220:223], a[92:95], v[220:223]\n"
    "ds_read_b128 v[124:127], v232 offset:28160\n"
    "v_mul_f32 v217, s52, v217\n"
    "s_waitcnt lgkmcnt(8)\n"
    "v_mfma_f32_16x16x32_bf16 v[224:227], a[224:227], a[64:67], 0\n"
    "ds_read_b128 v[128:131], v232 offset:32768\n"
    "v_exp_f32 v217, v217\n"
    "v_mfma_f32_16x16x32_bf16 v[224:227], a[228:231], a[68:71], v[224:227]\n"
    "ds_read_b128 v[132:135], v232 offset:33280\n"
    "v_mul_f32 v218, s52, v218\n"
    "v_cvt_pk_bf16_f32 v216, v216, v217\n"
    "v_mfma_f32_16x16x32_bf16 v[224:227], a[232:235], a[72:75], v[224:227]\n"
    "ds_read_b128 v[136:139], v232 offset:33792\n"
    "v_exp_f32 v218, v218\n"
    "v_accvgpr_write_b32 a132, v216\n"
    "v_mfma_f32_16x16x32_bf16 v[224:227], a[236:239], a[76:79], v[224:227]\n"
    "ds_read_b128 v[140:143], v232 offset:34304\n"
    "v_mul_f32 v219, s52, v219\n"
    "v_mfma_f32_16x16x32_bf16 v[224:227], a[176:179], a[80:83], v[224:227]\n"
    "ds_read_b128 v[144:147], v232 offset:34816\n"
    "v_exp_f32 v219, v219\n"
    "v_mfma_f32_16x16x32_bf16 v[224:227], a[180:183], a[84:87], v[224:227]\n"
    "ds_read_b128 v[148:151], v232 offset:35328\n"
    "s_add_u32 m0, s56, 0x8700\n"
    "s_add_u32 s57, s53, 0x18000\n"
    "buffer_load_dwordx4 v3, s[36:39], s57 offen offset:256 lds\n"
    "v_cvt_pk_bf16_f32 v218, v218, v219\n"
    "v_mfma_f32_16x16x32_bf16 v[224:227], a[184:187], a[88:91], v[224:227]\n"
    "ds_read_b128 v[152:155], v232 offset:35840\n"
    "v_mul_f32 v220, s52, v220\n"
    "v_accvgpr_write_b32 a133, v218\n"
    "v_mfma_f32_16x16x32_bf16 v[224:227], a[188:191], a[92:95], v[224:227]\n"
    "ds_read_b128 v[156:159], v232 offset:36352\n"
    "v_exp_f32 v220, v220\n"
    "v_mul_f32 v221, s52, v221\n"
    "s_waitcnt lgkmcnt(8)\n"
    "v_mfma_f32_16x16x32_bf16 v[228:231], v[96:99], a[64:67], 0\n"
    "ds_read_b128 a[208:211], v232 offset:40960\n"
    "v_exp_f32 v221, v221\n"
    "v_mfma_f32_16x16x32_bf16 v[228:231], v[100:103], a[68:71], v[228:231]\n"
    "ds_read_b128 a[212:215], v232 offset:41472\n"
    "v_mul_f32 v222, s52, v222\n"
    "v_cvt_pk_bf16_f32 v220, v220, v221\n"
    "v_mfma_f32_16x16x32_bf16 v[228:231], v[104:107], a[72:75], v[228:231]\n"
    "ds_read_b128 a[216:219], v232 offset:41984\n"
    "v_exp_f32 v222, v222\n"
    "v_accvgpr_write_b32 a134, v220\n"
    "v_mfma_f32_16x16x32_bf16 v[228:231], v[108:111], a[76:79], v[228:231]\n"
    "ds_read_b128 a[220:223], v232 offset:42496\n"
    "v_mul_f32 v223, s52, v223\n"
    "v_mfma_f32_16x16x32_bf16 v[228:231], v[112:115], a[80:83], v[228:231]\n"
    "ds_read_b128 a[224:227], v232 offset:43008\n"
    "v_exp_f32 v223, v223\n"
    "v_mfma_f32_16x16x32_bf16 v[228:231], v[116:119], a[84:87], v[228:231]\n"
    "ds_read_b128 a[228:231], v232 offset:43520\n"
    "v_cvt_pk_bf16_f32 v222, v222, v223\n"
    "v_mul_f32 v224, s52, v224\n"
    "v_mfma_f32_16x16x32_bf16 v[228:231], v[120:123], a[88:91], v[228:231]\n"
    "ds_read_b128 a[232:235], v232 offset:44032\n"
    "v_accvgpr_write_b32 a135, v222\n"
    "v_exp_f32 v224, v224\n"
    "v_mfma_f32_16x16x32_bf16 v[228:231], v[124:127], a[92:95], v[228:231]\n"
    "ds_read_b128 a[236:239], v232 offset:44544\n"
    "s_add_u32 m0, s56, 0x8a80\n"
    "s_add_u32 s57, s53, 0x18000\n"
    "buffer_load_dwordx4 v3, s[36:39], s57 offen offset:384 lds\n"
    "v_mul_f32 v225, s52, v225\n"
    "s_waitcnt lgkmcnt(8)\n"
    "v_mfma_f32_16x16x32_bf16 v[208:211], v[128:131], a[64:67], 0\n"
    "ds_read_b128 a[176:179], v232 offset:49152\n"
    "v_exp_f32 v225, v225\n"
    "v_mfma_f32_16x16x32_bf16 v[208:211], v[132:135], a[68:71], v[208:211]\n"
    "ds_read_b128 a[180:183], v232 offset:49664\n"
    "v_mul_f32 v226, s52, v226\n"
    "v_cvt_pk_bf16_f32 v224, v224, v225\n"
    "v_mfma_f32_16x16x32_bf16 v[208:211], v[136:139], a[72:75], v[208:211]\n"
    "ds_read_b128 a[184:187], v232 offset:50176\n"
    "v_exp_f32 v226, v226\n"
    "v_accvgpr_write_b32 a140, v224\n"
    "v_mfma_f32_16x16x32_bf16 v[208:211], v[140:143], a[76:79], v[208:211]\n"
    "ds_read_b128 a[188:191], v232 offset:50688\n"
    "v_mul_f32 v227, s52, v227\n"
    "v_mfma_f32_16x16x32_bf16 v[208:211], v[144:147], a[80:83], v[208:211]\n"
    "ds_read_b128 v[96:99], v232 offset:51200\n"
    "v_exp_f32 v227, v227\n"
    "v_mfma_f32_16x16x32_bf16 v[208:211], v[148:151], a[84:87], v[208:211]\n"
    "ds_read_b128 v[100:103], v232 offset:51712\n"
    "v_cvt_pk_bf16_f32 v226, v226, v227\n"
    "v_mul_f32 v228, s52, v228\n"
    "v_mfma_f32_16x16x32_bf16 v[208:211], v[152:155], a[88:91], v[208:211]\n"
    "ds_read_b128 v[104:107], v232 offset:52224\n"
    "v_accvgpr_write_b32 a141, v226\n"
    "v_exp_f32 v228, v228\n"
    "v_mfma_f32_16x16x32_bf16 v[208:211], v[156:159], a[92:95], v[208:211]\n"
    "ds_read_b128 v[108:111], v232 offset:52736\n"
    "v_mul_f32 v229, s52, v229\n"
    "v_mfma_f32_16x16x32_bf16 a[248:251], v[12:15], a[132:135], 0\n"
    "v_exp_f32 v229, v229\n"
    "s_waitcnt lgkmcnt(8)\n"
    "v_mfma_f32_16x16x32_bf16 v[212:215], a[208:211], a[64:67], 0\n"
    "ds_read_b128 v[112:115], v232 offset:57344\n"
    "s_add_u32 m0, s56, 0xc000\n"
    "s_add_u32 s57, s53, 0x1c000\n"
    "buffer_load_dwordx4 v3, s[36:39], s57 offen offset:0 lds\n"
    "v_mul_f32 v230, s52, v230\n"
    "v_mfma_f32_16x16x32_bf16 v[212:215], a[212:215], a[68:71], v[212:215]\n"
    "ds_read_b128 v[116:119], v232 offset:57856\n"
    "v_cvt_pk_bf16_f32 v228, v228, v229\n"
    "v_exp_f32 v230, v230\n"
    "v_mfma_f32_16x16x32_bf16 v[212:215], a[216:219], a[72:75], v[212:215]\n"
    "ds_read_b128 v[120:123], v232 offset:58368\n"
    "v_accvgpr_write_b32 a142, v228\n"
    "v_mul_f32 v231, s52, v231\n"
    "v_mfma_f32_16x16x32_bf16 v[212:215], a[220:223], a[76:79], v[212:215]\n"
    "ds_read_b128 v[124:127], v232 offset:58880\n"
    "v_exp_f32 v231, v231\n"
    "v_mfma_f32_16x16x32_bf16 v[212:215], a[224:227], a[80:83], v[212:215]\n"
    "ds_read_b128 v[128:131], v232 offset:59392\n"
    "v_cvt_pk_bf16_f32 v230, v230, v231\n"
    "v_mul_f32 v208, s52, v208\n"
    "v_mfma_f32_16x16x32_bf16 v[212:215], a[228:231], a[84:87], v[212:215]\n"
    "ds_read_b128 v[132:135], v232 offset:59904\n"
    "v_accvgpr_write_b32 a143, v230\n"
    "v_exp_f32 v208, v208\n"
    "v_mfma_f32_16x16x32_bf16 v[212:215], a[232:235], a[88:91], v[212:215]\n"
    "ds_read_b128 v[136:139], v232 offset:60416\n"
    "v_mul_f32 v209, s52, v209\n"
    "v_mfma_f32_16x16x32_bf16 v[212:215], a[236:239], a[92:95], v[212:215]\n"
    "ds_read_b128 v[140:143], v232 offset:60928\n"
    "v_exp_f32 v209, v209\n"
    "s_waitcnt lgkmcnt(8)\n"
    "v_mfma_f32_16x16x32_bf16 v[216:219], a[176:179], a[64:67], 0\n"
    "ds_read_b128 v[144:147], v233 offset:0\n"
    "v_mul_f32 v210, s52, v210\n"
    "v_cvt_pk_bf16_f32 v208, v208, v209\n"
    "v_mfma_f32_16x16x32_bf16 v[216:219], a[180:183], a[68:71], v[216:219]\n"
    "ds_read_b128 v[148:151], v233 offset:512\n"
    "v_exp_f32 v210, v210\n"
    "v_accvgpr_write_b32 a148, v208\n"
    "v_mfma_f32_16x16x32_bf16 v[216:219], a[184:187], a[72:75], v[216:219]\n"
    "ds_read_b128 v[152:155], v233 offset:1024\n"
    "buffer_store_dword v5, v5, s[48:51], 0 offen\n"
    "v_mul_f32 v211, s52, v211\n"
    "v_mfma_f32_16x16x32_bf16 v[216:219], a[188:191], a[76:79], v[216:219]\n"
    "ds_read_b128 v[156:159], v233 offset:1536\n"
    "v_exp_f32 v211, v211\n"
    "v_mfma_f32_16x16x32_bf16 v[216:219], v[96:99], a[80:83], v[216:219]\n"
    "ds_read_b128 a[208:211], v233 offset:2048\n"
    "v_cvt_pk_bf16_f32 v210, v210, v211\n"
    "v_mfma_f32_16x16x32_bf16 v[216:219], v[100:103], a[84:87], v[216:219]\n"
    "ds_read_b128 a[212:215], v233 offset:2560\n"
    "v_accvgpr_write_b32 a149, v210\n"
    "v_mul_f32 v212, s52, v212\n"
    "v_mfma_f32_16x16x32_bf16 v[216:219], v[104:107], a[88:91], v[216:219]\n"
    "ds_read_b128 a[216:219], v233 offset:3072\n"
    "v_exp_f32 v212, v212\n"
    "v_mfma_f32_16x16x32_bf16 v[216:219], v[108:111], a[92:95], v[216:219]\n"
    "ds_read_b128 a[220:223], v233 offset:3584\n"
    "v_mul_f32 v213, s52, v213\n"
    "v_mfma_f32_16x16x32_bf16 a[248:251], v[12:15], a[140:143], a[248:251]\n"
    "v_exp_f32 v213, v213\n"
    "s_waitcnt lgkmcnt(8)\n"
    "v_mfma_f32_16x16x32_bf16 v[220:223], v[112:115], a[64:67], 0\n"
    "ds_read_b128 a[224:227], v233 offset:8192\n"
    "v_mul_f32 v214, s52, v214\n"
    "v_cvt_pk_bf16_f32 v212, v212, v213\n"
    "v_mfma_f32_16x16x32_bf16 v[220:223], v[116:119], a[68:71], v[220:223]\n"
    "ds_read_b128 a[228:231], v233 offset:8704\n"
    "v_exp_f32 v214, v214\n"
    "v_accvgpr_write_b32 a150, v212\n"
    "v_mfma_f32_16x16x32_bf16 v[220:223], v[120:123], a[72:75], v[220:223]\n"
    "ds_read_b128 a[232:235], v233 offset:9216\n"
    "v_mul_f32 v215, s52, v215\n"
    "v_mfma_f32_16x16x32_bf16 v[220:223], v[124:127], a[76:79], v[220:223]\n"
    "ds_read_b128 a[236:239], v233 offset:9728\n"
    "s_add_u32 m0, s56, 0xc380\n"
    "s_add_u32 s57, s53, 0x1c000\n"
    "buffer_load_dwordx4 v3, s[36:39], s57 offen offset:128 lds\n"
    "v_exp_f32 v215, v215\n"
    "v_mfma_f32_16x16x32_bf16 v[220:223], v[128:131], a[80:83], v[220:223]\n"
    "ds_read_b128 a[176:179], v233 offset:10240\n"
    "v_cvt_pk_bf16_f32 v214, v214, v215\n"
    "v_mul_f32 v216, s52, v216\n"
    "v_mfma_f32_16x16x32_bf16 v[220:223], v[132:135], a[84:87], v[220:223]\n"
    "ds_read_b128 a[180:183], v233 offset:10752\n"
    "v_accvgpr_write_b32 a151, v214\n"
    "v_exp_f32 v216, v216\n"
    "v_mfma_f32_16x16x32_bf16 v[220:223], v[136:139], a[88:91], v[220:223]\n"
    "ds_read_b128 a[184:187], v233 offset:11264\n"
    "v_mul_f32 v217, s52, v217\n"
    "v_mfma_f32_16x16x32_bf16 v[220:223], v[140:143], a[92:95], v[220:223]\n"
    "ds_read_b128 a[188:191], v233 offset:11776\n"
    "v_exp_f32 v217, v217\n"
    "s_waitcnt vmcnt(15) lgkmcnt(8)\n"
    "s_barrier\n"
    "v_mfma_f32_16x16x32_bf16 v[224:227], v[144:147], a[64:67], 0\n"
    "ds_read_b128 v[96:99], v233 offset:16384\n"
    "v_mul_f32 v218, s52, v218\n"
    "v_cvt_pk_bf16_f32 v216, v216, v217\n"
    "v_mfma_f32_16x16x32_bf16 v[224:227], v[148:151], a[68:71], v[224:227]\n"
    "ds_read_b128 v[100:103], v233 offset:16896\n"
    "s_add_u32 m0, s56, 0xc700\n"
    "s_add_u32 s57, s53, 0x1c000\n"
    "buffer_load_dwordx4 v3, s[36:39], s57 offen offset:256 lds\n"
    "v_exp_f32 v218, v218\n"
    "v_mfma_f32_16x16x32_bf16 v[224:227], v[152:155], a[72:75], v[224:227]\n"
    "ds_read_b128 v[104:107], v233 offset:17408\n"
    "v_accvgpr_write_b32 a156, v216\n"
    "v_mul_f32 v219, s52, v219\n"
    "v_mfma_f32_16x16x32_bf16 v[224:227], v[156:159], a[76:79], v[224:227]\n"
    "ds_read_b128 v[108:111], v233 offset:17920\n"
    "v_exp_f32 v219, v219\n"
    "v_mfma_f32_16x16x32_bf16 v[224:227], a[208:211], a[80:83], v[224:227]\n"
    "ds_read_b128 v[112:115], v233 offset:18432\n"
    "v_cvt_pk_bf16_f32 v218, v218, v219\n"
    "v_mfma_f32_16x16x32_bf16 v[224:227], a[212:215], a[84:87], v[224:227]\n"
    "ds_read_b128 v[116:119], v233 offset:18944\n"
    "v_accvgpr_write_b32 a157, v218\n"
    "v_mul_f32 v220, s52, v220\n"
    "v_mfma_f32_16x16x32_bf16 v[224:227], a[216:219], a[88:91], v[224:227]\n"
    "ds_read_b128 v[120:123], v233 offset:19456\n"
    "v_exp_f32 v220, v220\n"
    "v_mfma_f32_16x16x32_bf16 v[224:227], a[220:223], a[92:95], v[224:227]\n"
    "ds_read_b128 v[124:127], v233 offset:19968\n"
    "v_mul_f32 v221, s52, v221\n"
    "v_mfma_f32_16x16x32_bf16 a[248:251], v[12:15], a[148:151], a[248:251]\n"
    "v_exp_f32 v221, v221\n"
    "s_waitcnt lgkmcnt(8)\n"
    "v_mfma_f32_16x16x32_bf16 v[228:231], a[224:227], a[64:67], 0\n"
    "ds_read_b128 v[128:131], v233 offset:24576\n"
    "v_mul_f32 v222, s52, v222\n"
    "v_cvt_pk_bf16_f32 v220, v220, v221\n"
    "v_mfma_f32_16x16x32_bf16 v[228:231], a[228:231], a[68:71], v[228:231]\n"
    "ds_read_b128 v[132:135], v233 offset:25088\n"
    "v_exp_f32 v222, v222\n"
    "v_accvgpr_write_b32 a158, v220\n"
    "v_mfma_f32_16x16x32_bf16 v[228:231], a[232:235], a[72:75], v[228:231]\n"
    "ds_read_b128 v[136:139], v233 offset:25600\n"
    "buffer_store_dword v5, v5, s[48:51], 0 offen\n"
    "v_mul_f32 v223, s52, v223\n"
    "v_mfma_f32_16x16x32_bf16 v[228:231], a[236:239], a[76:79], v[228:231]\n"
    "ds_read_b128 v[140:143], v233 offset:26112\n"
    "v_exp_f32 v223, v223\n"
    "v_mfma_f32_16x16x32_bf16 v[228:231], a[176:179], a[80:83], v[228:231]\n"
    "ds_read_b128 v[144:147], v233 offset:26624\n"
    "v_cvt_pk_bf16_f32 v222, v222, v223\n"
    "v_mul_f32 v224, s52, v224\n"
    "v_mfma_f32_16x16x32_bf16 v[228:231], a[180:183], a[84:87], v[228:231]\n"
    "ds_read_b128 v[148:151], v233 offset:27136\n"
    "v_accvgpr_write_b32 a159, v222\n"
    "v_exp_f32 v224, v224\n"
    "v_mfma_f32_16x16x32_bf16 v[228:231], a[184:187], a[88:91], v[228:231]\n"
    "ds_read_b128 v[152:155], v233 offset:27648\n"
    "v_mul_f32 v225, s52, v225\n"
    "v_mfma_f32_16x16x32_bf16 v[228:231], a[188:191], a[92:95], v[228:231]\n"
    "ds_read_b128 v[156:159], v233 offset:28160\n"
    "v_exp_f32 v225, v225\n"
    "s_waitcnt lgkmcnt(8)\n"
    "v_mfma_f32_16x16x32_bf16 v[208:211], v[96:99], a[0:3], 0\n"
    "ds_read_b128 a[208:211], v232 offset:0\n"
    "v_mul_f32 v226, s52, v226\n"
    "v_cvt_pk_bf16_f32 v40, v224, v225\n"
    "v_mfma_f32_16x16x32_bf16 v[208:211], v[100:103], a[4:7], v[208:211]\n"
    "v_exp_f32 v226, v226\n"
    "v_mfma_f32_16x16x32_bf16 v[208:211], v[104:107], a[8:11], v[208:211]\n"
    "v_mul_f32 v227, s52, v227\n"
    "v_mul_f32 v228, s52, v228\n"
    "v_mfma_f32_16x16x32_bf16 v[208:211], v[108:111], a[12:15], v[208:211]\n"
    "ds_read_b128 a[212:215], v232 offset:512\n"
    "v_exp_f32 v227, v227\n"
    "v_mfma_f32_16x16x32_bf16 v[208:211], v[112:115], a[16:19], v[208:211]\n"
    "s_add_u32 m0, s56, 0xca80\n"
    "s_add_u32 s57, s53, 0x1c000\n"
    "buffer_load_dwordx4 v3, s[36:39], s57 offen offset:384 lds\n"
    "v_cvt_pk_bf16_f32 v41, v226, v227\n"
    "v_mfma_f32_16x16x32_bf16 v[208:211], v[116:119], a[20:23], v[208:211]\n"
    "v_exp_f32 v228, v228\n"
    "v_mfma_f32_16x16x32_bf16 v[208:211], v[120:123], a[24:27], v[208:211]\n"
    "ds_read_b128 a[216:219], v232 offset:1024\n"
    "v_mul_f32 v229, s52, v229\n"
    "v_mfma_f32_16x16x32_bf16 v[208:211], v[124:127], a[28:31], v[208:211]\n"
    "v_exp_f32 v229, v229\n"
    "v_mfma_f32_16x16x32_bf16 a[248:251], v[12:15], a[156:159], a[248:251]\n"
    "v_mul_f32 v230, s52, v230\n"
    "v_cvt_pk_bf16_f32 v42, v228, v229\n"
    "v_mfma_f32_16x16x32_bf16 v[212:215], v[96:99], a[32:35], 0\n"
    "ds_read_b128 a[220:223], v232 offset:1536\n"
    "v_mul_f32 v231, s52, v231\n"
    "v_mfma_f32_16x16x32_bf16 v[212:215], v[100:103], a[36:39], v[212:215]\n"
    "v_exp_f32 v230, v230\n"
    "v_mfma_f32_16x16x32_bf16 v[212:215], v[104:107], a[40:43], v[212:215]\n"
    "v_exp_f32 v231, v231\n"
    "v_mfma_f32_16x16x32_bf16 v[212:215], v[108:111], a[44:47], v[212:215]\n"
    "ds_read_b128 a[224:227], v232 offset:2048\n"
    "v_cvt_pk_bf16_f32 v43, v230, v231\n"
    "v_mfma_f32_16x16x32_bf16 v[212:215], v[112:115], a[48:51], v[212:215]\n"
    "v_mul_f32 v208, s52, v208\n"
    "v_mul_f32 v209, s52, v209\n"
    "v_mfma_f32_16x16x32_bf16 v[212:215], v[116:119], a[52:55], v[212:215]\n"
    "s_add_u32 m0, s56, 0x0\n"
    "s_add_u32 s57, s53, 0x20000\n"
    "buffer_load_dwordx4 v3, s[36:39], s57 offen offset:0 lds\n"
    "v_exp_f32 v208, v208\n"
    "v_mfma_f32_16x16x32_bf16 v[212:215], v[120:123], a[56:59], v[212:215]\n"
    "ds_read_b128 a[228:231], v232 offset:2560\n"
    "v_mul_f32 v210, s52, v210\n"
    "v_mfma_f32_16x16x32_bf16 v[212:215], v[124:127], a[60:63], v[212:215]\n"
    "v_exp_f32 v209, v209\n"
    "v_mfma_f32_16x16x32_bf16 v[216:219], v[96:99], a[64:67], 0\n"
    "v_cvt_pk_bf16_f32 v48, v208, v209\n"
    "v_mul_f32 v211, s52, v211\n"
    "v_mfma_f32_16x16x32_bf16 v[216:219], v[100:103], a[68:71], v[216:219]\n"
    "ds_read_b128 a[232:235], v232 offset:3072\n"
    "v_exp_f32 v210, v210\n"
    "v_mfma_f32_16x16x32_bf16 v[216:219], v[104:107], a[72:75], v[216:219]\n"
    "v_exp_f32 v211, v211\n"
    "v_mfma_f32_16x16x32_bf16 v[216:219], v[108:111], a[76:79], v[216:219]\n"
    "v_cvt_pk_bf16_f32 v49, v210, v211\n"
    "v_mul_f32 v212, s52, v212\n"
    "v_mfma_f32_16x16x32_bf16 v[216:219], v[112:115], a[80:83], v[216:219]\n"
    "ds_read_b128 a[236:239], v232 offset:3584\n"
    "v_mul_f32 v213, s52, v213\n"
    "v_mfma_f32_16x16x32_bf16 v[216:219], v[116:119], a[84:87], v[216:219]\n"
    "v_exp_f32 v212, v212\n"
    "v_mfma_f32_16x16x32_bf16 v[216:219], v[120:123], a[88:91], v[216:219]\n"
    "v_exp_f32 v213, v213\n"
    "v_mfma_f32_16x16x32_bf16 v[216:219], v[124:127], a[92:95], v[216:219]\n"
    "s_add_u32 m0, s56, 0x380\n"
    "s_add_u32 s57, s53, 0x20000\n"
    "buffer_load_dwordx4 v3, s[36:39], s57 offen offset:128 lds\n"
    "s_waitcnt lgkmcnt(8)\n"
    "v_mfma_f32_16x16x32_bf16 v[220:223], v[128:131], a[0:3], 0\n"
    "ds_read_b128 a[176:179], v232 offset:8192\n"
    "v_mul_f32 v214, s52, v214\n"
    "v_mfma_f32_16x16x32_bf16 v[220:223], v[132:135], a[4:7], v[220:223]\n"
    "v_cvt_pk_bf16_f32 v52, v212, v213\n"
    "v_mul_f32 v215, s52, v215\n"
    "v_mfma_f32_16x16x32_bf16 v[220:223], v[136:139], a[8:11], v[220:223]\n"
    "v_exp_f32 v214, v214\n"
    "v_mfma_f32_16x16x32_bf16 v[220:223], v[140:143], a[12:15], v[220:223]\n"
    "ds_read_b128 a[180:183], v232 offset:8704\n"
    "v_mul_f32 v216, s52, v216\n"
    "v_mfma_f32_16x16x32_bf16 v[220:223], v[144:147], a[16:19], v[220:223]\n"
    "v_exp_f32 v215, v215\n"
    "v_mfma_f32_16x16x32_bf16 v[220:223], v[148:151], a[20:23], v[220:223]\n"
    "v_cvt_pk_bf16_f32 v53, v214, v215\n"
    "v_mul_f32 v217, s52, v217\n"
    "v_mfma_f32_16x16x32_bf16 v[220:223], v[152:155], a[24:27], v[220:223]\n"
    "ds_read_b128 a[184:187], v232 offset:9216\n"
    "v_exp_f32 v216, v216\n"
    "v_mfma_f32_16x16x32_bf16 v[220:223], v[156:159], a[28:31], v[220:223]\n"
    "v_exp_f32 v217, v217\n"
    "v_mfma_f32_16x16x32_bf16 v[20:23], v[128:131], a[32:35], 0\n"
    "v_mul_f32 v218, s52, v218\n"
    "v_cvt_pk_bf16_f32 v56, v216, v217\n"
    "v_mfma_f32_16x16x32_bf16 v[20:23], v[132:135], a[36:39], v[20:23]\n"
    "ds_read_b128 a[188:191], v232 offset:9728\n"
    "s_add_u32 m0, s56, 0x700\n"
    "s_add_u32 s57, s53, 0x20000\n"
    "buffer_load_dwordx4 v3, s[36:39], s57 offen offset:256 lds\n"
    "v_mfma_f32_16x16x32_bf16 v[20:23], v[136:139], a[40:43], v[20:23]\n"
    "v_exp_f32 v218, v218\n"
    "v_mfma_f32_16x16x32_bf16 v[20:23], v[140:143], a[44:47], v[20:23]\n"
    "v_mul_f32 v219, s52, v219\n"
    "v_mul_f32 v220, s52, v220\n"
    "v_mfma_f32_16x16x32_bf16 v[20:23], v[144:147], a[48:51], v[20:23]\n"
    "ds_read_b128 v[96:99], v232 offset:10240\n"
    "v_exp_f32 v219, v219\n"
    "v_mfma_f32_16x16x32_bf16 v[20:23], v[148:151], a[52:55], v[20:23]\n"
    "v_cvt_pk_bf16_f32 v57, v218, v219\n"
    "v_mul_f32 v221, s52, v221\n"
    "v_mfma_f32_16x16x32_bf16 v[20:23], v[152:155], a[56:59], v[20:23]\n"
    "v_exp_f32 v220, v220\n"
    "v_mfma_f32_16x16x32_bf16 v[20:23], v[156:159], a[60:63], v[20:23]\n"
    "ds_read_b128 v[100:103], v232 offset:10752\n"
    "v_mul_f32 v222, s52, v222\n"
    "v_mfma_f32_16x16x32_bf16 v[24:27], v[128:131], a[64:67], 0\n"
    "v_exp_f32 v221, v221\n"
    "v_mfma_f32_16x16x32_bf16 v[24:27], v[132:135], a[68:71], v[24:27]\n"
    "v_cvt_pk_bf16_f32 v50, v220, v221\n"
    "v_mul_f32 v223, s52, v223\n"
    "v_mfma_f32_16x16x32_bf16 v[24:27], v[136:139], a[72:75], v[24:27]\n"
    "ds_read_b128 v[104:107], v232 offset:11264\n"
    "v_exp_f32 v222, v222\n"
    "v_mfma_f32_16x16x32_bf16 v[24:27], v[140:143], a[76:79], v[24:27]\n"
    "s_add_u32 m0, s56, 0xa80\n"
    "s_add_u32 s57, s53, 0x20000\n"
    "buffer_load_dwordx4 v3, s[36:39], s57 offen offset:384 lds\n"
    "v_mfma_f32_16x16x32_bf16 v[24:27], v[144:147], a[80:83], v[24:27]\n"
    "v_exp_f32 v223, v223\n"
    "v_mfma_f32_16x16x32_bf16 v[24:27], v[148:151], a[84:87], v[24:27]\n"
    "ds_read_b128 v[108:111], v232 offset:11776\n"
    "v_cvt_pk_bf16_f32 v51, v222, v223\n"
    "v_mul_f32 v20, s52, v20\n"
    "v_mfma_f32_16x16x32_bf16 v[24:27], v[152:155], a[88:91], v[24:27]\n"
    "v_exp_f32 v20, v20\n"
    "v_mfma_f32_16x16x32_bf16 v[24:27], v[156:159], a[92:95], v[24:27]\n"
    "v_mul_f32 v21, s52, v21\n"
    "v_mul_f32 v22, s52, v22\n"
    "s_waitcnt vmcnt(14) lgkmcnt(8)\n"
    "v_mfma_f32_16x16x32_bf16 v[224:227], a[208:211], a[96:99], 0\n"
    "ds_read_b128 v[112:115], v232 offset:16384\n"
    "v_exp_f32 v21, v21\n"
    "v_mfma_f32_16x16x32_bf16 v[224:227], a[212:215], a[100:103], v[224:227]\n"
    "ds_read_b128 v[116:119], v232 offset:16896\n"
    "s_add_u32 m0, s56, 0x4000\n"
    "s_add_u32 s57, s53, 0x24000\n"
    "buffer_load_dwordx4 v3, s[36:39], s57 offen offset:0 lds\n"
    "v_cvt_pk_bf16_f32 v54, v20, v21\n"
    "v_mfma_f32_16x16x32_bf16 v[224:227], a[216:219], a[104:107], v[224:227]\n"
    "ds_read_b128 v[120:123], v232 offset:17408\n"
    "v_exp_f32 v22, v22\n"
    "v_mfma_f32_16x16x32_bf16 v[224:227], a[220:223], a[108:111], v[224:227]\n"
    "ds_read_b128 v[124:127], v232 offset:17920\n"
    "v_mfma_f32_16x16x32_bf16 v[224:227], a[224:227], a[112:115], v[224:227]\n"
    "ds_read_b128 v[128:131], v232 offset:18432\n"
    "v_mfma_f32_16x16x32_bf16 v[224:227], a[228:231], a[116:119], v[224:227]\n"
    "ds_read_b128 v[132:135], v232 offset:18944\n"
    "v_mfma_f32_16x16x32_bf16 v[224:227], a[232:235], a[120:123], v[224:227]\n"
    "ds_read_b128 v[136:139], v232 offset:19456\n"
    "v_mfma_f32_16x16x32_bf16 v[224:227], a[236:239], a[124:127], v[224:227]\n"
    "ds_read_b128 v[140:143], v232 offset:19968\n"
    "s_waitcnt lgkmcnt(8)\n"
    "v_mfma_f32_16x16x32_bf16 v[228:231], a[176:179], a[96:99], 0\n"
    "ds_read_b128 v[144:147], v232 offset:24576\n"
    "v_mfma_f32_16x16x32_bf16 v[228:231], a[180:183], a[100:103], v[228:231]\n"
    "ds_read_b128 v[148:151], v232 offset:25088\n"
    "v_mfma_f32_16x16x32_bf16 v[228:231], a[184:187], a[104:107], v[228:231]\n"
    "ds_read_b128 v[152:155], v232 offset:25600\n"
    "v_mul_f32 v224, s52, v224\n"
    "v_mfma_f32_16x16x32_bf16 v[228:231], a[188:191], a[108:111], v[228:231]\n"
    "ds_read_b128 v[156:159], v232 offset:26112\n"
    "s_add_u32 m0, s56, 0x4380\n"
    "s_add_u32 s57, s53, 0x24000\n"
    "buffer_load_dwordx4 v3, s[36:39], s57 offen offset:128 lds\n"
    "v_mfma_f32_16x16x32_bf16 v[228:231], v[96:99], a[112:115], v[228:231]\n"
    "ds_read_b128 a[208:211], v232 offset:26624\n"
    "v_mul_f32 v225, s52, v225\n"
    "v_mfma_f32_16x16x32_bf16 v[228:231], v[100:103], a[116:119], v[228:231]\n"
    "ds_read_b128 a[212:215], v232 offset:27136\n"
    "v_mul_f32 v226, s52, v226\n"
    "v_mfma_f32_16x16x32_bf16 v[228:231], v[104:107], a[120:123], v[228:231]\n"
    "ds_read_b128 a[216:219], v232 offset:27648\n"
    "v_exp_f32 v224, v224\n"
    "v_mfma_f32_16x16x32_bf16 v[228:231], v[108:111], a[124:127], v[228:231]\n"
    "ds_read_b128 a[220:223], v232 offset:28160\n"
    "v_mul_f32 v227, s52, v227\n"
    "s_waitcnt lgkmcnt(8)\n"
    "v_mfma_f32_16x16x32_bf16 v[208:211], v[112:115], a[96:99], 0\n"
    "ds_read_b128 a[224:227], v232 offset:32768\n"
    "v_exp_f32 v225, v225\n"
    "v_mfma_f32_16x16x32_bf16 v[208:211], v[116:119], a[100:103], v[208:211]\n"
    "ds_read_b128 a[228:231], v232 offset:33280\n"
    "v_cvt_pk_bf16_f32 v164, v224, v225\n"
    "v_mfma_f32_16x16x32_bf16 v[208:211], v[120:123], a[104:107], v[208:211]\n"
    "ds_read_b128 a[232:235], v232 offset:33792\n"
    "v_exp_f32 v226, v226\n"
    "v_mfma_f32_16x16x32_bf16 v[208:211], v[124:127], a[108:111], v[208:211]\n"
    "ds_read_b128 a[236:239], v232 offset:34304\n"
    "v_mul_f32 v228, s52, v228\n"
    "v_mfma_f32_16x16x32_bf16 v[208:211], v[128:131], a[112:115], v[208:211]\n"
    "ds_read_b128 a[176:179], v232 offset:34816\n"
    "v_exp_f32 v227, v227\n"
    "v_mfma_f32_16x16x32_bf16 v[208:211], v[132:135], a[116:119], v[208:211]\n"
    "ds_read_b128 a[180:183], v232 offset:35328\n"
    "s_add_u32 m0, s56, 0x4700\n"
    "s_add_u32 s57, s53, 0x24000\n"
    "buffer_load_dwordx4 v3, s[36:39], s57 offen offset:256 lds\n"
    "v_cvt_pk_bf16_f32 v165, v226, v227\n"
    "v_mfma_f32_16x16x32_bf16 v[208:211], v[136:139], a[120:123], v[208:211]\n"
    "ds_read_b128 a[184:187], v232 offset:35840\n"
    "v_exp_f32 v228, v228\n"
    "v_mfma_f32_16x16x32_bf16 v[208:211], v[140:143], a[124:127], v[208:211]\n"
    "ds_read_b128 a[188:191], v232 offset:36352\n"
    "v_mul_f32 v229, s52, v229\n"
    "s_waitcnt lgkmcnt(8)\n"
    "v_mfma_f32_16x16x32_bf16 v[212:215], v[144:147], a[96:99], 0\n"
    "ds_read_b128 v[96:99], v232 offset:40960\n"
    "v_exp_f32 v229, v229\n"
    "v_mfma_f32_16x16x32_bf16 v[212:215], v[148:151], a[100:103], v[212:215]\n"
    "ds_read_b128 v[100:103], v232 offset:41472\n"
    "v_mul_f32 v230, s52, v230\n"
    "v_cvt_pk_bf16_f32 v166, v228, v229\n"
    "v_mfma_f32_16x16x32_bf16 v[212:215], v[152:155], a[104:107], v[212:215]\n"
    "ds_read_b128 v[104:107], v232 offset:41984\n"
    "v_exp_f32 v230, v230\n"
    "v_mfma_f32_16x16x32_bf16 v[212:215], v[156:159], a[108:111], v[212:215]\n"
    "ds_read_b128 v[108:111], v232 offset:42496\n"
    "v_mul_f32 v231, s52, v231\n"
    "v_mfma_f32_16x16x32_bf16 v[212:215], a[208:211], a[112:115], v[212:215]\n"
    "ds_read_b128 v[112:115], v232 offset:43008\n"
    "v_exp_f32 v231, v231\n"
    "L_pad1_%=:\n"
    "s_cbranch_execz L_ret0_%=\n"
    "v_mfma_f32_16x16x32_bf16 v[212:215], a[212:215], a[116:119], v[212:215]\n"
    "ds_read_b128 v[116:119], v232 offset:43520\n"
    "v_cvt_pk_bf16_f32 v167, v230, v231\n"
    "v_mul_f32 v208, s52, v208\n"
    "v_mfma_f32_16x16x32_bf16 v[212:215], a[216:219], a[120:123], v[212:215]\n"
    "ds_read_b128 v[120:123], v232 offset:44032\n"
    "v_exp_f32 v208, v208\n"
    "v_mfma_f32_16x16x32_bf16 v[212:215], a[220:223], a[124:127], v[212:215]\n"
    "ds_read_b128 v[124:127], v232 offset:44544\n"
    "v_mul_f32 v209, s52, v209\n"
    "s_waitcnt lgkmcnt(8)\n"
    "v_mfma_f32_16x16x32_bf16 v[216:219], a[224:227], a[96:99], 0\n"
    "ds_read_b128 v[128:131], v232 offset:49152\n"
    "v_exp_f32 v209, v209\n"
    "v_mfma_f32_16x16x32_bf16 v[216:219], a[228:231], a[100:103], v[216:219]\n"
    "ds_read_b128 v[132:135], v232 offset:49664\n"
    "v_mul_f32 v210, s52, v210\n"
    "v_cvt_pk_bf16_f32 v172, v208, v209\n"
    "v_mfma_f32_16x16x32_bf16 v[216:219], a[232:235], a[104:107], v[216:219]\n"
    "ds_read_b128 v[136:139], v232 offset:50176\n"
    "v_exp_f32 v210, v210\n"
    "v_mfma_f32_16x16x32_bf16 v[216:219], a[236:239], a[108:111], v[216:219]\n"
    "ds_read_b128 v[140:143], v232 offset:50688\n"
    "v_mul_f32 v211, s52, v211\n"
    "v_mfma_f32_16x16x32_bf16 v[216:219], a[176:179], a[112:115], v[216:219]\n"
    "ds_read_b128 v[144:147], v232 offset:51200\n"
    "v_exp_f32 v211, v211\n"
    "v_mfma_f32_16x16x32_bf16 v[216:219], a[180:183], a[116:119], v[216:219]\n"
    "ds_read_b128 v[148:151], v232 offset:51712\n"
    "v_cvt_pk_bf16_f32 v173, v210, v211\n"
    "v_mul_f32 v212, s52, v212\n"
    "v_mfma_f32_16x16x32_bf16 v[216:219], a[184:187], a[120:123], v[216:219]\n"
    "ds_read_b128 v[152:155], v232 offset:52224\n"
    "v_exp_f32 v212, v212\n"
    "v_mfma_f32_16x16x32_bf16 v[216:219], a[188:191], a[124:127], v[216:219]\n"
    "ds_read_b128 v[156:159], v232 offset:52736\n"
    "v_mul_f32 v213, s52, v213\n"
    "v_mfma_f32_16x16x32_bf16 a[252:255], v[12:15], v[164:167], 0\n"
    "v_exp_f32 v213, v213\n"
    "s_waitcnt lgkmcnt(8)\n"
    "v_mfma_f32_16x16x32_bf16 v[220:223], v[96:99], a[96:99], 0\n"
    "ds_read_b128 a[208:211], v232 offset:57344\n"
    "v_mul_f32 v214, s52, v214\n"
    "v_cvt_pk_bf16_f32 v174, v212, v213\n"
    "v_mfma_f32_16x16x32_bf16 v[220:223], v[100:103], a[100:103], v[220:223]\n"
    "ds_read_b128 a[212:215], v232 offset:57856\n"
    "v_exp_f32 v214, v214\n"
    "v_mfma_f32_16x16x32_bf16 v[220:223], v[104:107], a[104:107], v[220:223]\n"
    "ds_read_b128 a[216:219], v232 offset:58368\n"
    "v_mul_f32 v215, s52, v215\n"
    "v_mfma_f32_16x16x32_bf16 v[220:223], v[108:111], a[108:111], v[220:223]\n"
    "ds_read_b128 a[220:223], v232 offset:58880\n"
    "v_exp_f32 v215, v215\n"
    "v_mfma_f32_16x16x32_bf16 v[220:223], v[112:115], a[112:115], v[220:223]\n"
    "ds_read_b128 a[224:227], v232 offset:59392\n"
    "v_cvt_pk_bf16_f32 v175, v214, v215\n"
    "v_mul_f32 v216, s52, v216\n"
    "v_mfma_f32_16x16x32_bf16 v[220:223], v[116:119], a[116:119], v[220:223]\n"
    "ds_read_b128 a[228:231], v232 offset:59904\n"
    "v_exp_f32 v216, v216\n"
    "v_mfma_f32_16x16x32_bf16 v[220:223], v[120:123], a[120:123], v[220:223]\n"
    "ds_read_b128 a[232:235], v232 offset:60416\n"
    "v_mul_f32 v217, s52, v217\n"
    "v_mfma_f32_16x16x32_bf16 v[220:223], v[124:127], a[124:127], v[220:223]\n"
    "ds_read_b128 a[236:239], v232 offset:60928\n"
    "v_exp_f32 v217, v217\n"
    "s_waitcnt lgkmcnt(8)\n"
    "v_mfma_f32_16x16x32_bf16 v[224:227], v[128:131], a[96:99], 0\n"
    "ds_read_b128 a[176:179], v233 offset:0\n"
    "v_mul_f32 v218, s52, v218\n"
    "v_cvt_pk_bf16_f32 v180, v216, v217\n"
    "v_mfma_f32_16x16x32_bf16 v[224:227], v[132:135], a[100:103], v[224:227]\n"
    "ds_read_b128 a[180:183], v233 offset:512\n"
    "v_exp_f32 v218, v218\n"
    "v_mfma_f32_16x16x32_bf16 v[224:227], v[136:139], a[104:107], v[224:227]\n"
    "ds_read_b128 a[184:187], v233 offset:1024\n"
    "v_mul_f32 v219, s52, v219\n"
    "v_mfma_f32_16x16x32_bf16 v[224:227], v[140:143], a[108:111], v[224:227]\n"
    "ds_read_b128 a[188:191], v233 offset:1536\n"
    "v_exp_f32 v219, v219\n"
    "v_mfma_f32_16x16x32_bf16 v[224:227], v[144:147], a[112:115], v[224:227]\n"
    "ds_read_b128 v[96:99], v233 offset:2048\n"
    "v_cvt_pk_bf16_f32 v181, v218, v219\n"
    "v_mfma_f32_16x16x32_bf16 v[224:227], v[148:151], a[116:119], v[224:227]\n"
    "ds_read_b128 v[100:103], v233 offset:2560\n"
    "v_mul_f32 v220, s52, v220\n"
    "v_mfma_f32_16x16x32_bf16 v[224:227], v[152:155], a[120:123], v[224:227]\n"
    "ds_read_b128 v[104:107], v233 offset:3072\n"
    "v_exp_f32 v220, v220\n"
    "v_mfma_f32_16x16x32_bf16 v[224:227], v[156:159], a[124:127], v[224:227]\n"
    "ds_read_b128 v[108:111], v233 offset:3584\n"
    "v_mul_f32 v221, s52, v221\n"
    "v_mfma_f32_16x16x32_bf16 a[252:255], v[12:15], v[172:175], a[252:255]\n"
    "v_exp_f32 v221, v221\n"
    "s_waitcnt lgkmcnt(8)\n"
    "v_mfma_f32_16x16x32_bf16 v[228:231], a[208:211], a[96:99], 0\n"
    "ds_read_b128 v[112:115], v233 offset:8192\n"
    "v_mul_f32 v222, s52, v222\n"
    "v_cvt_pk_bf16_f32 v182, v220, v221\n"
    "v_mfma_f32_16x16x32_bf16 v[228:231], a[212:215], a[100:103], v[228:231]\n"
    "ds_read_b128 v[116:119], v233 offset:8704\n"
    "v_exp_f32 v222, v222\n"
    "v_mfma_f32_16x16x32_bf16 v[228:231], a[216:219], a[104:107], v[228:231]\n"
    "ds_read_b128 v[120:123], v233 offset:9216\n"
    "v_mul_f32 v223, s52, v223\n"
    "v_mfma_f32_16x16x32_bf16 v[228:231], a[220:223], a[108:111], v[228:231]\n"
    "ds_read_b128 v[124:127], v233 offset:9728\n"
    "v_exp_f32 v223, v223\n"
    "v_mfma_f32_16x16x32_bf16 v[228:231], a[224:227], a[112:115], v[228:231]\n"
    "ds_read_b128 v[128:131], v233 offset:10240\n"
    "v_cvt_pk_bf16_f32 v183, v222, v223\n"
    "v_mul_f32 v224, s52, v224\n"
    "v_mfma_f32_16x16x32_bf16 v[228:231], a[228:231], a[116:119], v[228:231]\n"
    "ds_read_b128 v[132:135], v233 offset:10752\n"
    "v_exp_f32 v224, v224\n"
    "v_mfma_f32_16x16x32_bf16 v[228:231], a[232:235], a[120:123], v[228:231]\n"
    "ds_read_b128 v[136:139], v233 offset:11264\n"
    "v_mul_f32 v225, s52, v225\n"
    "v_mfma_f32_16x16x32_bf16 v[228:231], a[236:239], a[124:127], v[228:231]\n"
    "ds_read_b128 v[140:143], v233 offset:11776\n"
    "v_exp_f32 v225, v225\n"
    "s_waitcnt lgkmcnt(8)\n"
    "v_mfma_f32_16x16x32_bf16 v[208:211], a[176:179], a[96:99], 0\n"
    "ds_read_b128 v[144:147], v233 offset:16384\n"
    "v_mul_f32 v226, s52, v226\n"
    "v_cvt_pk_bf16_f32 v188, v224, v225\n"
    "v_mfma_f32_16x16x32_bf16 v[208:211], a[180:183], a[100:103], v[208:211]\n"
    "ds_read_b128 v[148:151], v233 offset:16896\n"
    "v_exp_f32 v226, v226\n"
    "v_mfma_f32_16x16x32_bf16 v[208:211], a[184:187], a[104:107], v[208:211]\n"
    "ds_read_b128 v[152:155], v233 offset:17408\n"
    "v_mul_f32 v227, s52, v227\n"
    "v_mfma_f32_16x16x32_bf16 v[208:211], a[188:191], a[108:111], v[208:211]\n"
    "ds_read_b128 v[156:159], v233 offset:17920\n"
    "v_exp_f32 v227, v227\n"
    "v_mfma_f32_16x16x32_bf16 v[208:211], v[96:99], a[112:115], v[208:211]\n"
    "ds_read_b128 a[208:211], v233 offset:18432\n"
    "v_cvt_pk_bf16_f32 v189, v226, v227\n"
    "v_mfma_f32_16x16x32_bf16 v[208:211], v[100:103], a[116:119], v[208:211]\n"
    "ds_read_b128 a[212:215], v233 offset:18944\n"
    "v_mul_f32 v228, s52, v228\n"
    "v_mfma_f32_16x16x32_bf16 v[208:211], v[104:107], a[120:123], v[208:211]\n"
    "ds_read_b128 a[216:219], v233 offset:19456\n"
    "v_exp_f32 v228, v228\n"
    "v_mfma_f32_16x16x32_bf16 v[208:211], v[108:111], a[124:127], v[208:211]\n"
    "ds_read_b128 a[220:223], v233 offset:19968\n"
    "v_mul_f32 v229, s52, v229\n"
    "v_mfma_f32_16x16x32_bf16 a[252:255], v[12:15], v[180:183], a[252:255]\n"
    "v_exp_f32 v229, v229\n"
    "s_waitcnt lgkmcnt(8)\n"
    "v_mfma_f32_16x16x32_bf16 v[212:215], v[112:115], a[96:99], 0\n"
    "ds_read_b128 a[224:227], v233 offset:24576\n"
    "v_mul_f32 v230, s52, v230\n"
    "v_cvt_pk_bf16_f32 v190, v228, v229\n"
    "v_mfma_f32_16x16x32_bf16 v[212:215], v[116:119], a[100:103], v[212:215]\n"
    "ds_read_b128 a[228:231], v233 offset:25088\n"
    "v_exp_f32 v230, v230\n"
    "v_mfma_f32_16x16x32_bf16 v[212:215], v[120:123], a[104:107], v[212:215]\n"
    "ds_read_b128 a[232:235], v233 offset:25600\n"
    "v_mul_f32 v231, s52, v231\n"
    "v_mfma_f32_16x16x32_bf16 v[212:215], v[124:127], a[108:111], v[212:215]\n"
    "ds_read_b128 a[236:239], v233 offset:26112\n"
    "v_exp_f32 v231, v231\n"
    "v_mfma_f32_16x16x32_bf16 v[212:215], v[128:131], a[112:115], v[212:215]\n"
    "ds_read_b128 a[176:179], v233 offset:26624\n"
    "v_cvt_pk_bf16_f32 v191, v230, v231\n"
    "v_mul_f32 v208, s52, v208\n"
    "v_mfma_f32_16x16x32_bf16 v[212:215], v[132:135], a[116:119], v[212:215]\n"
    "ds_read_b128 a[180:183], v233 offset:27136\n"
    "v_exp_f32 v208, v208\n"
    "v_mfma_f32_16x16x32_bf16 v[212:215], v[136:139], a[120:123], v[212:215]\n"
    "ds_read_b128 a[184:187], v233 offset:27648\n"
    "v_mul_f32 v209, s52, v209\n"
    "v_mfma_f32_16x16x32_bf16 v[212:215], v[140:143], a[124:127], v[212:215]\n"
    "ds_read_b128 a[188:191], v233 offset:28160\n"
    "v_exp_f32 v209, v209\n"
    "s_waitcnt vmcnt(13) lgkmcnt(8)\n"
    "s_barrier\n"
    "v_mfma_f32_16x16x32_bf16 v[216:219], v[144:147], a[96:99], 0\n"
    "ds_read_b128 a[192:195], v0 offset:32768\n"
    "v_mul_f32 v210, s52, v210\n"
    "v_cvt_pk_bf16_f32 v44, v208, v209\n"
    "v_mfma_f32_16x16x32_bf16 v[216:219], v[148:151], a[100:103], v[216:219]\n"
    "ds_read_b128 a[196:199], v0 offset:33280\n"
    "v_exp_f32 v210, v210\n"
    "v_mfma_f32_16x16x32_bf16 v[216:219], v[152:155], a[104:107], v[216:219]\n"
    "ds_read_b128 a[200:203], v0 offset:33792\n"
    "v_mul_f32 v211, s52, v211\n"
    "v_mfma_f32_16x16x32_bf16 v[216:219], v[156:159], a[108:111], v[216:219]\n"
    "ds_read_b128 a[204:207], v0 offset:34304\n"
    "v_exp_f32 v211, v211\n"
    "v_mfma_f32_16x16x32_bf16 v[216:219], a[208:211], a[112:115], v[216:219]\n"
    "ds_read_b128 a[160:163], v0 offset:34816\n"
    "v_cvt_pk_bf16_f32 v45, v210, v211\n"
    "v_mfma_f32_16x16x32_bf16 v[216:219], a[212:215], a[116:119], v[216:219]\n"
    "ds_read_b128 a[164:167], v0 offset:35328\n"
    "v_mul_f32 v212, s52, v212\n"
    "v_mfma_f32_16x16x32_bf16 v[216:219], a[216:219], a[120:123], v[216:219]\n"
    "ds_read_b128 a[168:171], v0 offset:35840\n"
    "v_exp_f32 v212, v212\n"
    "v_mfma_f32_16x16x32_bf16 v[216:219], a[220:223], a[124:127], v[216:219]\n"
    "ds_read_b128 a[172:175], v0 offset:36352\n"
    "v_mul_f32 v213, s52, v213\n"
    "v_mfma_f32_16x16x32_bf16 a[252:255], v[12:15], v[188:191], a[252:255]\n"
    "v_exp_f32 v213, v213\n"
    "s_waitcnt lgkmcnt(8)\n"
    "v_mfma_f32_16x16x32_bf16 v[28:31], a[224:227], a[96:99], 0\n"
    "v_mul_f32 v214, s52, v214\n"
    "v_cvt_pk_bf16_f32 v46, v212, v213\n"
    "v_mfma_f32_16x16x32_bf16 v[28:31], a[228:231], a[100:103], v[28:31]\n"
    "v_exp_f32 v214, v214\n"
    "v_mfma_f32_16x16x32_bf16 v[28:31], a[232:235], a[104:107], v[28:31]\n"
    "v_mul_f32 v215, s52, v215\n"
    "v_mul_f32 v216, s52, v216\n"
    "v_mfma_f32_16x16x32_bf16 v[28:31], a[236:239], a[108:111], v[28:31]\n"
    "v_exp_f32 v215, v215\n"
    "v_mfma_f32_16x16x32_bf16 v[28:31], a[176:179], a[112:115], v[28:31]\n"
    "v_cvt_pk_bf16_f32 v47, v214, v215\n"
    "v_exp_f32 v216, v216\n"
    "v_mul_f32 v217, s52, v217\n"
    "v_mul_f32 v218, s52, v218\n"
    "v_mul_f32 v219, s52, v219\n"
    "v_mfma_f32_16x16x32_bf16 v[28:31], a[180:183], a[116:119], v[28:31]\n"
    "v_exp_f32 v217, v217\n"
    "v_exp_f32 v218, v218\n"
    "v_exp_f32 v219, v219\n"
    "v_mfma_f32_16x16x32_bf16 v[28:31], a[184:187], a[120:123], v[28:31]\n"
    "v_cvt_pk_bf16_f32 v60, v216, v217\n"
    "v_mfma_f32_16x16x32_bf16 v[28:31], a[188:191], a[124:127], v[28:31]\n"
    "v_cvt_pk_bf16_f32 v61, v218, v219\n"
    "s_add_u32 s55, s77, s92\n"
    "s_mov_b32 s46, s81\n"
    "s_add_u32 s54, s72, s91\n"
    "s_lshl_b32 s85, s93, 2\n"
    "s_add_u32 s60, s77, s85\n"
    "s_add_u32 s61, s60, 0x2000\n"
    "s_add_u32 s62, s60, 0x4000\n"
    "s_add_u32 s63, s60, 0x6000\n"
    "s_add_u32 m0, s56, 0x10000\n"
    "s_add_u32 s57, s55, 0x0\n"
    "buffer_load_dwordx4 v3, s[44:47], s57 offen offset:0 nt lds\n"
    "s_add_u32 m0, s56, 0x10380\n"
    "s_add_u32 s57, s55, 0x0\n"
    "buffer_load_dwordx4 v3, s[44:47], s57 offen offset:128 nt lds\n"
    "s_add_u32 s57, s53, 0x24000\n"
    "s_add_u32 m0, s56, 0x4a80\n"
    "s_waitcnt lgkmcnt(0)\n"
    "s_nop 7\n"
    "s_branch L_ent_%=\n"
    "L_blk_%=:\n"
    "v_mfma_f32_16x16x32_bf16 v[16:19], a[192:195], a[0:3], 0\n"
    "L_pad2_%=:\n"
    "s_cbranch_execz L_ret0_%=\n"
    "s_add_u32 s57, s53, 0xc000\n"
    "v_mov_b32_dpp v104, v136 row_ror:8 row_mask:0xf bank_mask:0xc\n"
    "v_mov_b32_dpp v105, v137 row_ror:8 row_mask:0xf bank_mask:0xc\n"
    "v_mfma_f32_16x16x32_bf16 v[16:19], a[196:199], a[4:7], v[16:19]\n"
    "ds_read_b128 a[208:211], v0 offset:8192\n"
    "v_mov_b32_dpp v106, v138 row_ror:8 row_mask:0xf bank_mask:0xc\n"
    "v_mfma_f32_16x16x32_bf16 v[16:19], a[200:203], a[8:11], v[16:19]\n"
    "s_add_u32 m0, s56, 0xc700\n"
    "v_mov_b32_dpp v107, v139 row_ror:8 row_mask:0xf bank_mask:0xc\n"
    "v_mov_b32_dpp v136, v120 row_ror:8 row_mask:0xf bank_mask:0x3\n"
    "v_mfma_f32_16x16x32_bf16 v[16:19], a[204:207], a[12:15], v[16:19]\n"
    "buffer_load_dwordx4 v3, s[36:39], s57 offen offset:256 lds\n"
    "v_mfma_f32_16x16x32_bf16 v[16:19], a[160:163], a[16:19], v[16:19]\n"
    "ds_read_b128 a[212:215], v0 offset:8704\n"
    "v_mov_b32_dpp v137, v121 row_ror:8 row_mask:0xf bank_mask:0x3\n"
    "v_mfma_f32_16x16x32_bf16 v[16:19], a[164:167], a[20:23], v[16:19]\n"
    "v_mov_b32_dpp v138, v122 row_ror:8 row_mask:0xf bank_mask:0x3\n"
    "v_mov_b32_dpp v139, v123 row_ror:8 row_mask:0xf bank_mask:0x3\n"
    "v_mfma_f32_16x16x32_bf16 v[16:19], a[168:171], a[24:27], v[16:19]\n"
    "s_add_u32 s55, s77, s92\n"
    "v_mul_f32 v124, v11, v124\n"
    "v_mul_f32 v125, v11, v125\n"
    "v_mfma_f32_16x16x32_bf16 v[16:19], a[172:175], a[28:31], v[16:19]\n"
    "ds_read_b128 a[216:219], v0 offset:9216\n"
    "v_mul_f32 v126, v11, v126\n"
    "v_mfma_f32_16x16x32_bf16 v[160:163], a[172:175], a[60:63], 0\n"
    "s_mov_b32 s46, s81\n"
    "v_mul_f32 v127, v11, v127\n"
    "v_mul_f32 v140, v11, v140\n"
    "v_mfma_f32_16x16x32_bf16 v[160:163], a[168:171], a[56:59], v[160:163]\n"
    "s_add_u32 m0, s56, 0xca80\n"
    "v_mul_f32 v141, v11, v141\n"
    "v_mul_f32 v142, v11, v142\n"
    "v_mfma_f32_16x16x32_bf16 v[160:163], a[164:167], a[52:55], v[160:163]\n"
    "ds_read_b128 a[220:223], v0 offset:9728\n"
    "v_mul_f32 v16, s52, v16\n"
    "v_mfma_f32_16x16x32_bf16 v[160:163], a[160:163], a[48:51], v[160:163]\n"
    "buffer_load_dwordx4 v3, s[36:39], s57 offen offset:384 lds\n"
    "v_mfma_f32_16x16x32_bf16 v[160:163], a[204:207], a[44:47], v[160:163]\n"
    "v_exp_f32 v16, v16\n"
    "v_mfma_f32_16x16x32_bf16 v[160:163], a[200:203], a[40:43], v[160:163]\n"
    "ds_read_b128 a[224:227], v0 offset:10240\n"
    "v_mul_f32 v17, s52, v17\n"
    "v_mfma_f32_16x16x32_bf16 v[160:163], a[196:199], a[36:39], v[160:163]\n"
    "v_exp_f32 v17, v17\n"
    "v_mfma_f32_16x16x32_bf16 v[160:163], a[192:195], a[32:35], v[160:163]\n"
    "v_mul_f32 v18, s52, v18\n"
    "v_cvt_pk_bf16_f32 v16, v16, v17\n"
    "v_mfma_f32_16x16x32_bf16 v[24:27], a[192:195], a[64:67], 0\n"
    "ds_read_b128 a[228:231], v0 offset:10752\n"
    "v_accvgpr_write_b32 a128, v16\n"
    "v_mfma_f32_16x16x32_bf16 v[24:27], a[196:199], a[68:71], v[24:27]\n"
    "v_exp_f32 v18, v18\n"
    "v_mfma_f32_16x16x32_bf16 v[24:27], a[200:203], a[72:75], v[24:27]\n"
    "v_mul_f32 v19, s52, v19\n"
    "v_mul_f32 v160, s52, v160\n"
    "v_mfma_f32_16x16x32_bf16 v[24:27], a[204:207], a[76:79], v[24:27]\n"
    "ds_read_b128 a[232:235], v0 offset:11264\n"
    "v_mul_f32 v161, s52, v161\n"
    "v_mfma_f32_16x16x32_bf16 v[24:27], a[160:163], a[80:83], v[24:27]\n"
    "v_exp_f32 v19, v19\n"
    "v_mfma_f32_16x16x32_bf16 v[24:27], a[164:167], a[84:87], v[24:27]\n"
    "v_cvt_pk_bf16_f32 v18, v18, v19\n"
    "v_mul_f32 v162, s52, v162\n"
    "v_mfma_f32_16x16x32_bf16 v[24:27], a[168:171], a[88:91], v[24:27]\n"
    "ds_read_b128 a[236:239], v0 offset:11776\n"
    "v_accvgpr_write_b32 a129, v18\n"
    "v_mfma_f32_16x16x32_bf16 v[24:27], a[172:175], a[92:95], v[24:27]\n"
    "v_exp_f32 v160, v160\n"
    "v_mfma_f32_16x16x32_bf16 v[164:167], a[172:175], a[124:127], 0\n"
    "v_exp_f32 v161, v161\n"
    "v_mfma_f32_16x16x32_bf16 v[164:167], a[168:171], a[120:123], v[164:167]\n"
    "v_cvt_pk_bf16_f32 v160, v160, v161\n"
    "v_mul_f32 v163, s52, v163\n"
    "v_mfma_f32_16x16x32_bf16 v[164:167], a[164:167], a[116:119], v[164:167]\n"
    "v_exp_f32 v162, v162\n"
    "v_mfma_f32_16x16x32_bf16 v[164:167], a[160:163], a[112:115], v[164:167]\n"
    "v_exp_f32 v163, v163\n"
    "v_mfma_f32_16x16x32_bf16 v[164:167], a[204:207], a[108:111], v[164:167]\n"
    "buffer_store_dwordx4 v[96:99], v6, s[48:51], s64 offen nt sc1\n"
    "v_mfma_f32_16x16x32_bf16 v[164:167], a[200:203], a[104:107], v[164:167]\n"
    "v_cvt_pk_bf16_f32 v161, v162, v163\n"
    "v_mul_f32 v24, s52, v24\n"
    "v_mfma_f32_16x16x32_bf16 v[164:167], a[196:199], a[100:103], v[164:167]\n"
    "v_exp_f32 v24, v24\n"
    "v_mfma_f32_16x16x32_bf16 v[164:167], a[192:195], a[96:99], v[164:167]\n"
    "s_waitcnt vmcnt(13) lgkmcnt(0)\n"
    "v_mul_f32 v25, s52, v25\n"
    "v_mul_f32 v26, s52, v26\n"
    "v_mfma_f32_16x16x32_bf16 v[16:19], a[208:211], a[0:3], 0\n"
    "s_barrier\n"
    "v_exp_f32 v25, v25\n"
    "v_mfma_f32_16x16x32_bf16 v[16:19], a[212:215], a[4:7], v[16:19]\n"
    "ds_read_b128 a[192:195], v0 offset:16384\n"
    "v_cvt_pk_bf16_f32 v24, v24, v25\n"
    "v_mfma_f32_16x16x32_bf16 v[16:19], a[216:219], a[8:11], v[16:19]\n"
    "s_add_u32 s57, s53, 0x10000\n"
    "v_exp_f32 v26, v26\n"
    "v_mfma_f32_16x16x32_bf16 v[16:19], a[220:223], a[12:15], v[16:19]\n"
    "s_add_u32 m0, s56, 0x0\n"
    "v_accvgpr_write_b32 a132, v24\n"
    "v_mul_f32 v27, s52, v27\n"
    "v_mfma_f32_16x16x32_bf16 v[16:19], a[224:227], a[16:19], v[16:19]\n"
    "ds_read_b128 a[196:199], v0 offset:16896\n"
    "v_mul_f32 v164, s52, v164\n"
    "v_mfma_f32_16x16x32_bf16 v[16:19], a[228:231], a[20:23], v[16:19]\n"
    "buffer_load_dwordx4 v3, s[36:39], s57 offen offset:0 lds\n"
    "v_mfma_f32_16x16x32_bf16 v[16:19], a[232:235], a[24:27], v[16:19]\n"
    "v_exp_f32 v27, v27\n"
    "v_mfma_f32_16x16x32_bf16 v[16:19], a[236:239], a[28:31], v[16:19]\n"
    "ds_read_b128 a[200:203], v0 offset:17408\n"
    "v_cvt_pk_bf16_f32 v26, v26, v27\n"
    "v_mfma_f32_16x16x32_bf16 v[20:23], a[236:239], a[60:63], 0\n"
    "v_accvgpr_write_b32 a133, v26\n"
    "v_mul_f32 v165, s52, v165\n"
    "v_mfma_f32_16x16x32_bf16 v[20:23], a[232:235], a[56:59], v[20:23]\n"
    "v_exp_f32 v164, v164\n"
    "v_mfma_f32_16x16x32_bf16 v[20:23], a[228:231], a[52:55], v[20:23]\n"
    "ds_read_b128 a[204:207], v0 offset:17920\n"
    "v_mul_f32 v166, s52, v166\n"
    "v_mfma_f32_16x16x32_bf16 v[20:23], a[224:227], a[48:51], v[20:23]\n"
    "v_exp_f32 v165, v165\n"
    "v_mfma_f32_16x16x32_bf16 v[20:23], a[220:223], a[44:47], v[20:23]\n"
    "v_cvt_pk_bf16_f32 v164, v164, v165\n"
    "v_mul_f32 v167, s52, v167\n"
    "v_mfma_f32_16x16x32_bf16 v[20:23], a[216:219], a[40:43], v[20:23]\n"
    "ds_read_b128 a[160:163], v0 offset:18432\n"
    "v_mul_f32 v16, s52, v16\n"
    "v_mfma_f32_16x16x32_bf16 v[20:23], a[212:215], a[36:39], v[20:23]\n"
    "v_exp_f32 v166, v166\n"
    "v_mfma_f32_16x16x32_bf16 v[20:23], a[208:211], a[32:35], v[20:23]\n"
    "v_exp_f32 v167, v167\n"
    "v_mfma_f32_16x16x32_bf16 v[24:27], a[208:211], a[64:67], 0\n"
    "ds_read_b128 a[164:167], v0 offset:18944\n"
    "v_cvt_pk_bf16_f32 v165, v166, v167\n"
    "v_mfma_f32_16x16x32_bf16 v[24:27], a[212:215], a[68:71], v[24:27]\n"
    "s_add_u32 m0, s56, 0x380\n"
    "v_exp_f32 v16, v16\n"
    "v_mfma_f32_16x16x32_bf16 v[24:27], a[216:219], a[72:75], v[24:27]\n"
    "buffer_load_dwordx4 v3, s[36:39], s57 offen offset:128 lds\n"
    "v_mfma_f32_16x16x32_bf16 v[24:27], a[220:223], a[76:79], v[24:27]\n"
    "ds_read_b128 a[168:171], v0 offset:19456\n"
    "v_mul_f32 v17, s52, v17\n"
    "v_mfma_f32_16x16x32_bf16 v[24:27], a[224:227], a[80:83], v[24:27]\n"
    "v_exp_f32 v17, v17\n"
    "v_mfma_f32_16x16x32_bf16 v[24:27], a[228:231], a[84:87], v[24:27]\n"
    "v_mul_f32 v18, s52, v18\n"
    "v_cvt_pk_bf16_f32 v16, v16, v17\n"
    "v_mfma_f32_16x16x32_bf16 v[24:27], a[232:235], a[88:91], v[24:27]\n"
    "ds_read_b128 a[172:175], v0 offset:19968\n"
    "v_accvgpr_write_b32 a130, v16\n"
    "v_mfma_f32_16x16x32_bf16 v[24:27], a[236:239], a[92:95], v[24:27]\n"
    "v_exp_f32 v18, v18\n"
    "v_mfma_f32_16x16x32_bf16 v[28:31], a[236:239], a[124:127], 0\n"
    "v_mul_f32 v19, s52, v19\n"
    "v_mul_f32 v20, s52, v20\n"
    "v_mfma_f32_16x16x32_bf16 v[28:31], a[232:235], a[120:123], v[28:31]\n"
    "v_exp_f32 v19, v19\n"
    "v_mfma_f32_16x16x32_bf16 v[28:31], a[228:231], a[116:119], v[28:31]\n"
    "v_cvt_pk_bf16_f32 v18, v18, v19\n"
    "v_mul_f32 v21, s52, v21\n"
    "v_mfma_f32_16x16x32_bf16 v[28:31], a[224:227], a[112:115], v[28:31]\n"
    "v_accvgpr_write_b32 a131, v18\n"
    "v_mul_f32 v22, s52, v22\n"
    "v_mfma_f32_16x16x32_bf16 v[28:31], a[220:223], a[108:111], v[28:31]\n"
    "v_exp_f32 v20, v20\n"
    "v_mfma_f32_16x16x32_bf16 v[28:31], a[216:219], a[104:107], v[28:31]\n"
    "v_exp_f32 v21, v21\n"
    "v_mfma_f32_16x16x32_bf16 v[28:31], a[212:215], a[100:103], v[28:31]\n"
    "s_add_u32 m0, s56, 0x700\n"
    "v_cvt_pk_bf16_f32 v162, v20, v21\n"
    "v_mul_f32 v23, s52, v23\n"
    "v_mfma_f32_16x16x32_bf16 v[28:31], a[208:211], a[96:99], v[28:31]\n"
    "s_waitcnt lgkmcnt(0)\n"
    "v_exp_f32 v22, v22\n"
    "v_mfma_f32_16x16x32_bf16 v[16:19], a[192:195], a[0:3], 0\n"
    "buffer_load_dwordx4 v3, s[36:39], s57 offen offset:256 lds\n"
    "v_mfma_f32_16x16x32_bf16 v[16:19], a[196:199], a[4:7], v[16:19]\n"
    "ds_read_b128 a[208:211], v0 offset:24576\n"
    "v_mul_f32 v24, s52, v24\n"
    "v_mfma_f32_16x16x32_bf16 v[16:19], a[200:203], a[8:11], v[16:19]\n"
    "v_exp_f32 v23, v23\n"
    "v_mfma_f32_16x16x32_bf16 v[16:19], a[204:207], a[12:15], v[16:19]\n"
    "v_cvt_pk_bf16_f32 v163, v22, v23\n"
    "v_mul_f32 v25, s52, v25\n"
    "v_mfma_f32_16x16x32_bf16 v[16:19], a[160:163], a[16:19], v[16:19]\n"
    "ds_read_b128 a[212:215], v0 offset:25088\n"
    "v_mul_f32 v26, s52, v26\n"
    "v_mfma_f32_16x16x32_bf16 v[16:19], a[164:167], a[20:23], v[16:19]\n"
    "buffer_store_dwordx4 v[128:131], v6, s[48:51], s65 offen nt sc1\n"
    "v_mfma_f32_16x16x32_bf16 v[16:19], a[168:171], a[24:27], v[16:19]\n"
    "v_exp_f32 v24, v24\n"
    "v_mfma_f32_16x16x32_bf16 v[16:19], a[172:175], a[28:31], v[16:19]\n"
    "ds_read_b128 a[216:219], v0 offset:25600\n"
    "v_mul_f32 v27, s52, v27\n"
    "v_mfma_f32_16x16x32_bf16 v[168:171], a[172:175], a[60:63], 0\n"
    "v_exp_f32 v25, v25\n"
    "v_mfma_f32_16x16x32_bf16 v[168:171], a[168:171], a[56:59], v[168:171]\n"
    "v_cvt_pk_bf16_f32 v24, v24, v25\n"
    "v_mul_f32 v28, s52, v28\n"
    "v_mfma_f32_16x16x32_bf16 v[168:171], a[164:167], a[52:55], v[168:171]\n"
    "ds_read_b128 a[220:223], v0 offset:26112\n"
    "v_accvgpr_write_b32 a134, v24\n"
    "v_mfma_f32_16x16x32_bf16 v[168:171], a[160:163], a[48:51], v[168:171]\n"
    "v_exp_f32 v26, v26\n"
    "v_mfma_f32_16x16x32_bf16 v[168:171], a[204:207], a[44:47], v[168:171]\n"
    "s_add_u32 m0, s56, 0xa80\n"
    "v_exp_f32 v27, v27\n"
    "v_mul_f32 v29, s52, v29\n"
    "v_mfma_f32_16x16x32_bf16 v[168:171], a[200:203], a[40:43], v[168:171]\n"
    "ds_read_b128 a[224:227], v0 offset:26624\n"
    "v_cvt_pk_bf16_f32 v26, v26, v27\n"
    "v_mfma_f32_16x16x32_bf16 v[168:171], a[196:199], a[36:39], v[168:171]\n"
    "buffer_load_dwordx4 v3, s[36:39], s57 offen offset:384 lds\n"
    "v_mfma_f32_16x16x32_bf16 v[168:171], a[192:195], a[32:35], v[168:171]\n"
    "v_accvgpr_write_b32 a135, v26\n"
    "v_mul_f32 v30, s52, v30\n"
    "v_mfma_f32_16x16x32_bf16 v[24:27], a[192:195], a[64:67], 0\n"
    "ds_read_b128 a[228:231], v0 offset:27136\n"
    "v_mul_f32 v31, s52, v31\n"
    "v_mfma_f32_16x16x32_bf16 v[24:27], a[196:199], a[68:71], v[24:27]\n"
    "v_exp_f32 v28, v28\n"
    "v_mfma_f32_16x16x32_bf16 v[24:27], a[200:203], a[72:75], v[24:27]\n"
    "v_exp_f32 v29, v29\n"
    "v_mul_f32 v16, s52, v16\n"
    "v_mfma_f32_16x16x32_bf16 v[24:27], a[204:207], a[76:79], v[24:27]\n"
    "ds_read_b128 a[232:235], v0 offset:27648\n"
    "v_cvt_pk_bf16_f32 v166, v28, v29\n"
    "v_mfma_f32_16x16x32_bf16 v[24:27], a[160:163], a[80:83], v[24:27]\n"
    "v_exp_f32 v30, v30\n"
    "v_mul_f32 v17, s52, v17\n"
    "v_mfma_f32_16x16x32_bf16 v[24:27], a[164:167], a[84:87], v[24:27]\n"
    "v_exp_f32 v31, v31\n"
    "v_mfma_f32_16x16x32_bf16 v[24:27], a[168:171], a[88:91], v[24:27]\n"
    "ds_read_b128 a[236:239], v0 offset:28160\n"
    "v_cvt_pk_bf16_f32 v167, v30, v31\n"
    "v_mul_f32 v18, s52, v18\n"
    "v_mfma_f32_16x16x32_bf16 v[24:27], a[172:175], a[92:95], v[24:27]\n"
    "v_exp_f32 v16, v16\n"
    "v_mfma_f32_16x16x32_bf16 v[172:175], a[172:175], a[124:127], 0\n"
    "v_exp_f32 v17, v17\n"
    "v_mul_f32 v19, s52, v19\n"
    "v_mfma_f32_16x16x32_bf16 v[172:175], a[168:171], a[120:123], v[172:175]\n"
    "v_cvt_pk_bf16_f32 v16, v16, v17\n"
    "v_mul_f32 v168, s52, v168\n"
    "v_mfma_f32_16x16x32_bf16 v[172:175], a[164:167], a[116:119], v[172:175]\n"
    "v_exp_f32 v18, v18\n"
    "v_accvgpr_write_b32 a136, v16\n"
    "v_mfma_f32_16x16x32_bf16 v[172:175], a[160:163], a[112:115], v[172:175]\n"
    "v_exp_f32 v19, v19\n"
    "v_mfma_f32_16x16x32_bf16 v[172:175], a[204:207], a[108:111], v[172:175]\n"
    "v_cvt_pk_bf16_f32 v18, v18, v19\n"
    "v_mul_f32 v169, s52, v169\n"
    "v_mfma_f32_16x16x32_bf16 v[172:175], a[200:203], a[104:107], v[172:175]\n"
    "v_accvgpr_write_b32 a137, v18\n"
    "v_mul_f32 v170, s52, v170\n"
    "v_mfma_f32_16x16x32_bf16 v[172:175], a[196:199], a[100:103], v[172:175]\n"
    "v_exp_f32 v168, v168\n"
    "v_mfma_f32_16x16x32_bf16 v[172:175], a[192:195], a[96:99], v[172:175]\n"
    "s_waitcnt vmcnt(11) lgkmcnt(0)\n"
    "v_exp_f32 v169, v169\n"
    "v_mfma_f32_16x16x32_bf16 v[16:19], a[208:211], a[0:3], 0\n"
    "s_barrier\n"
    "v_cvt_pk_bf16_f32 v168, v168, v169\n"
    "v_mul_f32 v171, s52, v171\n"
    "v_mfma_f32_16x16x32_bf16 v[16:19], a[212:215], a[4:7], v[16:19]\n"
    "ds_read_b128 a[192:195], v0 offset:32768\n"
    "v_mul_f32 v24, s52, v24\n"
    "v_mfma_f32_16x16x32_bf16 v[16:19], a[216:219], a[8:11], v[16:19]\n"
    "s_add_u32 s57, s53, 0x14000\n"
    "v_exp_f32 v170, v170\n"
    "v_mfma_f32_16x16x32_bf16 v[16:19], a[220:223], a[12:15], v[16:19]\n"
    "s_add_u32 m0, s56, 0x4000\n"
    "v_exp_f32 v171, v171\n"
    "v_mfma_f32_16x16x32_bf16 v[16:19], a[224:227], a[16:19], v[16:19]\n"
    "ds_read_b128 a[196:199], v0 offset:33280\n"
    "v_cvt_pk_bf16_f32 v169, v170, v171\n"
    "v_mul_f32 v25, s52, v25\n"
    "v_mfma_f32_16x16x32_bf16 v[16:19], a[228:231], a[20:23], v[16:19]\n"
    "buffer_load_dwordx4 v3, s[36:39], s57 offen offset:0 lds\n"
    "v_mfma_f32_16x16x32_bf16 v[16:19], a[232:235], a[24:27], v[16:19]\n"
    "v_exp_f32 v24, v24\n"
    "v_mul_f32 v26, s52, v26\n"
    "v_mfma_f32_16x16x32_bf16 v[16:19], a[236:239], a[28:31], v[16:19]\n"
    "ds_read_b128 a[200:203], v0 offset:33792\n"
    "v_mul_f32 v27, s52, v27\n"
    "v_mfma_f32_16x16x32_bf16 v[20:23], a[236:239], a[60:63], 0\n"
    "v_exp_f32 v25, v25\n"
    "v_mfma_f32_16x16x32_bf16 v[20:23], a[232:235], a[56:59], v[20:23]\n"
    "buffer_store_dwordx4 v[100:103], v6, s[48:51], s66 offen nt sc1\n"
    "v_mfma_f32_16x16x32_bf16 v[20:23], a[228:231], a[52:55], v[20:23]\n"
    "ds_read_b128 a[204:207], v0 offset:34304\n"
    "v_cvt_pk_bf16_f32 v24, v24, v25\n"
    "v_mul_f32 v172, s52, v172\n"
    "v_mfma_f32_16x16x32_bf16 v[20:23], a[224:227], a[48:51], v[20:23]\n"
    "v_exp_f32 v26, v26\n"
    "v_accvgpr_write_b32 a140, v24\n"
    "v_mfma_f32_16x16x32_bf16 v[20:23], a[220:223], a[44:47], v[20:23]\n"
    "v_exp_f32 v27, v27\n"
    "v_mul_f32 v173, s52, v173\n"
    "v_mfma_f32_16x16x32_bf16 v[20:23], a[216:219], a[40:43], v[20:23]\n"
    "ds_read_b128 a[160:163], v0 offset:34816\n"
    "v_cvt_pk_bf16_f32 v26, v26, v27\n"
    "v_mfma_f32_16x16x32_bf16 v[20:23], a[212:215], a[36:39], v[20:23]\n"
    "v_accvgpr_write_b32 a141, v26\n"
    "v_mul_f32 v174, s52, v174\n"
    "v_mfma_f32_16x16x32_bf16 v[20:23], a[208:211], a[32:35], v[20:23]\n"
    "v_exp_f32 v172, v172\n"
    "v_mfma_f32_16x16x32_bf16 v[24:27], a[208:211], a[64:67], 0\n"
    "ds_read_b128 a[164:167], v0 offset:35328\n"
    "v_mul_f32 v175, s52, v175\n"
    "v_mfma_f32_16x16x32_bf16 v[24:27], a[212:215], a[68:71], v[24:27]\n"
    "s_add_u32 m0, s56, 0x4380\n"
    "v_exp_f32 v173, v173\n"
    "v_mfma_f32_16x16x32_bf16 v[24:27], a[216:219], a[72:75], v[24:27]\n"
    "buffer_load_dwordx4 v3, s[36:39], s57 offen offset:128 lds\n"
    "v_mfma_f32_16x16x32_bf16 v[24:27], a[220:223], a[76:79], v[24:27]\n"
    "ds_read_b128 a[168:171], v0 offset:35840\n"
    "v_cvt_pk_bf16_f32 v172, v172, v173\n"
    "v_mul_f32 v16, s52, v16\n"
    "v_mfma_f32_16x16x32_bf16 v[24:27], a[224:227], a[80:83], v[24:27]\n"
    "v_exp_f32 v174, v174\n"
    "v_mul_f32 v17, s52, v17\n"
    "v_mfma_f32_16x16x32_bf16 v[24:27], a[228:231], a[84:87], v[24:27]\n"
    "v_exp_f32 v175, v175\n"
    "v_mfma_f32_16x16x32_bf16 v[24:27], a[232:235], a[88:91], v[24:27]\n"
    "ds_read_b128 a[172:175], v0 offset:36352\n"
    "v_cvt_pk_bf16_f32 v173, v174, v175\n"
    "v_mul_f32 v18, s52, v18\n"
    "v_mfma_f32_16x16x32_bf16 v[24:27], a[236:239], a[92:95], v[24:27]\n"
    "v_exp_f32 v16, v16\n"
    "v_mfma_f32_16x16x32_bf16 v[28:31], a[236:239], a[124:127], 0\n"
    "v_exp_f32 v17, v17\n"
    "v_mul_f32 v19, s52, v19\n"
    "v_mfma_f32_16x16x32_bf16 v[28:31], a[232:235], a[120:123], v[28:31]\n"
    "v_cvt_pk_bf16_f32 v16, v16, v17\n"
    "v_mul_f32 v20, s52, v20\n"
    "v_mfma_f32_16x16x32_bf16 v[28:31], a[228:231], a[116:119], v[28:31]\n"
    "v_exp_f32 v18, v18\n"
    "v_accvgpr_write_b32 a138, v16\n"
    "v_mfma_f32_16x16x32_bf16 v[28:31], a[224:227], a[112:115], v[28:31]\n"
    "v_exp_f32 v19, v19\n"
    "v_mfma_f32_16x16x32_bf16 v[28:31], a[220:223], a[108:111], v[28:31]\n"
    "v_cvt_pk_bf16_f32 v18, v18, v19\n"
    "v_mul_f32 v21, s52, v21\n"
    "v_mfma_f32_16x16x32_bf16 v[28:31], a[216:219], a[104:107], v[28:31]\n"
    "v_accvgpr_write_b32 a139, v18\n"
    "v_mul_f32 v22, s52, v22\n"
    "v_mfma_f32_16x16x32_bf16 v[28:31], a[212:215], a[100:103], v[28:31]\n"
    "v_exp_f32 v20, v20\n"
    "v_mfma_f32_16x16x32_bf16 v[28:31], a[208:211], a[96:99], v[28:31]\n"
    "s_waitcnt lgkmcnt(0)\n"
    "v_exp_f32 v21, v21\n"
    "v_mfma_f32_16x16x32_bf16 v[16:19], a[192:195], a[0:3], 0\n"
    "s_add_u32 m0, s56, 0x4700\n"
    "v_cvt_pk_bf16_f32 v170, v20, v21\n"
    "v_mul_f32 v23, s52, v23\n"
    "v_mfma_f32_16x16x32_bf16 v[16:19], a[196:199], a[4:7], v[16:19]\n"
    "ds_read_b128 a[208:211], v0 offset:40960\n"
    "v_mul_f32 v24, s52, v24\n"
    "v_mfma_f32_16x16x32_bf16 v[16:19], a[200:203], a[8:11], v[16:19]\n"
    "buffer_load_dwordx4 v3, s[36:39], s57 offen offset:256 lds\n"
    "v_mfma_f32_16x16x32_bf16 v[16:19], a[204:207], a[12:15], v[16:19]\n"
    "v_exp_f32 v22, v22\n"
    "v_mfma_f32_16x16x32_bf16 v[16:19], a[160:163], a[16:19], v[16:19]\n"
    "ds_read_b128 a[212:215], v0 offset:41472\n"
    "v_mul_f32 v25, s52, v25\n"
    "v_mfma_f32_16x16x32_bf16 v[16:19], a[164:167], a[20:23], v[16:19]\n"
    "L_pad3_%=:\n"
    "s_cbranch_execz L_ret0_%=\n"
    "v_exp_f32 v23, v23\n"
    "v_mfma_f32_16x16x32_bf16 v[16:19], a[168:171], a[24:27], v[16:19]\n"
    "buffer_store_dwordx4 v[132:135], v6, s[48:51], s67 offen nt sc1\n"
    "v_mfma_f32_16x16x32_bf16 v[16:19], a[172:175], a[28:31], v[16:19]\n"
    "ds_read_b128 a[216:219], v0 offset:41984\n"
    "v_cvt_pk_bf16_f32 v171, v22, v23\n"
    "v_mul_f32 v26, s52, v26\n"
    "v_mfma_f32_16x16x32_bf16 v[176:179], a[172:175], a[60:63], 0\n"
    "v_exp_f32 v24, v24\n"
    "v_mul_f32 v27, s52, v27\n"
    "v_mfma_f32_16x16x32_bf16 v[176:179], a[168:171], a[56:59], v[176:179]\n"
    "v_exp_f32 v25, v25\n"
    "v_mfma_f32_16x16x32_bf16 v[176:179], a[164:167], a[52:55], v[176:179]\n"
    "ds_read_b128 a[220:223], v0 offset:42496\n"
    "v_cvt_pk_bf16_f32 v24, v24, v25\n"
    "v_mul_f32 v28, s52, v28\n"
    "v_mfma_f32_16x16x32_bf16 v[176:179], a[160:163], a[48:51], v[176:179]\n"
    "v_exp_f32 v26, v26\n"
    "v_accvgpr_write_b32 a142, v24\n"
    "v_mfma_f32_16x16x32_bf16 v[176:179], a[204:207], a[44:47], v[176:179]\n"
    "v_exp_f32 v27, v27\n"
    "v_mul_f32 v29, s52, v29\n"
    "v_mfma_f32_16x16x32_bf16 v[176:179], a[200:203], a[40:43], v[176:179]\n"
    "ds_read_b128 a[224:227], v0 offset:43008\n"
    "v_cvt_pk_bf16_f32 v26, v26, v27\n"
    "v_mfma_f32_16x16x32_bf16 v[176:179], a[196:199], a[36:39], v[176:179]\n"
    "s_add_u32 m0, s56, 0x4a80\n"
    "v_accvgpr_write_b32 a143, v26\n"
    "v_mul_f32 v30, s52, v30\n"
    "v_mfma_f32_16x16x32_bf16 v[176:179], a[192:195], a[32:35], v[176:179]\n"
    "buffer_load_dwordx4 v3, s[36:39], s57 offen offset:384 lds\n"
    "v_mfma_f32_16x16x32_bf16 v[24:27], a[192:195], a[64:67], 0\n"
    "ds_read_b128 a[228:231], v0 offset:43520\n"
    "v_mul_f32 v31, s52, v31\n"
    "v_mfma_f32_16x16x32_bf16 v[24:27], a[196:199], a[68:71], v[24:27]\n"
    "v_exp_f32 v28, v28\n"
    "v_mfma_f32_16x16x32_bf16 v[24:27], a[200:203], a[72:75], v[24:27]\n"
    "v_exp_f32 v29, v29\n"
    "v_mul_f32 v16, s52, v16\n"
    "v_mfma_f32_16x16x32_bf16 v[24:27], a[204:207], a[76:79], v[24:27]\n"
    "ds_read_b128 a[232:235], v0 offset:44032\n"
    "v_cvt_pk_bf16_f32 v174, v28, v29\n"
    "v_mfma_f32_16x16x32_bf16 v[24:27], a[160:163], a[80:83], v[24:27]\n"
    "v_exp_f32 v30, v30\n"
    "v_mul_f32 v17, s52, v17\n"
    "v_mfma_f32_16x16x32_bf16 v[24:27], a[164:167], a[84:87], v[24:27]\n"
    "v_exp_f32 v31, v31\n"
    "v_mfma_f32_16x16x32_bf16 v[24:27], a[168:171], a[88:91], v[24:27]\n"
    "ds_read_b128 a[236:239], v0 offset:44544\n"
    "v_cvt_pk_bf16_f32 v175, v30, v31\n"
    "v_mul_f32 v18, s52, v18\n"
    "v_mfma_f32_16x16x32_bf16 v[24:27], a[172:175], a[92:95], v[24:27]\n"
    "v_exp_f32 v16, v16\n"
    "v_mfma_f32_16x16x32_bf16 v[180:183], a[172:175], a[124:127], 0\n"
    "v_exp_f32 v17, v17\n"
    "v_mul_f32 v19, s52, v19\n"
    "v_mfma_f32_16x16x32_bf16 v[180:183], a[168:171], a[120:123], v[180:183]\n"
    "v_cvt_pk_bf16_f32 v16, v16, v17\n"
    "v_mul_f32 v176, s52, v176\n"
    "v_mfma_f32_16x16x32_bf16 v[180:183], a[164:167], a[116:119], v[180:183]\n"
    "v_exp_f32 v18, v18\n"
    "v_accvgpr_write_b32 a144, v16\n"
    "v_mfma_f32_16x16x32_bf16 v[180:183], a[160:163], a[112:115], v[180:183]\n"
    "v_exp_f32 v19, v19\n"
    "v_mfma_f32_16x16x32_bf16 v[180:183], a[204:207], a[108:111], v[180:183]\n"
    "v_cvt_pk_bf16_f32 v18, v18, v19\n"
    "v_mul_f32 v177, s52, v177\n"
    "v_mfma_f32_16x16x32_bf16 v[180:183], a[200:203], a[104:107], v[180:183]\n"
    "v_accvgpr_write_b32 a145, v18\n"
    "v_mul_f32 v178, s52, v178\n"
    "v_mfma_f32_16x16x32_bf16 v[180:183], a[196:199], a[100:103], v[180:183]\n"
    "v_exp_f32 v176, v176\n"
    "v_mfma_f32_16x16x32_bf16 v[180:183], a[192:195], a[96:99], v[180:183]\n"
    "v_exp_f32 v177, v177\n"
    "v_mfma_f32_16x16x32_bf16 a[240:243], v[12:15], a[128:131], 0\n"
    "v_cvt_pk_bf16_f32 v176, v176, v177\n"
    "v_mul_f32 v179, s52, v179\n"
    "v_mfma_f32_16x16x32_bf16 a[244:247], v[12:15], v[160:163], 0\n"
    "s_waitcnt vmcnt(12) lgkmcnt(0)\n"
    "v_exp_f32 v178, v178\n"
    "v_mfma_f32_16x16x32_bf16 v[16:19], a[208:211], a[0:3], 0\n"
    "s_barrier\n"
    "v_exp_f32 v179, v179\n"
    "v_mul_f32 v24, s52, v24\n"
    "v_mfma_f32_16x16x32_bf16 v[16:19], a[212:215], a[4:7], v[16:19]\n"
    "ds_read_b128 a[192:195], v0 offset:49152\n"
    "v_cvt_pk_bf16_f32 v177, v178, v179\n"
    "v_mfma_f32_16x16x32_bf16 v[16:19], a[216:219], a[8:11], v[16:19]\n"
    "s_add_u32 s57, s53, 0x18000\n"
    "v_exp_f32 v24, v24\n"
    "v_mul_f32 v25, s52, v25\n"
    "v_mfma_f32_16x16x32_bf16 v[16:19], a[220:223], a[12:15], v[16:19]\n"
    "s_add_u32 m0, s56, 0x8000\n"
    "v_exp_f32 v25, v25\n"
    "v_mfma_f32_16x16x32_bf16 v[16:19], a[224:227], a[16:19], v[16:19]\n"
    "ds_read_b128 a[196:199], v0 offset:49664\n"
    "v_mul_f32 v26, s52, v26\n"
    "v_cvt_pk_bf16_f32 v24, v24, v25\n"
    "v_mfma_f32_16x16x32_bf16 v[16:19], a[228:231], a[20:23], v[16:19]\n"
    "buffer_load_dwordx4 v3, s[36:39], s57 offen offset:0 lds\n"
    "v_mfma_f32_16x16x32_bf16 v[16:19], a[232:235], a[24:27], v[16:19]\n"
    "v_exp_f32 v26, v26\n"
    "v_accvgpr_write_b32 a148, v24\n"
    "v_mfma_f32_16x16x32_bf16 v[16:19], a[236:239], a[28:31], v[16:19]\n"
    "ds_read_b128 a[200:203], v0 offset:50176\n"
    "v_mul_f32 v27, s52, v27\n"
    "v_mfma_f32_16x16x32_bf16 v[20:23], a[236:239], a[60:63], 0\n"
    "v_exp_f32 v27, v27\n"
    "v_mul_f32 v180, s52, v180\n"
    "v_mfma_f32_16x16x32_bf16 v[20:23], a[232:235], a[56:59], v[20:23]\n"
    "buffer_store_dwordx4 v[104:107], v6, s[48:51], s68 offen nt sc1\n"
    "v_mfma_f32_16x16x32_bf16 v[20:23], a[228:231], a[52:55], v[20:23]\n"
    "ds_read_b128 a[204:207], v0 offset:50688\n"
    "v_cvt_pk_bf16_f32 v26, v26, v27\n"
    "v_mul_f32 v181, s52, v181\n"
    "v_mfma_f32_16x16x32_bf16 v[20:23], a[224:227], a[48:51], v[20:23]\n"
    "v_accvgpr_write_b32 a149, v26\n"
    "v_mul_f32 v182, s52, v182\n"
    "v_mfma_f32_16x16x32_bf16 v[20:23], a[220:223], a[44:47], v[20:23]\n"
    "v_exp_f32 v180, v180\n"
    "v_mfma_f32_16x16x32_bf16 v[20:23], a[216:219], a[40:43], v[20:23]\n"
    "ds_read_b128 a[160:163], v0 offset:51200\n"
    "v_mul_f32 v183, s52, v183\n"
    "v_mfma_f32_16x16x32_bf16 v[20:23], a[212:215], a[36:39], v[20:23]\n"
    "v_exp_f32 v181, v181\n"
    "v_mfma_f32_16x16x32_bf16 v[20:23], a[208:211], a[32:35], v[20:23]\n"
    "v_cvt_pk_bf16_f32 v180, v180, v181\n"
    "v_mul_f32 v16, s52, v16\n"
    "v_mfma_f32_16x16x32_bf16 v[24:27], a[208:211], a[64:67], 0\n"
    "ds_read_b128 a[164:167], v0 offset:51712\n"
    "v_mul_f32 v17, s52, v17\n"
    "v_mfma_f32_16x16x32_bf16 v[24:27], a[212:215], a[68:71], v[24:27]\n"
    "v_exp_f32 v182, v182\n"
    "v_mfma_f32_16x16x32_bf16 v[24:27], a[216:219], a[72:75], v[24:27]\n"
    "s_add_u32 m0, s56, 0x8380\n"
    "v_exp_f32 v183, v183\n"
    "v_mfma_f32_16x16x32_bf16 v[24:27], a[220:223], a[76:79], v[24:27]\n"
    "ds_read_b128 a[168:171], v0 offset:52224\n"
    "v_cvt_pk_bf16_f32 v181, v182, v183\n"
    "v_mfma_f32_16x16x32_bf16 v[24:27], a[224:227], a[80:83], v[24:27]\n"
    "buffer_load_dwordx4 v3, s[36:39], s57 offen offset:128 lds\n"
    "v_mfma_f32_16x16x32_bf16 v[24:27], a[228:231], a[84:87], v[24:27]\n"
    "v_exp_f32 v16, v16\n"
    "v_mfma_f32_16x16x32_bf16 v[24:27], a[232:235], a[88:91], v[24:27]\n"
    "ds_read_b128 a[172:175], v0 offset:52736\n"
    "v_mul_f32 v18, s52, v18\n"
    "v_mfma_f32_16x16x32_bf16 v[24:27], a[236:239], a[92:95], v[24:27]\n"
    "v_exp_f32 v17, v17\n"
    "v_mfma_f32_16x16x32_bf16 v[28:31], a[236:239], a[124:127], 0\n"
    "v_cvt_pk_bf16_f32 v16, v16, v17\n"
    "v_mul_f32 v19, s52, v19\n"
    "v_mfma_f32_16x16x32_bf16 v[28:31], a[232:235], a[120:123], v[28:31]\n"
    "v_exp_f32 v18, v18\n"
    "v_accvgpr_write_b32 a146, v16\n"
    "v_mfma_f32_16x16x32_bf16 v[28:31], a[228:231], a[116:119], v[28:31]\n"
    "v_exp_f32 v19, v19\n"
    "v_mul_f32 v20, s52, v20\n"
    "v_mfma_f32_16x16x32_bf16 v[28:31], a[224:227], a[112:115], v[28:31]\n"
    "v_cvt_pk_bf16_f32 v18, v18, v19\n"
    "v_mul_f32 v21, s52, v21\n"
    "v_mfma_f32_16x16x32_bf16 v[28:31], a[220:223], a[108:111], v[28:31]\n"
    "v_accvgpr_write_b32 a147, v18\n"
    "v_mul_f32 v22, s52, v22\n"
    "v_mfma_f32_16x16x32_bf16 v[28:31], a[216:219], a[104:107], v[28:31]\n"
    "v_exp_f32 v20, v20\n"
    "v_mfma_f32_16x16x32_bf16 v[28:31], a[212:215], a[100:103], v[28:31]\n"
    "v_exp_f32 v21, v21\n"
    "v_mfma_f32_16x16x32_bf16 v[28:31], a[208:211], a[96:99], v[28:31]\n"
    "v_cvt_pk_bf16_f32 v178, v20, v21\n"
    "v_mul_f32 v23, s52, v23\n"
    "v_mfma_f32_16x16x32_bf16 a[248:251], v[12:15], a[132:135], 0\n"
    "s_add_u32 m0, s56, 0x8700\n"
    "v_exp_f32 v22, v22\n"
    "v_mfma_f32_16x16x32_bf16 a[252:255], v[12:15], v[164:167], 0\n"
    "s_waitcnt lgkmcnt(0)\n"
    "v_exp_f32 v23, v23\n"
    "v_mfma_f32_16x16x32_bf16 v[16:19], a[192:195], a[0:3], 0\n"
    "buffer_load_dwordx4 v3, s[36:39], s57 offen offset:256 lds\n"
    "v_mfma_f32_16x16x32_bf16 v[16:19], a[196:199], a[4:7], v[16:19]\n"
    "ds_read_b128 a[208:211], v0 offset:57344\n"
    "v_cvt_pk_bf16_f32 v179, v22, v23\n"
    "v_mul_f32 v24, s52, v24\n"
    "v_mfma_f32_16x16x32_bf16 v[16:19], a[200:203], a[8:11], v[16:19]\n"
    "s_add_u32 s54, s72, s91\n"
    "v_exp_f32 v24, v24\n"
    "v_mul_f32 v25, s52, v25\n"
    "v_mfma_f32_16x16x32_bf16 v[16:19], a[204:207], a[12:15], v[16:19]\n"
    "v_exp_f32 v25, v25\n"
    "v_mfma_f32_16x16x32_bf16 v[16:19], a[160:163], a[16:19], v[16:19]\n"
    "ds_read_b128 a[212:215], v0 offset:57856\n"
    "v_mul_f32 v26, s52, v26\n"
    "v_cvt_pk_bf16_f32 v24, v24, v25\n"
    "v_mfma_f32_16x16x32_bf16 v[16:19], a[164:167], a[20:23], v[16:19]\n"
    "buffer_store_dwordx4 v[136:139], v6, s[48:51], s69 offen nt sc1\n"
    "v_mfma_f32_16x16x32_bf16 v[16:19], a[168:171], a[24:27], v[16:19]\n"
    "v_exp_f32 v26, v26\n"
    "v_accvgpr_write_b32 a150, v24\n"
    "v_mfma_f32_16x16x32_bf16 v[16:19], a[172:175], a[28:31], v[16:19]\n"
    "ds_read_b128 a[216:219], v0 offset:58368\n"
    "v_mul_f32 v27, s52, v27\n"
    "v_mfma_f32_16x16x32_bf16 v[184:187], a[172:175], a[60:63], 0\n"
    "v_exp_f32 v27, v27\n"
    "v_mul_f32 v28, s52, v28\n"
    "v_mfma_f32_16x16x32_bf16 v[184:187], a[168:171], a[56:59], v[184:187]\n"
    "v_cvt_pk_bf16_f32 v26, v26, v27\n"
    "v_mul_f32 v29, s52, v29\n"
    "v_mfma_f32_16x16x32_bf16 v[184:187], a[164:167], a[52:55], v[184:187]\n"
    "ds_read_b128 a[220:223], v0 offset:58880\n"
    "v_accvgpr_write_b32 a151, v26\n"
    "v_mfma_f32_16x16x32_bf16 v[184:187], a[160:163], a[48:51], v[184:187]\n"
    "v_exp_f32 v28, v28\n"
    "v_mfma_f32_16x16x32_bf16 v[184:187], a[204:207], a[44:47], v[184:187]\n"
    "v_exp_f32 v29, v29\n"
    "v_mul_f32 v30, s52, v30\n"
    "v_mfma_f32_16x16x32_bf16 v[184:187], a[200:203], a[40:43], v[184:187]\n"
    "ds_read_b128 a[224:227], v0 offset:59392\n"
    "v_cvt_pk_bf16_f32 v182, v28, v29\n"
    "v_mfma_f32_16x16x32_bf16 v[184:187], a[196:199], a[36:39], v[184:187]\n"
    "s_add_u32 m0, s56, 0x8a80\n"
    "v_exp_f32 v30, v30\n"
    "v_mul_f32 v31, s52, v31\n"
    "v_mfma_f32_16x16x32_bf16 v[184:187], a[192:195], a[32:35], v[184:187]\n"
    "buffer_load_dwordx4 v3, s[36:39], s57 offen offset:384 lds\n"
    "v_mfma_f32_16x16x32_bf16 v[24:27], a[192:195], a[64:67], 0\n"
    "ds_read_b128 a[228:231], v0 offset:59904\n"
    "v_mul_f32 v16, s52, v16\n"
    "v_mfma_f32_16x16x32_bf16 v[24:27], a[196:199], a[68:71], v[24:27]\n"
    "v_exp_f32 v31, v31\n"
    "v_mfma_f32_16x16x32_bf16 v[24:27], a[200:203], a[72:75], v[24:27]\n"
    "v_cvt_pk_bf16_f32 v183, v30, v31\n"
    "v_mul_f32 v17, s52, v17\n"
    "v_mfma_f32_16x16x32_bf16 v[24:27], a[204:207], a[76:79], v[24:27]\n"
    "ds_read_b128 a[232:235], v0 offset:60416\n"
    "v_mul_f32 v18, s52, v18\n"
    "v_mfma_f32_16x16x32_bf16 v[24:27], a[160:163], a[80:83], v[24:27]\n"
    "v_exp_f32 v16, v16\n"
    "v_mfma_f32_16x16x32_bf16 v[24:27], a[164:167], a[84:87], v[24:27]\n"
    "v_exp_f32 v17, v17\n"
    "v_mfma_f32_16x16x32_bf16 v[24:27], a[168:171], a[88:91], v[24:27]\n"
    "ds_read_b128 a[236:239], v0 offset:60928\n"
    "v_cvt_pk_bf16_f32 v16, v16, v17\n"
    "v_mfma_f32_16x16x32_bf16 v[24:27], a[172:175], a[92:95], v[24:27]\n"
    "v_exp_f32 v18, v18\n"
    "v_accvgpr_write_b32 a152, v16\n"
    "v_mfma_f32_16x16x32_bf16 v[188:191], a[172:175], a[124:127], 0\n"
    "v_mul_f32 v19, s52, v19\n"
    "v_mul_f32 v184, s52, v184\n"
    "v_mfma_f32_16x16x32_bf16 v[188:191], a[168:171], a[120:123], v[188:191]\n"
    "v_exp_f32 v19, v19\n"
    "v_mfma_f32_16x16x32_bf16 v[188:191], a[164:167], a[116:119], v[188:191]\n"
    "v_cvt_pk_bf16_f32 v18, v18, v19\n"
    "v_mul_f32 v185, s52, v185\n"
    "v_mfma_f32_16x16x32_bf16 v[188:191], a[160:163], a[112:115], v[188:191]\n"
    "v_accvgpr_write_b32 a153, v18\n"
    "v_mul_f32 v186, s52, v186\n"
    "v_mfma_f32_16x16x32_bf16 v[188:191], a[204:207], a[108:111], v[188:191]\n"
    "v_exp_f32 v184, v184\n"
    "v_mul_f32 v143, v11, v143\n"
    "v_mul_f32 v156, v11, v156\n"
    "v_mul_f32 v157, v11, v157\n"
    "v_mul_f32 v158, v11, v158\n"
    "v_mul_f32 v159, v11, v159\n"
    "v_mfma_f32_16x16x32_bf16 v[188:191], a[200:203], a[104:107], v[188:191]\n"
    "v_exp_f32 v185, v185\n"
    "v_cvt_pk_bf16_f32 v140, v140, v141\n"
    "v_mfma_f32_16x16x32_bf16 v[188:191], a[196:199], a[100:103], v[188:191]\n"
    "v_cvt_pk_bf16_f32 v184, v184, v185\n"
    "v_mul_f32 v187, s52, v187\n"
    "v_cvt_pk_bf16_f32 v141, v142, v143\n"
    "v_mfma_f32_16x16x32_bf16 v[188:191], a[192:195], a[96:99], v[188:191]\n"
    "v_exp_f32 v186, v186\n"
    "v_cvt_pk_bf16_f32 v110, v124, v125\n"
    "v_cvt_pk_bf16_f32 v111, v126, v127\n"
    "v_cvt_pk_bf16_f32 v142, v156, v157\n"
    "v_cvt_pk_bf16_f32 v143, v158, v159\n"
    "v_mfma_f32_16x16x32_bf16 a[240:243], v[12:15], a[136:139], a[240:243]\n"
    "v_exp_f32 v187, v187\n"
    "v_mfma_f32_16x16x32_bf16 a[244:247], v[12:15], v[168:171], a[244:247]\n"
    "s_waitcnt vmcnt(12) lgkmcnt(0)\n"
    "v_cvt_pk_bf16_f32 v185, v186, v187\n"
    "v_mul_f32 v24, s52, v24\n"
    "v_permlane16_swap_b32 v108, v110\n"
    "v_permlane16_swap_b32 v140, v142\n"
    "v_mfma_f32_16x16x32_bf16 v[16:19], a[208:211], a[0:3], 0\n"
    "s_barrier\n"
    "v_exp_f32 v24, v24\n"
    "v_permlane16_swap_b32 v109, v111\n"
    "v_permlane16_swap_b32 v141, v143\n"
    "v_mfma_f32_16x16x32_bf16 v[16:19], a[212:215], a[4:7], v[16:19]\n"
    "ds_read_b128 a[192:195], v0 offset:0\n"
    "v_mul_f32 v25, s52, v25\n"
    "v_mfma_f32_16x16x32_bf16 v[16:19], a[216:219], a[8:11], v[16:19]\n"
    "s_add_u32 s57, s53, 0x1c000\n"
    "v_exp_f32 v25, v25\n"
    "v_mov_b32 v124, v108\n"
    "v_mov_b32 v125, v109\n"
    "v_mov_b32 v126, v110\n"
    "v_mov_b32 v127, v111\n"
    "v_mfma_f32_16x16x32_bf16 v[16:19], a[220:223], a[12:15], v[16:19]\n"
    "s_add_u32 m0, s56, 0xc000\n"
    "v_mul_f32 v26, s52, v26\n"
    "v_cvt_pk_bf16_f32 v24, v24, v25\n"
    "v_mfma_f32_16x16x32_bf16 v[16:19], a[224:227], a[16:19], v[16:19]\n"
    "ds_read_b128 a[196:199], v0 offset:512\n"
    "v_accvgpr_write_b32 a156, v24\n"
    "v_mfma_f32_16x16x32_bf16 v[16:19], a[228:231], a[20:23], v[16:19]\n"
    "buffer_load_dwordx4 v3, s[36:39], s57 offen offset:0 lds\n"
    "v_mov_b32_dpp v108, v140 row_ror:8 row_mask:0xf bank_mask:0xc\n"
    "v_mov_b32_dpp v109, v141 row_ror:8 row_mask:0xf bank_mask:0xc\n"
    "v_mov_b32_dpp v110, v142 row_ror:8 row_mask:0xf bank_mask:0xc\n"
    "v_mov_b32_dpp v111, v143 row_ror:8 row_mask:0xf bank_mask:0xc\n"
    "v_mfma_f32_16x16x32_bf16 v[16:19], a[232:235], a[24:27], v[16:19]\n"
    "v_exp_f32 v26, v26\n"
    "v_mul_f32 v27, s52, v27\n"
    "v_mfma_f32_16x16x32_bf16 v[16:19], a[236:239], a[28:31], v[16:19]\n"
    "ds_read_b128 a[200:203], v0 offset:1024\n"
    "v_mul_f32 v188, s52, v188\n"
    "v_mfma_f32_16x16x32_bf16 v[20:23], a[236:239], a[60:63], 0\n"
    "v_exp_f32 v27, v27\n"
    "v_mfma_f32_16x16x32_bf16 v[20:23], a[232:235], a[56:59], v[20:23]\n"
    "buffer_store_dwordx4 v[108:111], v6, s[48:51], s70 offen nt sc1\n"
    "v_mfma_f32_16x16x32_bf16 v[20:23], a[228:231], a[52:55], v[20:23]\n"
    "ds_read_b128 a[204:207], v0 offset:1536\n"
    "v_cvt_pk_bf16_f32 v26, v26, v27\n"
    "v_mul_f32 v189, s52, v189\n"
    "v_mfma_f32_16x16x32_bf16 v[20:23], a[224:227], a[48:51], v[20:23]\n"
    "v_accvgpr_write_b32 a157, v26\n"
    "v_mul_f32 v190, s52, v190\n"
    "v_mfma_f32_16x16x32_bf16 v[20:23], a[220:223], a[44:47], v[20:23]\n"
    "v_exp_f32 v188, v188\n"
    "v_mfma_f32_16x16x32_bf16 v[20:23], a[216:219], a[40:43], v[20:23]\n"
    "ds_read_b128 a[160:163], v0 offset:2048\n"
    "v_mul_f32 v191, s52, v191\n"
    "v_mfma_f32_16x16x32_bf16 v[20:23], a[212:215], a[36:39], v[20:23]\n"
    "v_exp_f32 v189, v189\n"
    "v_mfma_f32_16x16x32_bf16 v[20:23], a[208:211], a[32:35], v[20:23]\n"
    "v_cvt_pk_bf16_f32 v188, v188, v189\n"
    "v_mul_f32 v16, s52, v16\n"
    "v_mfma_f32_16x16x32_bf16 v[24:27], a[208:211], a[64:67], 0\n"
    "ds_read_b128 a[164:167], v0 offset:2560\n"
    "v_mul_f32 v17, s52, v17\n"
    "v_mfma_f32_16x16x32_bf16 v[24:27], a[212:215], a[68:71], v[24:27]\n"
    "v_exp_f32 v190, v190\n"
    "v_mfma_f32_16x16x32_bf16 v[24:27], a[216:219], a[72:75], v[24:27]\n"
    "s_add_u32 m0, s56, 0xc380\n"
    "v_exp_f32 v191, v191\n"
    "v_mfma_f32_16x16x32_bf16 v[24:27], a[220:223], a[76:79], v[24:27]\n"
    "ds_read_b128 a[168:171], v0 offset:3072\n"
    "v_cvt_pk_bf16_f32 v189, v190, v191\n"
    "v_mfma_f32_16x16x32_bf16 v[24:27], a[224:227], a[80:83], v[24:27]\n"
    "buffer_load_dwordx4 v3, s[36:39], s57 offen offset:128 lds\n"
    "v_mfma_f32_16x16x32_bf16 v[24:27], a[228:231], a[84:87], v[24:27]\n"
    "v_exp_f32 v16, v16\n"
    "v_mfma_f32_16x16x32_bf16 v[24:27], a[232:235], a[88:91], v[24:27]\n"
    "ds_read_b128 a[172:175], v0 offset:3584\n"
    "v_mul_f32 v18, s52, v18\n"
    "v_mfma_f32_16x16x32_bf16 v[24:27], a[236:239], a[92:95], v[24:27]\n"
    "v_exp_f32 v17, v17\n"
    "v_mfma_f32_16x16x32_bf16 v[28:31], a[236:239], a[124:127], 0\n"
    "v_cvt_pk_bf16_f32 v16, v16, v17\n"
    "v_mul_f32 v19, s52, v19\n"
    "v_mfma_f32_16x16x32_bf16 v[28:31], a[232:235], a[120:123], v[28:31]\n"
    "v_exp_f32 v18, v18\n"
    "v_accvgpr_write_b32 a154, v16\n"
    "v_mfma_f32_16x16x32_bf16 v[28:31], a[228:231], a[116:119], v[28:31]\n"
    "v_exp_f32 v19, v19\n"
    "v_mul_f32 v20, s52, v20\n"
    "v_mfma_f32_16x16x32_bf16 v[28:31], a[224:227], a[112:115], v[28:31]\n"
    "v_cvt_pk_bf16_f32 v18, v18, v19\n"
    "v_mul_f32 v21, s52, v21\n"
    "v_mfma_f32_16x16x32_bf16 v[28:31], a[220:223], a[108:111], v[28:31]\n"
    "v_accvgpr_write_b32 a155, v18\n"
    "v_mul_f32 v22, s52, v22\n"
    "v_mfma_f32_16x16x32_bf16 v[28:31], a[216:219], a[104:107], v[28:31]\n"
    "v_exp_f32 v20, v20\n"
    "v_mfma_f32_16x16x32_bf16 v[28:31], a[212:215], a[100:103], v[28:31]\n"
    "v_exp_f32 v21, v21\n"
    "v_mfma_f32_16x16x32_bf16 v[28:31], a[208:211], a[96:99], v[28:31]\n"
    "v_cvt_pk_bf16_f32 v186, v20, v21\n"
    "v_mul_f32 v23, s52, v23\n"
    "v_mfma_f32_16x16x32_bf16 a[248:251], v[12:15], a[140:143], a[248:251]\n"
    "s_add_u32 m0, s56, 0xc700\n"
    "v_exp_f32 v22, v22\n"
    "v_mfma_f32_16x16x32_bf16 a[252:255], v[12:15], v[172:175], a[252:255]\n"
    "s_waitcnt lgkmcnt(0)\n"
    "v_exp_f32 v23, v23\n"
    "v_mfma_f32_16x16x32_bf16 v[32:35], a[192:195], a[0:3], 0\n"
    "buffer_load_dwordx4 v3, s[36:39], s57 offen offset:256 lds\n"
    "v_mfma_f32_16x16x32_bf16 v[32:35], a[196:199], a[4:7], v[32:35]\n"
    "ds_read_b128 a[208:211], v0 offset:8192\n"
    "v_cvt_pk_bf16_f32 v187, v22, v23\n"
    "v_mul_f32 v24, s52, v24\n"
    "v_mfma_f32_16x16x32_bf16 v[32:35], a[200:203], a[8:11], v[32:35]\n"
    "s_lshl_b32 s85, s93, 2\n"
    "v_exp_f32 v24, v24\n"
    "v_mul_f32 v25, s52, v25\n"
    "v_mfma_f32_16x16x32_bf16 v[32:35], a[204:207], a[12:15], v[32:35]\n"
    "s_add_u32 s60, s77, s85\n"
    "v_exp_f32 v25, v25\n"
    "v_mov_b32_dpp v140, v124 row_ror:8 row_mask:0xf bank_mask:0x3\n"
    "v_mov_b32_dpp v141, v125 row_ror:8 row_mask:0xf bank_mask:0x3\n"
    "v_mov_b32_dpp v142, v126 row_ror:8 row_mask:0xf bank_mask:0x3\n"
    "v_mov_b32_dpp v143, v127 row_ror:8 row_mask:0xf bank_mask:0x3\n"
    "v_mfma_f32_16x16x32_bf16 v[32:35], a[160:163], a[16:19], v[32:35]\n"
    "ds_read_b128 a[212:215], v0 offset:8704\n"
    "v_mul_f32 v26, s52, v26\n"
    "v_cvt_pk_bf16_f32 v24, v24, v25\n"
    "v_mfma_f32_16x16x32_bf16 v[32:35], a[164:167], a[20:23], v[32:35]\n"
    "buffer_store_dwordx4 v[140:143], v6, s[48:51], s71 offen nt sc1\n"
    "v_mfma_f32_16x16x32_bf16 v[32:35], a[168:171], a[24:27], v[32:35]\n"
    "s_add_u32 s61, s60, 0x2000\n"
    "v_exp_f32 v26, v26\n"
    "v_accvgpr_write_b32 a158, v24\n"
    "v_mfma_f32_16x16x32_bf16 v[32:35], a[172:175], a[28:31], v[32:35]\n"
    "ds_read_b128 a[216:219], v0 offset:9216\n"
    "v_mul_f32 v27, s52, v27\n"
    "v_mfma_f32_16x16x32_bf16 v[36:39], a[172:175], a[60:63], 0\n"
    "s_add_u32 s62, s60, 0x4000\n"
    "v_exp_f32 v27, v27\n"
    "v_mul_f32 v28, s52, v28\n"
    "v_mfma_f32_16x16x32_bf16 v[36:39], a[168:171], a[56:59], v[36:39]\n"
    "s_add_u32 s63, s60, 0x6000\n"
    "v_cvt_pk_bf16_f32 v26, v26, v27\n"
    "v_mul_f32 v29, s52, v29\n"
    "v_mfma_f32_16x16x32_bf16 v[36:39], a[164:167], a[52:55], v[36:39]\n"
    "ds_read_b128 a[220:223], v0 offset:9728\n"
    "v_accvgpr_write_b32 a159, v26\n"
    "v_mfma_f32_16x16x32_bf16 v[36:39], a[160:163], a[48:51], v[36:39]\n"
    "v_exp_f32 v28, v28\n"
    "v_mfma_f32_16x16x32_bf16 v[36:39], a[204:207], a[44:47], v[36:39]\n"
    "v_exp_f32 v29, v29\n"
    "v_mul_f32 v30, s52, v30\n"
    "v_mfma_f32_16x16x32_bf16 v[36:39], a[200:203], a[40:43], v[36:39]\n"
    "ds_read_b128 a[224:227], v0 offset:10240\n"
    "v_cvt_pk_bf16_f32 v190, v28, v29\n"
    "v_mfma_f32_16x16x32_bf16 v[36:39], a[196:199], a[36:39], v[36:39]\n"
    "s_add_u32 m0, s56, 0xca80\n"
    "v_exp_f32 v30, v30\n"
    "v_mul_f32 v31, s52, v31\n"
    "v_mfma_f32_16x16x32_bf16 v[36:39], a[192:195], a[32:35], v[36:39]\n"
    "buffer_load_dwordx4 v3, s[36:39], s57 offen offset:384 lds\n"
    "v_mfma_f32_16x16x32_bf16 v[40:43], a[192:195], a[64:67], 0\n"
    "ds_read_b128 a[228:231], v0 offset:10752\n"
    "v_mul_f32 v32, s52, v32\n"
    "v_mfma_f32_16x16x32_bf16 v[40:43], a[196:199], a[68:71], v[40:43]\n"
    "v_exp_f32 v31, v31\n"
    "v_mfma_f32_16x16x32_bf16 v[40:43], a[200:203], a[72:75], v[40:43]\n"
    "v_cvt_pk_bf16_f32 v191, v30, v31\n"
    "v_mul_f32 v33, s52, v33\n"
    "v_mfma_f32_16x16x32_bf16 v[40:43], a[204:207], a[76:79], v[40:43]\n"
    "ds_read_b128 a[232:235], v0 offset:11264\n"
    "v_mul_f32 v34, s52, v34\n"
    "v_mfma_f32_16x16x32_bf16 v[40:43], a[160:163], a[80:83], v[40:43]\n"
    "v_exp_f32 v32, v32\n"
    "v_mfma_f32_16x16x32_bf16 v[40:43], a[164:167], a[84:87], v[40:43]\n"
    "v_exp_f32 v33, v33\n"
    "v_mfma_f32_16x16x32_bf16 v[40:43], a[168:171], a[88:91], v[40:43]\n"
    "ds_read_b128 a[236:239], v0 offset:11776\n"
    "v_cvt_pk_bf16_f32 v32, v32, v33\n"
    "v_mfma_f32_16x16x32_bf16 v[40:43], a[172:175], a[92:95], v[40:43]\n"
    "v_exp_f32 v34, v34\n"
    "v_mfma_f32_16x16x32_bf16 v[44:47], a[172:175], a[124:127], 0\n"
    "v_mul_f32 v35, s52, v35\n"
    "v_mul_f32 v36, s52, v36\n"
    "v_mfma_f32_16x16x32_bf16 v[44:47], a[168:171], a[120:123], v[44:47]\n"
    "v_exp_f32 v35, v35\n"
    "v_mfma_f32_16x16x32_bf16 v[44:47], a[164:167], a[116:119], v[44:47]\n"
    "v_cvt_pk_bf16_f32 v33, v34, v35\n"
    "v_mul_f32 v37, s52, v37\n"
    "v_mfma_f32_16x16x32_bf16 v[44:47], a[160:163], a[112:115], v[44:47]\n"
    "v_exp_f32 v36, v36\n"
    "v_mfma_f32_16x16x32_bf16 v[44:47], a[204:207], a[108:111], v[44:47]\n"
    "v_exp_f32 v37, v37\n"
    "v_mfma_f32_16x16x32_bf16 v[44:47], a[200:203], a[104:107], v[44:47]\n"
    "v_mul_f32 v38, s52, v38\n"
    "v_cvt_pk_bf16_f32 v36, v36, v37\n"
    "v_mfma_f32_16x16x32_bf16 v[44:47], a[196:199], a[100:103], v[44:47]\n"
    "v_exp_f32 v38, v38\n"
    "v_mfma_f32_16x16x32_bf16 v[44:47], a[192:195], a[96:99], v[44:47]\n"
    "v_mul_f32 v39, s52, v39\n"
    "v_mul_f32 v40, s52, v40\n"
    "v_mfma_f32_16x16x32_bf16 a[240:243], v[12:15], a[144:147], a[240:243]\n"
    "v_exp_f32 v39, v39\n"
    "v_mfma_f32_16x16x32_bf16 a[244:247], v[12:15], v[176:179], a[244:247]\n"
    "s_waitcnt vmcnt(12) lgkmcnt(0)\n"
    "v_cvt_pk_bf16_f32 v37, v38, v39\n"
    "v_mul_f32 v41, s52, v41\n"
    "v_mfma_f32_16x16x32_bf16 v[16:19], a[208:211], a[0:3], 0\n"
    "s_barrier\n"
    "v_exp_f32 v40, v40\n"
    "v_mfma_f32_16x16x32_bf16 v[16:19], a[212:215], a[4:7], v[16:19]\n"
    "ds_read_b128 a[192:195], v0 offset:16384\n"
    "v_mul_f32 v42, s52, v42\n"
    "v_mfma_f32_16x16x32_bf16 v[16:19], a[216:219], a[8:11], v[16:19]\n"
    "s_add_u32 s57, s53, 0x20000\n"
    "v_exp_f32 v41, v41\n"
    "v_mfma_f32_16x16x32_bf16 v[16:19], a[220:223], a[12:15], v[16:19]\n"
    "s_add_u32 m0, s56, 0x0\n"
    "v_cvt_pk_bf16_f32 v40, v40, v41\n"
    "v_mul_f32 v43, s52, v43\n"
    "v_mfma_f32_16x16x32_bf16 v[16:19], a[224:227], a[16:19], v[16:19]\n"
    "ds_read_b128 a[196:199], v0 offset:16896\n"
    "v_mul_f32 v44, s52, v44\n"
    "v_mfma_f32_16x16x32_bf16 v[16:19], a[228:231], a[20:23], v[16:19]\n"
    "buffer_load_dwordx4 v3, s[36:39], s57 offen offset:0 lds\n"
    "v_mfma_f32_16x16x32_bf16 v[16:19], a[232:235], a[24:27], v[16:19]\n"
    "v_exp_f32 v42, v42\n"
    "v_mfma_f32_16x16x32_bf16 v[16:19], a[236:239], a[28:31], v[16:19]\n"
    "ds_read_b128 a[200:203], v0 offset:17408\n"
    "v_mul_f32 v45, s52, v45\n"
    "v_mfma_f32_16x16x32_bf16 v[20:23], a[236:239], a[60:63], 0\n"
    "v_exp_f32 v43, v43\n"
    "v_mfma_f32_16x16x32_bf16 v[20:23], a[232:235], a[56:59], v[20:23]\n"
    "v_cvt_pk_bf16_f32 v41, v42, v43\n"
    "v_mul_f32 v46, s52, v46\n"
    "v_mfma_f32_16x16x32_bf16 v[20:23], a[228:231], a[52:55], v[20:23]\n"
    "ds_read_b128 a[204:207], v0 offset:17920\n"
    "v_mul_f32 v47, s52, v47\n"
    "v_mfma_f32_16x16x32_bf16 v[20:23], a[224:227], a[48:51], v[20:23]\n"
    "v_exp_f32 v44, v44\n"
    "v_mfma_f32_16x16x32_bf16 v[20:23], a[220:223], a[44:47], v[20:23]\n"
    "s_add_u32 m0, s56, 0x380\n"
    "v_exp_f32 v45, v45\n"
    "v_mfma_f32_16x16x32_bf16 v[20:23], a[216:219], a[40:43], v[20:23]\n"
    "ds_read_b128 a[160:163], v0 offset:18432\n"
    "v_cvt_pk_bf16_f32 v44, v44, v45\n"
    "v_mfma_f32_16x16x32_bf16 v[20:23], a[212:215], a[36:39], v[20:23]\n"
    "buffer_load_dwordx4 v3, s[36:39], s57 offen offset:128 lds\n"
    "v_mfma_f32_16x16x32_bf16 v[20:23], a[208:211], a[32:35], v[20:23]\n"
    "v_exp_f32 v46, v46\n"
    "v_mfma_f32_16x16x32_bf16 v[24:27], a[208:211], a[64:67], 0\n"
    "ds_read_b128 a[164:167], v0 offset:18944\n"
    "v_mul_f32 v16, s52, v16\n"
    "v_mfma_f32_16x16x32_bf16 v[24:27], a[212:215], a[68:71], v[24:27]\n"
    "v_exp_f32 v47, v47\n"
    "v_mfma_f32_16x16x32_bf16 v[24:27], a[216:219], a[72:75], v[24:27]\n"
    "v_cvt_pk_bf16_f32 v45, v46, v47\n"
    "v_mul_f32 v17, s52, v17\n"
    "v_mfma_f32_16x16x32_bf16 v[24:27], a[220:223], a[76:79], v[24:27]\n"
    "ds_read_b128 a[168:171], v0 offset:19456\n"
    "v_mul_f32 v18, s52, v18\n"
    "v_mfma_f32_16x16x32_bf16 v[24:27], a[224:227], a[80:83], v[24:27]\n"
    "v_exp_f32 v16, v16\n"
    "v_mfma_f32_16x16x32_bf16 v[24:27], a[228:231], a[84:87], v[24:27]\n"
    "v_exp_f32 v17, v17\n"
    "v_mfma_f32_16x16x32_bf16 v[24:27], a[232:235], a[88:91], v[24:27]\n"
    "ds_read_b128 a[172:175], v0 offset:19968\n"
    "v_cvt_pk_bf16_f32 v34, v16, v17\n"
    "v_mfma_f32_16x16x32_bf16 v[24:27], a[236:239], a[92:95], v[24:27]\n"
    "s_add_u32 m0, s56, 0x700\n"
    "v_exp_f32 v18, v18\n"
    "v_mfma_f32_16x16x32_bf16 v[28:31], a[236:239], a[124:127], 0\n"
    "buffer_load_dwordx4 v3, s[36:39], s57 offen offset:256 lds\n"
    "v_mfma_f32_16x16x32_bf16 v[28:31], a[232:235], a[120:123], v[28:31]\n"
    "v_mul_f32 v19, s52, v19\n"
    "v_mul_f32 v20, s52, v20\n"
    "v_mfma_f32_16x16x32_bf16 v[28:31], a[228:231], a[116:119], v[28:31]\n"
    "v_exp_f32 v19, v19\n"
    "v_mfma_f32_16x16x32_bf16 v[28:31], a[224:227], a[112:115], v[28:31]\n"
    "v_cvt_pk_bf16_f32 v35, v18, v19\n"
    "v_mul_f32 v21, s52, v21\n"
    "v_mfma_f32_16x16x32_bf16 v[28:31], a[220:223], a[108:111], v[28:31]\n"
    "v_exp_f32 v20, v20\n"
    "v_mfma_f32_16x16x32_bf16 v[28:31], a[216:219], a[104:107], v[28:31]\n"
    "v_exp_f32 v21, v21\n"
    "v_mfma_f32_16x16x32_bf16 v[28:31], a[212:215], a[100:103], v[28:31]\n"
    "v_mul_f32 v22, s52, v22\n"
    "v_cvt_pk_bf16_f32 v38, v20, v21\n"
    "v_mfma_f32_16x16x32_bf16 v[28:31], a[208:211], a[96:99], v[28:31]\n"
    "v_exp_f32 v22, v22\n"
    "v_mfma_f32_16x16x32_bf16 a[248:251], v[12:15], a[148:151], a[248:251]\n"
    "s_add_u32 m0, s56, 0xa80\n"
    "v_mul_f32 v23, s52, v23\n"
    "v_mul_f32 v24, s52, v24\n"
    "v_mfma_f32_16x16x32_bf16 a[252:255], v[12:15], v[180:183], a[252:255]\n"
    "s_waitcnt lgkmcnt(0)\n"
    "v_exp_f32 v23, v23\n"
    "v_mfma_f32_16x16x32_bf16 v[48:51], a[192:195], a[0:3], 0\n"
    "buffer_load_dwordx4 v3, s[36:39], s57 offen offset:384 lds\n"
    "v_mfma_f32_16x16x32_bf16 v[48:51], a[196:199], a[4:7], v[48:51]\n"
    "ds_read_b128 a[208:211], v0 offset:24576\n"
    "v_cvt_pk_bf16_f32 v39, v22, v23\n"
    "v_mfma_f32_16x16x32_bf16 v[48:51], a[200:203], a[8:11], v[48:51]\n"
    "v_exp_f32 v24, v24\n"
    "v_mfma_f32_16x16x32_bf16 v[48:51], a[204:207], a[12:15], v[48:51]\n"
    "v_mul_f32 v25, s52, v25\n"
    "v_mul_f32 v26, s52, v26\n"
    "v_mfma_f32_16x16x32_bf16 v[48:51], a[160:163], a[16:19], v[48:51]\n"
    "ds_read_b128 a[212:215], v0 offset:25088\n"
    "v_mul_f32 v27, s52, v27\n"
    "v_mfma_f32_16x16x32_bf16 v[48:51], a[164:167], a[20:23], v[48:51]\n"
    "v_exp_f32 v25, v25\n"
    "v_mfma_f32_16x16x32_bf16 v[48:51], a[168:171], a[24:27], v[48:51]\n"
    "v_cvt_pk_bf16_f32 v42, v24, v25\n"
    "v_mul_f32 v28, s52, v28\n"
    "v_mfma_f32_16x16x32_bf16 v[48:51], a[172:175], a[28:31], v[48:51]\n"
    "ds_read_b128 a[216:219], v0 offset:25600\n"
    "v_mul_f32 v29, s52, v29\n"
    "v_mfma_f32_16x16x32_bf16 v[52:55], a[172:175], a[60:63], 0\n"
    "s_add_u32 s57, s55, 0x0\n"
    "v_exp_f32 v26, v26\n"
    "v_mfma_f32_16x16x32_bf16 v[52:55], a[168:171], a[56:59], v[52:55]\n"
    "s_add_u32 m0, s56, 0x10000\n"
    "v_exp_f32 v27, v27\n"
    "v_mfma_f32_16x16x32_bf16 v[52:55], a[164:167], a[52:55], v[52:55]\n"
    "ds_read_b128 a[220:223], v0 offset:26112\n"
    "v_cvt_pk_bf16_f32 v43, v26, v27\n"
    "v_mfma_f32_16x16x32_bf16 v[52:55], a[160:163], a[48:51], v[52:55]\n"
    "buffer_load_dwordx4 v3, s[44:47], s57 offen offset:0 nt lds\n"
    "v_mfma_f32_16x16x32_bf16 v[52:55], a[204:207], a[44:47], v[52:55]\n"
    "v_exp_f32 v28, v28\n"
    "v_mfma_f32_16x16x32_bf16 v[52:55], a[200:203], a[40:43], v[52:55]\n"
    "ds_read_b128 a[224:227], v0 offset:26624\n"
    "v_mul_f32 v30, s52, v30\n"
    "v_mfma_f32_16x16x32_bf16 v[52:55], a[196:199], a[36:39], v[52:55]\n"
    "v_exp_f32 v29, v29\n"
    "v_mfma_f32_16x16x32_bf16 v[52:55], a[192:195], a[32:35], v[52:55]\n"
    "v_cvt_pk_bf16_f32 v46, v28, v29\n"
    "v_mul_f32 v31, s52, v31\n"
    "v_mfma_f32_16x16x32_bf16 v[56:59], a[192:195], a[64:67], 0\n"
    "ds_read_b128 a[228:231], v0 offset:27136\n"
    "v_mul_f32 v48, s52, v48\n"
    "v_mfma_f32_16x16x32_bf16 v[56:59], a[196:199], a[68:71], v[56:59]\n"
    "v_exp_f32 v30, v30\n"
    "v_mfma_f32_16x16x32_bf16 v[56:59], a[200:203], a[72:75], v[56:59]\n"
    "s_add_u32 m0, s56, 0x10380\n"
    "v_exp_f32 v31, v31\n"
    "v_mfma_f32_16x16x32_bf16 v[56:59], a[204:207], a[76:79], v[56:59]\n"
    "ds_read_b128 a[232:235], v0 offset:27648\n"
    "v_cvt_pk_bf16_f32 v47, v30, v31\n"
    "v_mfma_f32_16x16x32_bf16 v[56:59], a[160:163], a[80:83], v[56:59]\n"
    "buffer_load_dwordx4 v3, s[44:47], s57 offen offset:128 nt lds\n"
    "L_pad4_%=:\n"
    "s_cbranch_execz L_ret1_%=\n"
    "v_mfma_f32_16x16x32_bf16 v[56:59], a[164:167], a[84:87], v[56:59]\n"
    "v_exp_f32 v48, v48\n"
    "v_mfma_f32_16x16x32_bf16 v[56:59], a[168:171], a[88:91], v[56:59]\n"
    "ds_read_b128 a[236:239], v0 offset:28160\n"
    "v_mul_f32 v49, s52, v49\n"
    "v_mfma_f32_16x16x32_bf16 v[56:59], a[172:175], a[92:95], v[56:59]\n"
    "v_exp_f32 v49, v49\n"
    "v_mfma_f32_16x16x32_bf16 v[60:63], a[172:175], a[124:127], 0\n"
    "v_mul_f32 v50, s52, v50\n"
    "v_cvt_pk_bf16_f32 v48, v48, v49\n"
    "v_mfma_f32_16x16x32_bf16 v[60:63], a[168:171], a[120:123], v[60:63]\n"
    "v_exp_f32 v50, v50\n"
    "v_mfma_f32_16x16x32_bf16 v[60:63], a[164:167], a[116:119], v[60:63]\n"
    "v_mul_f32 v51, s52, v51\n"
    "v_mul_f32 v52, s52, v52\n"
    "v_mfma_f32_16x16x32_bf16 v[60:63], a[160:163], a[112:115], v[60:63]\n"
    "v_exp_f32 v51, v51\n"
    "v_mfma_f32_16x16x32_bf16 v[60:63], a[204:207], a[108:111], v[60:63]\n"
    "v_cvt_pk_bf16_f32 v49, v50, v51\n"
    "v_mul_f32 v53, s52, v53\n"
    "v_mfma_f32_16x16x32_bf16 v[60:63], a[200:203], a[104:107], v[60:63]\n"
    "v_exp_f32 v52, v52\n"
    "v_mfma_f32_16x16x32_bf16 v[60:63], a[196:199], a[100:103], v[60:63]\n"
    "v_exp_f32 v53, v53\n"
    "v_mfma_f32_16x16x32_bf16 v[60:63], a[192:195], a[96:99], v[60:63]\n"
    "v_mul_f32 v54, s52, v54\n"
    "v_cvt_pk_bf16_f32 v52, v52, v53\n"
    "v_mfma_f32_16x16x32_bf16 a[240:243], v[12:15], a[152:155], a[240:243]\n"
    "v_exp_f32 v54, v54\n"
    "v_mfma_f32_16x16x32_bf16 a[244:247], v[12:15], v[184:187], a[244:247]\n"
    "s_waitcnt vmcnt(12) lgkmcnt(0)\n"
    "v_mul_f32 v55, s52, v55\n"
    "v_mul_f32 v56, s52, v56\n"
    "v_mfma_f32_16x16x32_bf16 v[16:19], a[208:211], a[0:3], 0\n"
    "s_barrier\n"
    "v_exp_f32 v55, v55\n"
    "v_mfma_f32_16x16x32_bf16 v[16:19], a[212:215], a[4:7], v[16:19]\n"
    "ds_read_b128 a[192:195], v0 offset:32768\n"
    "v_cvt_pk_bf16_f32 v53, v54, v55\n"
    "v_mfma_f32_16x16x32_bf16 v[16:19], a[216:219], a[8:11], v[16:19]\n"
    "s_add_u32 s57, s53, 0x24000\n"
    "v_exp_f32 v56, v56\n"
    "v_mfma_f32_16x16x32_bf16 v[16:19], a[220:223], a[12:15], v[16:19]\n"
    "s_add_u32 m0, s56, 0x4000\n"
    "v_mul_f32 v57, s52, v57\n"
    "v_mul_f32 v58, s52, v58\n"
    "v_mfma_f32_16x16x32_bf16 v[16:19], a[224:227], a[16:19], v[16:19]\n"
    "ds_read_b128 a[196:199], v0 offset:33280\n"
    "v_mul_f32 v59, s52, v59\n"
    "v_mfma_f32_16x16x32_bf16 v[16:19], a[228:231], a[20:23], v[16:19]\n"
    "buffer_load_dwordx4 v3, s[36:39], s57 offen offset:0 lds\n"
    "v_mfma_f32_16x16x32_bf16 v[16:19], a[232:235], a[24:27], v[16:19]\n"
    "v_exp_f32 v57, v57\n"
    "v_mfma_f32_16x16x32_bf16 v[16:19], a[236:239], a[28:31], v[16:19]\n"
    "ds_read_b128 a[200:203], v0 offset:33792\n"
    "v_cvt_pk_bf16_f32 v56, v56, v57\n"
    "v_mfma_f32_16x16x32_bf16 v[20:23], a[236:239], a[60:63], 0\n"
    "v_exp_f32 v58, v58\n"
    "v_mfma_f32_16x16x32_bf16 v[20:23], a[232:235], a[56:59], v[20:23]\n"
    "v_exp_f32 v59, v59\n"
    "v_mfma_f32_16x16x32_bf16 v[20:23], a[228:231], a[52:55], v[20:23]\n"
    "ds_read_b128 a[204:207], v0 offset:34304\n"
    "v_cvt_pk_bf16_f32 v57, v58, v59\n"
    "v_mfma_f32_16x16x32_bf16 v[20:23], a[224:227], a[48:51], v[20:23]\n"
    "v_mul_f32 v60, s52, v60\n"
    "v_mul_f32 v61, s52, v61\n"
    "v_mfma_f32_16x16x32_bf16 v[20:23], a[220:223], a[44:47], v[20:23]\n"
    "s_add_u32 m0, s56, 0x4380\n"
    "v_exp_f32 v60, v60\n"
    "v_mfma_f32_16x16x32_bf16 v[20:23], a[216:219], a[40:43], v[20:23]\n"
    "ds_read_b128 a[160:163], v0 offset:34816\n"
    "v_mul_f32 v62, s52, v62\n"
    "v_mfma_f32_16x16x32_bf16 v[20:23], a[212:215], a[36:39], v[20:23]\n"
    "buffer_load_dwordx4 v3, s[36:39], s57 offen offset:128 lds\n"
    "v_mfma_f32_16x16x32_bf16 v[20:23], a[208:211], a[32:35], v[20:23]\n"
    "v_exp_f32 v61, v61\n"
    "v_mfma_f32_16x16x32_bf16 v[24:27], a[208:211], a[64:67], 0\n"
    "ds_read_b128 a[164:167], v0 offset:35328\n"
    "v_cvt_pk_bf16_f32 v60, v60, v61\n"
    "v_mfma_f32_16x16x32_bf16 v[24:27], a[212:215], a[68:71], v[24:27]\n"
    "v_exp_f32 v62, v62\n"
    "v_mfma_f32_16x16x32_bf16 v[24:27], a[216:219], a[72:75], v[24:27]\n"
    "v_mul_f32 v63, s52, v63\n"
    "v_mul_f32 v16, s52, v16\n"
    "v_mfma_f32_16x16x32_bf16 v[24:27], a[220:223], a[76:79], v[24:27]\n"
    "ds_read_b128 a[168:171], v0 offset:35840\n"
    "v_mul_f32 v17, s52, v17\n"
    "v_mfma_f32_16x16x32_bf16 v[24:27], a[224:227], a[80:83], v[24:27]\n"
    "v_exp_f32 v63, v63\n"
    "v_mfma_f32_16x16x32_bf16 v[24:27], a[228:231], a[84:87], v[24:27]\n"
    "v_cvt_pk_bf16_f32 v61, v62, v63\n"
    "v_mul_f32 v18, s52, v18\n"
    "v_mfma_f32_16x16x32_bf16 v[24:27], a[232:235], a[88:91], v[24:27]\n"
    "ds_read_b128 a[172:175], v0 offset:36352\n"
    "v_mul_f32 v19, s52, v19\n"
    "v_mfma_f32_16x16x32_bf16 v[24:27], a[236:239], a[92:95], v[24:27]\n"
    "s_add_u32 m0, s56, 0x4700\n"
    "v_exp_f32 v16, v16\n"
    "v_mfma_f32_16x16x32_bf16 v[28:31], a[236:239], a[124:127], 0\n"
    "buffer_load_dwordx4 v3, s[36:39], s57 offen offset:256 lds\n"
    "v_mfma_f32_16x16x32_bf16 v[28:31], a[232:235], a[120:123], v[28:31]\n"
    "v_exp_f32 v17, v17\n"
    "v_mfma_f32_16x16x32_bf16 v[28:31], a[228:231], a[116:119], v[28:31]\n"
    "v_cvt_pk_bf16_f32 v50, v16, v17\n"
    "v_mul_f32 v20, s52, v20\n"
    "v_mfma_f32_16x16x32_bf16 v[28:31], a[224:227], a[112:115], v[28:31]\n"
    "v_exp_f32 v18, v18\n"
    "v_mfma_f32_16x16x32_bf16 v[28:31], a[220:223], a[108:111], v[28:31]\n"
    "v_exp_f32 v19, v19\n"
    "v_mfma_f32_16x16x32_bf16 v[28:31], a[216:219], a[104:107], v[28:31]\n"
    "v_cvt_pk_bf16_f32 v51, v18, v19\n"
    "v_mul_f32 v21, s52, v21\n"
    "v_mfma_f32_16x16x32_bf16 v[28:31], a[212:215], a[100:103], v[28:31]\n"
    "v_exp_f32 v20, v20\n"
    "v_mfma_f32_16x16x32_bf16 v[28:31], a[208:211], a[96:99], v[28:31]\n"
    "v_exp_f32 v21, v21\n"
    "v_mfma_f32_16x16x32_bf16 a[248:251], v[12:15], a[156:159], a[248:251]\n"
    "s_add_u32 m0, s56, 0x4a80\n"
    "v_mul_f32 v22, s52, v22\n"
    "v_cvt_pk_bf16_f32 v54, v20, v21\n"
    "v_mfma_f32_16x16x32_bf16 a[252:255], v[12:15], v[188:191], a[252:255]\n"
    "s_waitcnt lgkmcnt(0)\n"
    "v_exp_f32 v22, v22\n"
    "L_ent_%=:\n"
    "v_mfma_f32_16x16x32_bf16 v[64:67], a[192:195], a[0:3], 0\n"
    "L_pad5_%=:\n"
    "s_cbranch_execz L_ret1_%=\n"
    "buffer_load_dwordx4 v3, s[36:39], s57 offen offset:384 lds\n"
    "v_mfma_f32_16x16x32_bf16 v[64:67], a[196:199], a[4:7], v[64:67]\n"
    "ds_read_b128 a[208:211], v0 offset:40960\n"
    "v_mul_f32 v23, s52, v23\n"
    "v_mfma_f32_16x16x32_bf16 v[64:67], a[200:203], a[8:11], v[64:67]\n"
    "v_exp_f32 v23, v23\n"
    "v_mfma_f32_16x16x32_bf16 v[64:67], a[204:207], a[12:15], v[64:67]\n"
    "v_cvt_pk_bf16_f32 v55, v22, v23\n"
    "v_mul_f32 v24, s52, v24\n"
    "v_mfma_f32_16x16x32_bf16 v[64:67], a[160:163], a[16:19], v[64:67]\n"
    "ds_read_b128 a[212:215], v0 offset:41472\n"
    "v_mul_f32 v25, s52, v25\n"
    "v_mfma_f32_16x16x32_bf16 v[64:67], a[164:167], a[20:23], v[64:67]\n"
    "v_exp_f32 v24, v24\n"
    "v_mfma_f32_16x16x32_bf16 v[64:67], a[168:171], a[24:27], v[64:67]\n"
    "v_exp_f32 v25, v25\n"
    "v_mfma_f32_16x16x32_bf16 v[64:67], a[172:175], a[28:31], v[64:67]\n"
    "ds_read_b128 a[216:219], v0 offset:41984\n"
    "v_mul_f32 v26, s52, v26\n"
    "v_mfma_f32_16x16x32_bf16 v[68:71], a[172:175], a[60:63], 0\n"
    "s_add_u32 s57, s55, 0x0\n"
    "v_cvt_pk_bf16_f32 v58, v24, v25\n"
    "v_mul_f32 v27, s52, v27\n"
    "v_mfma_f32_16x16x32_bf16 v[68:71], a[168:171], a[56:59], v[68:71]\n"
    "s_add_u32 m0, s56, 0x10700\n"
    "v_exp_f32 v26, v26\n"
    "v_mfma_f32_16x16x32_bf16 v[68:71], a[164:167], a[52:55], v[68:71]\n"
    "ds_read_b128 a[220:223], v0 offset:42496\n"
    "v_mul_f32 v28, s52, v28\n"
    "v_mfma_f32_16x16x32_bf16 v[68:71], a[160:163], a[48:51], v[68:71]\n"
    "buffer_load_dwordx4 v3, s[44:47], s57 offen offset:256 nt lds\n"
    "v_mfma_f32_16x16x32_bf16 v[68:71], a[204:207], a[44:47], v[68:71]\n"
    "v_exp_f32 v27, v27\n"
    "v_mfma_f32_16x16x32_bf16 v[68:71], a[200:203], a[40:43], v[68:71]\n"
    "ds_read_b128 a[224:227], v0 offset:43008\n"
    "v_cvt_pk_bf16_f32 v59, v26, v27\n"
    "v_mfma_f32_16x16x32_bf16 v[68:71], a[196:199], a[36:39], v[68:71]\n"
    "v_exp_f32 v28, v28\n"
    "v_mfma_f32_16x16x32_bf16 v[68:71], a[192:195], a[32:35], v[68:71]\n"
    "v_mul_f32 v29, s52, v29\n"
    "v_mul_f32 v30, s52, v30\n"
    "v_mfma_f32_16x16x32_bf16 v[72:75], a[192:195], a[64:67], 0\n"
    "ds_read_b128 a[228:231], v0 offset:43520\n"
    "v_mul_f32 v31, s52, v31\n"
    "v_mfma_f32_16x16x32_bf16 v[72:75], a[196:199], a[68:71], v[72:75]\n"
    "v_exp_f32 v29, v29\n"
    "v_mfma_f32_16x16x32_bf16 v[72:75], a[200:203], a[72:75], v[72:75]\n"
    "s_add_u32 m0, s56, 0x10a80\n"
    "v_cvt_pk_bf16_f32 v62, v28, v29\n"
    "v_mul_f32 v64, s52, v64\n"
    "v_mfma_f32_16x16x32_bf16 v[72:75], a[204:207], a[76:79], v[72:75]\n"
    "ds_read_b128 a[232:235], v0 offset:44032\n"
    "v_mul_f32 v65, s52, v65\n"
    "v_mfma_f32_16x16x32_bf16 v[72:75], a[160:163], a[80:83], v[72:75]\n"
    "buffer_load_dwordx4 v3, s[44:47], s57 offen offset:384 nt lds\n"
    "v_mfma_f32_16x16x32_bf16 v[72:75], a[164:167], a[84:87], v[72:75]\n"
    "v_exp_f32 v30, v30\n"
    "v_mfma_f32_16x16x32_bf16 v[72:75], a[168:171], a[88:91], v[72:75]\n"
    "ds_read_b128 a[236:239], v0 offset:44544\n"
    "v_mul_f32 v66, s52, v66\n"
    "v_mfma_f32_16x16x32_bf16 v[72:75], a[172:175], a[92:95], v[72:75]\n"
    "v_exp_f32 v31, v31\n"
    "v_mfma_f32_16x16x32_bf16 v[76:79], a[172:175], a[124:127], 0\n"
    "v_cvt_pk_bf16_f32 v63, v30, v31\n"
    "v_mul_f32 v67, s52, v67\n"
    "v_mfma_f32_16x16x32_bf16 v[76:79], a[168:171], a[120:123], v[76:79]\n"
    "v_exp_f32 v64, v64\n"
    "v_mfma_f32_16x16x32_bf16 v[76:79], a[164:167], a[116:119], v[76:79]\n"
    "v_exp_f32 v65, v65\n"
    "v_mfma_f32_16x16x32_bf16 v[76:79], a[160:163], a[112:115], v[76:79]\n"
    "v_cvt_pk_bf16_f32 v64, v64, v65\n"
    "v_mul_f32 v68, s52, v68\n"
    "v_mfma_f32_16x16x32_bf16 v[76:79], a[204:207], a[108:111], v[76:79]\n"
    "v_exp_f32 v66, v66\n"
    "v_mfma_f32_16x16x32_bf16 v[76:79], a[200:203], a[104:107], v[76:79]\n"
    "v_exp_f32 v67, v67\n"
    "v_mfma_f32_16x16x32_bf16 v[76:79], a[196:199], a[100:103], v[76:79]\n"
    "v_cvt_pk_bf16_f32 v65, v66, v67\n"
    "v_mul_f32 v69, s52, v69\n"
    "v_mfma_f32_16x16x32_bf16 v[76:79], a[192:195], a[96:99], v[76:79]\n"
    "v_exp_f32 v68, v68\n"
    "v_mfma_f32_16x16x32_bf16 a[240:243], v[12:15], v[32:35], a[240:243]\n"
    "v_exp_f32 v69, v69\n"
    "v_mfma_f32_16x16x32_bf16 a[244:247], v[12:15], v[36:39], a[244:247]\n"
    "s_waitcnt vmcnt(12) lgkmcnt(0)\n"
    "v_mul_f32 v70, s52, v70\n"
    "v_cvt_pk_bf16_f32 v68, v68, v69\n"
    "v_mfma_f32_16x16x32_bf16 v[16:19], a[208:211], a[0:3], 0\n"
    "s_barrier\n"
    "v_exp_f32 v70, v70\n"
    "v_mfma_f32_16x16x32_bf16 v[16:19], a[212:215], a[4:7], v[16:19]\n"
    "ds_read_b128 a[192:195], v0 offset:49152\n"
    "v_mul_f32 v71, s52, v71\n"
    "v_mfma_f32_16x16x32_bf16 v[16:19], a[216:219], a[8:11], v[16:19]\n"
    "s_add_u32 s57, s53, 0x28000\n"
    "v_and_b32 v11, 32, v5\n"
    "v_mul_f32 v72, s52, v72\n"
    "v_mfma_f32_16x16x32_bf16 v[16:19], a[220:223], a[12:15], v[16:19]\n"
    "s_add_u32 m0, s56, 0x8000\n"
    "v_lshl_add_u32 v11, v11, 2, v5\n"
    "v_mul_f32 v73, s52, v73\n"
    "v_mfma_f32_16x16x32_bf16 v[16:19], a[224:227], a[16:19], v[16:19]\n"
    "ds_read_b128 a[196:199], v0 offset:49664\n"
    "v_mul_f32 v74, s52, v74\n"
    "v_mfma_f32_16x16x32_bf16 v[16:19], a[228:231], a[20:23], v[16:19]\n"
    "buffer_load_dwordx4 v3, s[36:39], s57 offen offset:0 lds\n"
    "v_mfma_f32_16x16x32_bf16 v[16:19], a[232:235], a[24:27], v[16:19]\n"
    "s_cmp_eq_u32 s58, 1\n"
    "s_cselect_b32 s88, s89, s42\n"
    "v_exp_f32 v71, v71\n"
    "v_mfma_f32_16x16x32_bf16 v[16:19], a[236:239], a[28:31], v[16:19]\n"
    "ds_read_b128 a[200:203], v0 offset:50176\n"
    "v_cvt_pk_bf16_f32 v69, v70, v71\n"
    "v_mfma_f32_16x16x32_bf16 v[20:23], a[236:239], a[60:63], 0\n"
    "v_exp_f32 v72, v72\n"
    "v_mfma_f32_16x16x32_bf16 v[20:23], a[232:235], a[56:59], v[20:23]\n"
    "v_exp_f32 v73, v73\n"
    "v_mfma_f32_16x16x32_bf16 v[20:23], a[228:231], a[52:55], v[20:23]\n"
    "ds_read_b128 a[204:207], v0 offset:50688\n"
    "v_cvt_pk_bf16_f32 v72, v72, v73\n"
    "v_mfma_f32_16x16x32_bf16 v[20:23], a[224:227], a[48:51], v[20:23]\n"
    "v_exp_f32 v74, v74\n"
    "v_mfma_f32_16x16x32_bf16 v[20:23], a[220:223], a[44:47], v[20:23]\n"
    "s_add_u32 m0, s56, 0x8380\n"
    "v_mul_f32 v75, s52, v75\n"
    "v_mul_f32 v76, s52, v76\n"
    "v_mfma_f32_16x16x32_bf16 v[20:23], a[216:219], a[40:43], v[20:23]\n"
    "ds_read_b128 a[160:163], v0 offset:51200\n"
    "v_mul_f32 v77, s52, v77\n"
    "v_mfma_f32_16x16x32_bf16 v[20:23], a[212:215], a[36:39], v[20:23]\n"
    "buffer_load_dwordx4 v3, s[36:39], s57 offen offset:128 lds\n"
    "v_mfma_f32_16x16x32_bf16 v[20:23], a[208:211], a[32:35], v[20:23]\n"
    "v_exp_f32 v75, v75\n"
    "v_mfma_f32_16x16x32_bf16 v[24:27], a[208:211], a[64:67], 0\n"
    "ds_read_b128 a[164:167], v0 offset:51712\n"
    "v_cvt_pk_bf16_f32 v73, v74, v75\n"
    "v_mfma_f32_16x16x32_bf16 v[24:27], a[212:215], a[68:71], v[24:27]\n"
    "v_exp_f32 v76, v76\n"
    "v_mfma_f32_16x16x32_bf16 v[24:27], a[216:219], a[72:75], v[24:27]\n"
    "v_exp_f32 v77, v77\n"
    "v_mfma_f32_16x16x32_bf16 v[24:27], a[220:223], a[76:79], v[24:27]\n"
    "ds_read_b128 a[168:171], v0 offset:52224\n"
    "v_mul_f32 v78, s52, v78\n"
    "v_mfma_f32_16x16x32_bf16 v[24:27], a[224:227], a[80:83], v[24:27]\n"
    "v_cvt_pk_bf16_f32 v76, v76, v77\n"
    "v_mul_f32 v79, s52, v79\n"
    "v_mfma_f32_16x16x32_bf16 v[24:27], a[228:231], a[84:87], v[24:27]\n"
    "v_exp_f32 v78, v78\n"
    "v_mfma_f32_16x16x32_bf16 v[24:27], a[232:235], a[88:91], v[24:27]\n"
    "ds_read_b128 a[172:175], v0 offset:52736\n"
    "v_mul_f32 v16, s52, v16\n"
    "v_mfma_f32_16x16x32_bf16 v[24:27], a[236:239], a[92:95], v[24:27]\n"
    "s_add_u32 m0, s56, 0x8700\n"
    "v_exp_f32 v79, v79\n"
    "v_mfma_f32_16x16x32_bf16 v[28:31], a[236:239], a[124:127], 0\n"
    "buffer_load_dwordx4 v3, s[36:39], s57 offen offset:256 lds\n"
    "v_mfma_f32_16x16x32_bf16 v[28:31], a[232:235], a[120:123], v[28:31]\n"
    "v_cvt_pk_bf16_f32 v77, v78, v79\n"
    "v_mul_f32 v17, s52, v17\n"
    "v_mfma_f32_16x16x32_bf16 v[28:31], a[228:231], a[116:119], v[28:31]\n"
    "v_exp_f32 v16, v16\n"
    "v_mfma_f32_16x16x32_bf16 v[28:31], a[224:227], a[112:115], v[28:31]\n"
    "v_exp_f32 v17, v17\n"
    "v_mfma_f32_16x16x32_bf16 v[28:31], a[220:223], a[108:111], v[28:31]\n"
    "v_mul_f32 v18, s52, v18\n"
    "v_cvt_pk_bf16_f32 v66, v16, v17\n"
    "v_mfma_f32_16x16x32_bf16 v[28:31], a[216:219], a[104:107], v[28:31]\n"
    "v_exp_f32 v18, v18\n"
    "v_mfma_f32_16x16x32_bf16 v[28:31], a[212:215], a[100:103], v[28:31]\n"
    "v_mul_f32 v19, s52, v19\n"
    "v_mul_f32 v20, s52, v20\n"
    "v_mfma_f32_16x16x32_bf16 v[28:31], a[208:211], a[96:99], v[28:31]\n"
    "v_exp_f32 v19, v19\n"
    "v_mfma_f32_16x16x32_bf16 a[248:251], v[12:15], v[40:43], a[248:251]\n"
    "s_add_u32 m0, s56, 0x8a80\n"
    "v_cvt_pk_bf16_f32 v67, v18, v19\n"
    "v_mul_f32 v21, s52, v21\n"
    "v_mfma_f32_16x16x32_bf16 a[252:255], v[12:15], v[44:47], a[252:255]\n"
    "s_waitcnt lgkmcnt(0)\n"
    "v_exp_f32 v20, v20\n"
    "v_mfma_f32_16x16x32_bf16 v[80:83], a[192:195], a[0:3], 0\n"
    "buffer_load_dwordx4 v3, s[36:39], s57 offen offset:384 lds\n"
    "v_mfma_f32_16x16x32_bf16 v[80:83], a[196:199], a[4:7], v[80:83]\n"
    "ds_read_b128 a[208:211], v0 offset:57344\n"
    "v_mul_f32 v22, s52, v22\n"
    "v_mfma_f32_16x16x32_bf16 v[80:83], a[200:203], a[8:11], v[80:83]\n"
    "v_exp_f32 v21, v21\n"
    "v_mfma_f32_16x16x32_bf16 v[80:83], a[204:207], a[12:15], v[80:83]\n"
    "v_cvt_pk_bf16_f32 v70, v20, v21\n"
    "v_mul_f32 v23, s52, v23\n"
    "v_mfma_f32_16x16x32_bf16 v[80:83], a[160:163], a[16:19], v[80:83]\n"
    "ds_read_b128 a[212:215], v0 offset:57856\n"
    "v_mul_f32 v24, s52, v24\n"
    "v_mfma_f32_16x16x32_bf16 v[80:83], a[164:167], a[20:23], v[80:83]\n"
    "buffer_load_dword a176, v11, s[40:43], s88 offen\n"
    "v_mfma_f32_16x16x32_bf16 v[80:83], a[168:171], a[24:27], v[80:83]\n"
    "s_add_u32 s88, s88, 0x2000\n"
    "v_exp_f32 v22, v22\n"
    "v_mfma_f32_16x16x32_bf16 v[80:83], a[172:175], a[28:31], v[80:83]\n"
    "ds_read_b128 a[216:219], v0 offset:58368\n"
    "v_mul_f32 v25, s52, v25\n"
    "v_mfma_f32_16x16x32_bf16 v[84:87], a[172:175], a[60:63], 0\n"
    "s_add_u32 s57, s55, 0x10000\n"
    "v_exp_f32 v23, v23\n"
    "v_mfma_f32_16x16x32_bf16 v[84:87], a[168:171], a[56:59], v[84:87]\n"
    "s_add_u32 m0, s56, 0x14000\n"
    "v_cvt_pk_bf16_f32 v71, v22, v23\n"
    "v_mul_f32 v26, s52, v26\n"
    "v_mfma_f32_16x16x32_bf16 v[84:87], a[164:167], a[52:55], v[84:87]\n"
    "ds_read_b128 a[220:223], v0 offset:58880\n"
    "v_mul_f32 v27, s52, v27\n"
    "v_mfma_f32_16x16x32_bf16 v[84:87], a[160:163], a[48:51], v[84:87]\n"
    "buffer_load_dwordx4 v3, s[44:47], s57 offen offset:0 nt lds\n"
    "v_mfma_f32_16x16x32_bf16 v[84:87], a[204:207], a[44:47], v[84:87]\n"
    "v_exp_f32 v24, v24\n"
    "v_mfma_f32_16x16x32_bf16 v[84:87], a[200:203], a[40:43], v[84:87]\n"
    "ds_read_b128 a[224:227], v0 offset:59392\n"
    "v_mul_f32 v28, s52, v28\n"
    "v_mfma_f32_16x16x32_bf16 v[84:87], a[196:199], a[36:39], v[84:87]\n"
    "v_exp_f32 v25, v25\n"
    "v_mfma_f32_16x16x32_bf16 v[84:87], a[192:195], a[32:35], v[84:87]\n"
    "v_cvt_pk_bf16_f32 v74, v24, v25\n"
    "v_mul_f32 v29, s52, v29\n"
    "v_mfma_f32_16x16x32_bf16 v[88:91], a[192:195], a[64:67], 0\n"
    "ds_read_b128 a[228:231], v0 offset:59904\n"
    "v_mul_f32 v30, s52, v30\n"
    "v_mfma_f32_16x16x32_bf16 v[88:91], a[196:199], a[68:71], v[88:91]\n"
    "v_exp_f32 v26, v26\n"
    "v_mfma_f32_16x16x32_bf16 v[88:91], a[200:203], a[72:75], v[88:91]\n"
    "s_add_u32 m0, s56, 0x14380\n"
    "v_exp_f32 v27, v27\n"
    "v_mfma_f32_16x16x32_bf16 v[88:91], a[204:207], a[76:79], v[88:91]\n"
    "ds_read_b128 a[232:235], v0 offset:60416\n"
    "v_cvt_pk_bf16_f32 v75, v26, v27\n"
    "v_mfma_f32_16x16x32_bf16 v[88:91], a[160:163], a[80:83], v[88:91]\n"
    "buffer_load_dwordx4 v3, s[44:47], s57 offen offset:128 nt lds\n"
    "v_mfma_f32_16x16x32_bf16 v[88:91], a[164:167], a[84:87], v[88:91]\n"
    "v_exp_f32 v28, v28\n"
    "v_mfma_f32_16x16x32_bf16 v[88:91], a[168:171], a[88:91], v[88:91]\n"
    "ds_read_b128 a[236:239], v0 offset:60928\n"
    "v_mul_f32 v31, s52, v31\n"
    "v_mfma_f32_16x16x32_bf16 v[88:91], a[172:175], a[92:95], v[88:91]\n"
    "v_exp_f32 v29, v29\n"
    "v_mfma_f32_16x16x32_bf16 v[92:95], a[172:175], a[124:127], 0\n"
    "v_cvt_pk_bf16_f32 v78, v28, v29\n"
    "v_mul_f32 v80, s52, v80\n"
    "v_mfma_f32_16x16x32_bf16 v[92:95], a[168:171], a[120:123], v[92:95]\n"
    "v_exp_f32 v30, v30\n"
    "v_mfma_f32_16x16x32_bf16 v[92:95], a[164:167], a[116:119], v[92:95]\n"
    "v_exp_f32 v31, v31\n"
    "v_mfma_f32_16x16x32_bf16 v[92:95], a[160:163], a[112:115], v[92:95]\n"
    "v_cvt_pk_bf16_f32 v79, v30, v31\n"
    "v_mul_f32 v81, s52, v81\n"
    "v_mfma_f32_16x16x32_bf16 v[92:95], a[204:207], a[108:111], v[92:95]\n"
    "v_exp_f32 v80, v80\n"
    "v_mfma_f32_16x16x32_bf16 v[92:95], a[200:203], a[104:107], v[92:95]\n"
    "v_exp_f32 v81, v81\n"
    "v_mfma_f32_16x16x32_bf16 v[92:95], a[196:199], a[100:103], v[92:95]\n"
    "v_mul_f32 v82, s52, v82\n"
    "v_cvt_pk_bf16_f32 v80, v80, v81\n"
    "v_mfma_f32_16x16x32_bf16 v[92:95], a[192:195], a[96:99], v[92:95]\n"
    "v_exp_f32 v82, v82\n"
    "v_mfma_f32_16x16x32_bf16 a[240:243], v[12:15], v[48:51], a[240:243]\n"
    "v_mul_f32 v83, s52, v83\n"
    "v_mul_f32 v84, s52, v84\n"
    "v_mfma_f32_16x16x32_bf16 a[244:247], v[12:15], v[52:55], a[244:247]\n"
    "s_waitcnt vmcnt(15) lgkmcnt(0)\n"
    "v_exp_f32 v83, v83\n"
    "v_mfma_f32_16x16x32_bf16 v[16:19], a[208:211], a[0:3], 0\n"
    "s_barrier\n"
    "v_cvt_pk_bf16_f32 v81, v82, v83\n"
    "v_mul_f32 v85, s52, v85\n"
    "v_mfma_f32_16x16x32_bf16 v[16:19], a[212:215], a[4:7], v[16:19]\n"
    "ds_read_b128 a[192:195], v0 offset:0\n"
    "v_mul_f32 v86, s52, v86\n"
    "v_mfma_f32_16x16x32_bf16 v[16:19], a[216:219], a[8:11], v[16:19]\n"
    "s_add_u32 s57, s53, 0x2c000\n"
    "v_exp_f32 v84, v84\n"
    "v_mfma_f32_16x16x32_bf16 v[16:19], a[220:223], a[12:15], v[16:19]\n"
    "s_add_u32 m0, s56, 0xc000\n"
    "v_exp_f32 v85, v85\n"
    "v_mfma_f32_16x16x32_bf16 v[16:19], a[224:227], a[16:19], v[16:19]\n"
    "ds_read_b128 a[196:199], v0 offset:512\n"
    "v_cvt_pk_bf16_f32 v84, v84, v85\n"
    "v_mfma_f32_16x16x32_bf16 v[16:19], a[228:231], a[20:23], v[16:19]\n"
    "buffer_load_dwordx4 v3, s[36:39], s57 offen offset:0 lds\n"
    "v_mfma_f32_16x16x32_bf16 v[16:19], a[232:235], a[24:27], v[16:19]\n"
    "v_exp_f32 v86, v86\n"
    "v_mfma_f32_16x16x32_bf16 v[16:19], a[236:239], a[28:31], v[16:19]\n"
    "ds_read_b128 a[200:203], v0 offset:1024\n"
    "v_mul_f32 v87, s52, v87\n"
    "v_mfma_f32_16x16x32_bf16 v[20:23], a[236:239], a[60:63], 0\n"
    "v_exp_f32 v87, v87\n"
    "v_mfma_f32_16x16x32_bf16 v[20:23], a[232:235], a[56:59], v[20:23]\n"
    "v_cvt_pk_bf16_f32 v85, v86, v87\n"
    "v_mul_f32 v88, s52, v88\n"
    "v_mfma_f32_16x16x32_bf16 v[20:23], a[228:231], a[52:55], v[20:23]\n"
    "ds_read_b128 a[204:207], v0 offset:1536\n"
    "v_mul_f32 v89, s52, v89\n"
    "v_mfma_f32_16x16x32_bf16 v[20:23], a[224:227], a[48:51], v[20:23]\n"
    "v_exp_f32 v88, v88\n"
    "v_mfma_f32_16x16x32_bf16 v[20:23], a[220:223], a[44:47], v[20:23]\n"
    "s_add_u32 m0, s56, 0xc380\n"
    "v_exp_f32 v89, v89\n"
    "v_mfma_f32_16x16x32_bf16 v[20:23], a[216:219], a[40:43], v[20:23]\n"
    "ds_read_b128 a[160:163], v0 offset:2048\n"
    "v_mul_f32 v90, s52, v90\n"
    "v_mfma_f32_16x16x32_bf16 v[20:23], a[212:215], a[36:39], v[20:23]\n"
    "buffer_load_dwordx4 v3, s[36:39], s57 offen offset:128 lds\n"
    "v_mfma_f32_16x16x32_bf16 v[20:23], a[208:211], a[32:35], v[20:23]\n"
    "v_cvt_pk_bf16_f32 v88, v88, v89\n"
    "v_mul_f32 v91, s52, v91\n"
    "v_mfma_f32_16x16x32_bf16 v[24:27], a[208:211], a[64:67], 0\n"
    "ds_read_b128 a[164:167], v0 offset:2560\n"
    "v_mul_f32 v92, s52, v92\n"
    "v_mfma_f32_16x16x32_bf16 v[24:27], a[212:215], a[68:71], v[24:27]\n"
    "v_exp_f32 v90, v90\n"
    "v_mfma_f32_16x16x32_bf16 v[24:27], a[216:219], a[72:75], v[24:27]\n"
    "v_exp_f32 v91, v91\n"
    "v_mfma_f32_16x16x32_bf16 v[24:27], a[220:223], a[76:79], v[24:27]\n"
    "ds_read_b128 a[168:171], v0 offset:3072\n"
    "v_cvt_pk_bf16_f32 v89, v90, v91\n"
    "v_mfma_f32_16x16x32_bf16 v[24:27], a[224:227], a[80:83], v[24:27]\n"
    "v_exp_f32 v92, v92\n"
    "v_mfma_f32_16x16x32_bf16 v[24:27], a[228:231], a[84:87], v[24:27]\n"
    "v_mul_f32 v93, s52, v93\n"
    "v_mul_f32 v94, s52, v94\n"
    "v_mfma_f32_16x16x32_bf16 v[24:27], a[232:235], a[88:91], v[24:27]\n"
    "ds_read_b128 a[172:175], v0 offset:3584\n"
    "v_mul_f32 v95, s52, v95\n"
    "v_mfma_f32_16x16x32_bf16 v[24:27], a[236:239], a[92:95], v[24:27]\n"
    "s_add_u32 m0, s56, 0xc700\n"
    "v_exp_f32 v93, v93\n"
    "v_mfma_f32_16x16x32_bf16 v[28:31], a[236:239], a[124:127], 0\n"
    "buffer_load_dwordx4 v3, s[36:39], s57 offen offset:256 lds\n"
    "v_mfma_f32_16x16x32_bf16 v[28:31], a[232:235], a[120:123], v[28:31]\n"
    "v_cvt_pk_bf16_f32 v92, v92, v93\n"
    "v_mul_f32 v16, s52, v16\n"
    "v_mfma_f32_16x16x32_bf16 v[28:31], a[228:231], a[116:119], v[28:31]\n"
    "v_exp_f32 v94, v94\n"
    "v_mfma_f32_16x16x32_bf16 v[28:31], a[224:227], a[112:115], v[28:31]\n"
    "v_exp_f32 v95, v95\n"
    "v_mfma_f32_16x16x32_bf16 v[28:31], a[220:223], a[108:111], v[28:31]\n"
    "v_cvt_pk_bf16_f32 v93, v94, v95\n"
    "v_mul_f32 v17, s52, v17\n"
    "v_mfma_f32_16x16x32_bf16 v[28:31], a[216:219], a[104:107], v[28:31]\n"
    "v_exp_f32 v16, v16\n"
    "v_mfma_f32_16x16x32_bf16 v[28:31], a[212:215], a[100:103], v[28:31]\n"
    "v_exp_f32 v17, v17\n"
    "v_mfma_f32_16x16x32_bf16 v[28:31], a[208:211], a[96:99], v[28:31]\n"
    "v_mul_f32 v18, s52, v18\n"
    "v_cvt_pk_bf16_f32 v82, v16, v17\n"
    "v_mfma_f32_16x16x32_bf16 a[248:251], v[12:15], v[56:59], a[248:251]\n"
    "s_add_u32 m0, s56, 0xca80\n"
    "v_exp_f32 v18, v18\n"
    "v_mfma_f32_16x16x32_bf16 a[252:255], v[12:15], v[60:63], a[252:255]\n"
    "s_waitcnt lgkmcnt(0)\n"
    "v_mul_f32 v19, s52, v19\n"
    "v_mul_f32 v20, s52, v20\n"
    "v_mfma_f32_16x16x32_bf16 v[96:99], a[192:195], a[0:3], 0\n"
    "buffer_load_dwordx4 v3, s[36:39], s57 offen offset:384 lds\n"
    "v_mfma_f32_16x16x32_bf16 v[96:99], a[196:199], a[4:7], v[96:99]\n"
    "ds_read_b128 a[208:211], v0 offset:8192\n"
    "v_mul_f32 v21, s52, v21\n"
    "v_mfma_f32_16x16x32_bf16 v[96:99], a[200:203], a[8:11], v[96:99]\n"
    "v_exp_f32 v19, v19\n"
    "v_mfma_f32_16x16x32_bf16 v[96:99], a[204:207], a[12:15], v[96:99]\n"
    "v_cvt_pk_bf16_f32 v83, v18, v19\n"
    "v_mul_f32 v22, s52, v22\n"
    "v_mfma_f32_16x16x32_bf16 v[96:99], a[160:163], a[16:19], v[96:99]\n"
    "ds_read_b128 a[212:215], v0 offset:8704\n"
    "v_mul_f32 v23, s52, v23\n"
    "v_mfma_f32_16x16x32_bf16 v[96:99], a[164:167], a[20:23], v[96:99]\n"
    "v_exp_f32 v20, v20\n"
    "v_mfma_f32_16x16x32_bf16 v[96:99], a[168:171], a[24:27], v[96:99]\n"
    "v_exp_f32 v21, v21\n"
    "v_mfma_f32_16x16x32_bf16 v[96:99], a[172:175], a[28:31], v[96:99]\n"
    "ds_read_b128 a[216:219], v0 offset:9216\n"
    "v_cvt_pk_bf16_f32 v86, v20, v21\n"
    "v_mfma_f32_16x16x32_bf16 v[100:103], a[172:175], a[60:63], 0\n"
    "s_add_u32 s57, s55, 0x10000\n"
    "v_exp_f32 v22, v22\n"
    "v_mfma_f32_16x16x32_bf16 v[100:103], a[168:171], a[56:59], v[100:103]\n"
    "s_add_u32 m0, s56, 0x14700\n"
    "v_exp_f32 v23, v23\n"
    "v_mfma_f32_16x16x32_bf16 v[100:103], a[164:167], a[52:55], v[100:103]\n"
    "ds_read_b128 a[220:223], v0 offset:9728\n"
    "v_cvt_pk_bf16_f32 v87, v22, v23\n"
    "v_mfma_f32_16x16x32_bf16 v[100:103], a[160:163], a[48:51], v[100:103]\n"
    "buffer_load_dwordx4 v3, s[44:47], s57 offen offset:256 nt lds\n"
    "v_mfma_f32_16x16x32_bf16 v[100:103], a[204:207], a[44:47], v[100:103]\n"
    "v_mul_f32 v24, s52, v24\n"
    "v_mul_f32 v25, s52, v25\n"
    "v_mfma_f32_16x16x32_bf16 v[100:103], a[200:203], a[40:43], v[100:103]\n"
    "ds_read_b128 a[224:227], v0 offset:10240\n"
    "v_mul_f32 v26, s52, v26\n"
    "v_mfma_f32_16x16x32_bf16 v[100:103], a[196:199], a[36:39], v[100:103]\n"
    "v_exp_f32 v24, v24\n"
    "v_mfma_f32_16x16x32_bf16 v[100:103], a[192:195], a[32:35], v[100:103]\n"
    "v_exp_f32 v25, v25\n"
    "v_mfma_f32_16x16x32_bf16 v[104:107], a[192:195], a[64:67], 0\n"
    "ds_read_b128 a[228:231], v0 offset:10752\n"
    "v_cvt_pk_bf16_f32 v90, v24, v25\n"
    "v_mfma_f32_16x16x32_bf16 v[104:107], a[196:199], a[68:71], v[104:107]\n"
    "v_exp_f32 v26, v26\n"
    "v_mfma_f32_16x16x32_bf16 v[104:107], a[200:203], a[72:75], v[104:107]\n"
    "s_add_u32 m0, s56, 0x14a80\n"
    "v_mul_f32 v27, s52, v27\n"
    "v_mul_f32 v28, s52, v28\n"
    "v_mfma_f32_16x16x32_bf16 v[104:107], a[204:207], a[76:79], v[104:107]\n"
    "ds_read_b128 a[232:235], v0 offset:11264\n"
    "v_mul_f32 v29, s52, v29\n"
    "v_mfma_f32_16x16x32_bf16 v[104:107], a[160:163], a[80:83], v[104:107]\n"
    "buffer_load_dwordx4 v3, s[44:47], s57 offen offset:384 nt lds\n"
    "v_mfma_f32_16x16x32_bf16 v[104:107], a[164:167], a[84:87], v[104:107]\n"
    "v_exp_f32 v27, v27\n"
    "v_mfma_f32_16x16x32_bf16 v[104:107], a[168:171], a[88:91], v[104:107]\n"
    "ds_read_b128 a[236:239], v0 offset:11776\n"
    "v_cvt_pk_bf16_f32 v91, v26, v27\n"
    "v_mfma_f32_16x16x32_bf16 v[104:107], a[172:175], a[92:95], v[104:107]\n"
    "v_exp_f32 v28, v28\n"
    "v_mfma_f32_16x16x32_bf16 v[108:111], a[172:175], a[124:127], 0\n"
    "v_exp_f32 v29, v29\n"
    "v_mfma_f32_16x16x32_bf16 v[108:111], a[168:171], a[120:123], v[108:111]\n"
    "v_mul_f32 v30, s52, v30\n"
    "v_cvt_pk_bf16_f32 v94, v28, v29\n"
    "v_mfma_f32_16x16x32_bf16 v[108:111], a[164:167], a[116:119], v[108:111]\n"
    "v_exp_f32 v30, v30\n"
    "v_mfma_f32_16x16x32_bf16 v[108:111], a[160:163], a[112:115], v[108:111]\n"
    "v_mul_f32 v31, s52, v31\n"
    "v_mul_f32 v96, s52, v96\n"
    "v_mfma_f32_16x16x32_bf16 v[108:111], a[204:207], a[108:111], v[108:111]\n"
    "v_exp_f32 v31, v31\n"
    "v_mfma_f32_16x16x32_bf16 v[108:111], a[200:203], a[104:107], v[108:111]\n"
    "v_cvt_pk_bf16_f32 v95, v30, v31\n"
    "v_mul_f32 v97, s52, v97\n"
    "v_mfma_f32_16x16x32_bf16 v[108:111], a[196:199], a[100:103], v[108:111]\n"
    "v_exp_f32 v96, v96\n"
    "v_mfma_f32_16x16x32_bf16 v[108:111], a[192:195], a[96:99], v[108:111]\n"
    "v_exp_f32 v97, v97\n"
    "v_mfma_f32_16x16x32_bf16 a[240:243], v[12:15], v[64:67], a[240:243]\n"
    "v_mul_f32 v98, s52, v98\n"
    "v_cvt_pk_bf16_f32 v96, v96, v97\n"
    "v_mfma_f32_16x16x32_bf16 a[244:247], v[12:15], v[68:71], a[244:247]\n"
    "s_waitcnt vmcnt(15) lgkmcnt(0)\n"
    "v_exp_f32 v98, v98\n"
    "v_mfma_f32_16x16x32_bf16 v[16:19], a[208:211], a[0:3], 0\n"
    "s_barrier\n"
    "v_mul_f32 v99, s52, v99\n"
    "v_mul_f32 v100, s52, v100\n"
    "v_mfma_f32_16x16x32_bf16 v[16:19], a[212:215], a[4:7], v[16:19]\n"
    "ds_read_b128 a[192:195], v0 offset:16384\n"
    "v_mul_f32 v101, s52, v101\n"
    "v_mfma_f32_16x16x32_bf16 v[16:19], a[216:219], a[8:11], v[16:19]\n"
    "s_add_u32 s57, s53, 0x30000\n"
    "v_exp_f32 v99, v99\n"
    "v_mfma_f32_16x16x32_bf16 v[16:19], a[220:223], a[12:15], v[16:19]\n"
    "s_add_u32 m0, s56, 0x0\n"
    "v_cvt_pk_bf16_f32 v97, v98, v99\n"
    "v_mul_f32 v102, s52, v102\n"
    "v_mfma_f32_16x16x32_bf16 v[16:19], a[224:227], a[16:19], v[16:19]\n"
    "ds_read_b128 a[196:199], v0 offset:16896\n"
    "v_mul_f32 v103, s52, v103\n"
    "v_mfma_f32_16x16x32_bf16 v[16:19], a[228:231], a[20:23], v[16:19]\n"
    "buffer_load_dwordx4 v3, s[36:39], s57 offen offset:0 lds\n"
    "v_mfma_f32_16x16x32_bf16 v[16:19], a[232:235], a[24:27], v[16:19]\n"
    "v_exp_f32 v100, v100\n"
    "v_mfma_f32_16x16x32_bf16 v[16:19], a[236:239], a[28:31], v[16:19]\n"
    "ds_read_b128 a[200:203], v0 offset:17408\n"
    "v_mul_f32 v104, s52, v104\n"
    "v_mfma_f32_16x16x32_bf16 v[20:23], a[236:239], a[60:63], 0\n"
    "v_exp_f32 v101, v101\n"
    "v_mfma_f32_16x16x32_bf16 v[20:23], a[232:235], a[56:59], v[20:23]\n"
    "v_cvt_pk_bf16_f32 v100, v100, v101\n"
    "v_mul_f32 v105, s52, v105\n"
    "v_mfma_f32_16x16x32_bf16 v[20:23], a[228:231], a[52:55], v[20:23]\n"
    "ds_read_b128 a[204:207], v0 offset:17920\n"
    "v_mul_f32 v106, s52, v106\n"
    "v_mfma_f32_16x16x32_bf16 v[20:23], a[224:227], a[48:51], v[20:23]\n"
    "v_exp_f32 v102, v102\n"
    "v_mfma_f32_16x16x32_bf16 v[20:23], a[220:223], a[44:47], v[20:23]\n"
    "s_add_u32 m0, s56, 0x380\n"
    "v_exp_f32 v103, v103\n"
    "v_mfma_f32_16x16x32_bf16 v[20:23], a[216:219], a[40:43], v[20:23]\n"
    "ds_read_b128 a[160:163], v0 offset:18432\n"
    "v_cvt_pk_bf16_f32 v101, v102, v103\n"
    "v_mfma_f32_16x16x32_bf16 v[20:23], a[212:215], a[36:39], v[20:23]\n"
    "buffer_load_dwordx4 v3, s[36:39], s57 offen offset:128 lds\n"
    "v_mfma_f32_16x16x32_bf16 v[20:23], a[208:211], a[32:35], v[20:23]\n"
    "v_exp_f32 v104, v104\n"
    "v_mfma_f32_16x16x32_bf16 v[24:27], a[208:211], a[64:67], 0\n"
    "ds_read_b128 a[164:167], v0 offset:18944\n"
    "v_mul_f32 v107, s52, v107\n"
    "v_mfma_f32_16x16x32_bf16 v[24:27], a[212:215], a[68:71], v[24:27]\n"
    "v_exp_f32 v105, v105\n"
    "v_mfma_f32_16x16x32_bf16 v[24:27], a[216:219], a[72:75], v[24:27]\n"
    "v_cvt_pk_bf16_f32 v104, v104, v105\n"
    "v_mul_f32 v108, s52, v108\n"
    "v_mfma_f32_16x16x32_bf16 v[24:27], a[220:223], a[76:79], v[24:27]\n"
    "ds_read_b128 a[168:171], v0 offset:19456\n"
    "v_mul_f32 v109, s52, v109\n"
    "v_mfma_f32_16x16x32_bf16 v[24:27], a[224:227], a[80:83], v[24:27]\n"
    "L_pad6_%=:\n"
    "s_cbranch_execz L_ret1_%=\n"
    "v_exp_f32 v106, v106\n"
    "v_mfma_f32_16x16x32_bf16 v[24:27], a[228:231], a[84:87], v[24:27]\n"
    "v_exp_f32 v107, v107\n"
    "v_mfma_f32_16x16x32_bf16 v[24:27], a[232:235], a[88:91], v[24:27]\n"
    "ds_read_b128 a[172:175], v0 offset:19968\n"
    "v_cvt_pk_bf16_f32 v105, v106, v107\n"
    "v_mfma_f32_16x16x32_bf16 v[24:27], a[236:239], a[92:95], v[24:27]\n"
    "s_add_u32 m0, s56, 0x700\n"
    "v_exp_f32 v108, v108\n"
    "v_mfma_f32_16x16x32_bf16 v[28:31], a[236:239], a[124:127], 0\n"
    "buffer_load_dwordx4 v3, s[36:39], s57 offen offset:256 lds\n"
    "v_mfma_f32_16x16x32_bf16 v[28:31], a[232:235], a[120:123], v[28:31]\n"
    "v_exp_f32 v109, v109\n"
    "v_mfma_f32_16x16x32_bf16 v[28:31], a[228:231], a[116:119], v[28:31]\n"
    "v_mul_f32 v110, s52, v110\n"
    "v_cvt_pk_bf16_f32 v108, v108, v109\n"
    "v_mfma_f32_16x16x32_bf16 v[28:31], a[224:227], a[112:115], v[28:31]\n"
    "v_exp_f32 v110, v110\n"
    "v_mfma_f32_16x16x32_bf16 v[28:31], a[220:223], a[108:111], v[28:31]\n"
    "v_mul_f32 v111, s52, v111\n"
    "v_mul_f32 v16, s52, v16\n"
    "v_mfma_f32_16x16x32_bf16 v[28:31], a[216:219], a[104:107], v[28:31]\n"
    "v_exp_f32 v111, v111\n"
    "v_mfma_f32_16x16x32_bf16 v[28:31], a[212:215], a[100:103], v[28:31]\n"
    "v_cvt_pk_bf16_f32 v109, v110, v111\n"
    "v_mul_f32 v17, s52, v17\n"
    "v_mfma_f32_16x16x32_bf16 v[28:31], a[208:211], a[96:99], v[28:31]\n"
    "v_exp_f32 v16, v16\n"
    "v_mfma_f32_16x16x32_bf16 a[248:251], v[12:15], v[72:75], a[248:251]\n"
    "s_add_u32 m0, s56, 0xa80\n"
    "v_exp_f32 v17, v17\n"
    "v_mfma_f32_16x16x32_bf16 a[252:255], v[12:15], v[76:79], a[252:255]\n"
    "s_waitcnt lgkmcnt(0)\n"
    "v_mul_f32 v18, s52, v18\n"
    "v_cvt_pk_bf16_f32 v98, v16, v17\n"
    "v_mfma_f32_16x16x32_bf16 v[112:115], a[192:195], a[0:3], 0\n"
    "buffer_load_dwordx4 v3, s[36:39], s57 offen offset:384 lds\n"
    "v_mfma_f32_16x16x32_bf16 v[112:115], a[196:199], a[4:7], v[112:115]\n"
    "ds_read_b128 a[208:211], v0 offset:24576\n"
    "v_mul_f32 v19, s52, v19\n"
    "v_mfma_f32_16x16x32_bf16 v[112:115], a[200:203], a[8:11], v[112:115]\n"
    "v_exp_f32 v18, v18\n"
    "v_mfma_f32_16x16x32_bf16 v[112:115], a[204:207], a[12:15], v[112:115]\n"
    "v_exp_f32 v19, v19\n"
    "v_mfma_f32_16x16x32_bf16 v[112:115], a[160:163], a[16:19], v[112:115]\n"
    "ds_read_b128 a[212:215], v0 offset:25088\n"
    "v_cvt_pk_bf16_f32 v99, v18, v19\n"
    "v_mfma_f32_16x16x32_bf16 v[112:115], a[164:167], a[20:23], v[112:115]\n"
    "v_mul_f32 v20, s52, v20\n"
    "v_mul_f32 v21, s52, v21\n"
    "v_mfma_f32_16x16x32_bf16 v[112:115], a[168:171], a[24:27], v[112:115]\n"
    "v_exp_f32 v20, v20\n"
    "v_mfma_f32_16x16x32_bf16 v[112:115], a[172:175], a[28:31], v[112:115]\n"
    "ds_read_b128 a[216:219], v0 offset:25600\n"
    "v_mul_f32 v22, s52, v22\n"
    "v_mfma_f32_16x16x32_bf16 v[116:119], a[172:175], a[60:63], 0\n"
    "s_add_u32 s57, s55, 0x2000\n"
    "v_exp_f32 v21, v21\n"
    "v_mfma_f32_16x16x32_bf16 v[116:119], a[168:171], a[56:59], v[116:119]\n"
    "s_add_u32 m0, s56, 0x18000\n"
    "v_cvt_pk_bf16_f32 v102, v20, v21\n"
    "v_mul_f32 v23, s52, v23\n"
    "v_mfma_f32_16x16x32_bf16 v[116:119], a[164:167], a[52:55], v[116:119]\n"
    "ds_read_b128 a[220:223], v0 offset:26112\n"
    "v_mul_f32 v24, s52, v24\n"
    "v_mfma_f32_16x16x32_bf16 v[116:119], a[160:163], a[48:51], v[116:119]\n"
    "buffer_load_dwordx4 v3, s[44:47], s57 offen offset:0 nt lds\n"
    "v_mfma_f32_16x16x32_bf16 v[116:119], a[204:207], a[44:47], v[116:119]\n"
    "v_exp_f32 v22, v22\n"
    "v_mfma_f32_16x16x32_bf16 v[116:119], a[200:203], a[40:43], v[116:119]\n"
    "ds_read_b128 a[224:227], v0 offset:26624\n"
    "v_mul_f32 v25, s52, v25\n"
    "v_mfma_f32_16x16x32_bf16 v[116:119], a[196:199], a[36:39], v[116:119]\n"
    "v_exp_f32 v23, v23\n"
    "v_mfma_f32_16x16x32_bf16 v[116:119], a[192:195], a[32:35], v[116:119]\n"
    "v_cvt_pk_bf16_f32 v103, v22, v23\n"
    "v_mul_f32 v26, s52, v26\n"
    "v_mfma_f32_16x16x32_bf16 v[120:123], a[192:195], a[64:67], 0\n"
    "ds_read_b128 a[228:231], v0 offset:27136\n"
    "v_mul_f32 v27, s52, v27\n"
    "v_mfma_f32_16x16x32_bf16 v[120:123], a[196:199], a[68:71], v[120:123]\n"
    "v_exp_f32 v24, v24\n"
    "v_mfma_f32_16x16x32_bf16 v[120:123], a[200:203], a[72:75], v[120:123]\n"
    "s_add_u32 m0, s56, 0x18380\n"
    "v_exp_f32 v25, v25\n"
    "v_mfma_f32_16x16x32_bf16 v[120:123], a[204:207], a[76:79], v[120:123]\n"
    "ds_read_b128 a[232:235], v0 offset:27648\n"
    "v_cvt_pk_bf16_f32 v106, v24, v25\n"
    "v_mfma_f32_16x16x32_bf16 v[120:123], a[160:163], a[80:83], v[120:123]\n"
    "buffer_load_dwordx4 v3, s[44:47], s57 offen offset:128 nt lds\n"
    "v_mfma_f32_16x16x32_bf16 v[120:123], a[164:167], a[84:87], v[120:123]\n"
    "v_exp_f32 v26, v26\n"
    "v_mfma_f32_16x16x32_bf16 v[120:123], a[168:171], a[88:91], v[120:123]\n"
    "ds_read_b128 a[236:239], v0 offset:28160\n"
    "v_mul_f32 v28, s52, v28\n"
    "v_mfma_f32_16x16x32_bf16 v[120:123], a[172:175], a[92:95], v[120:123]\n"
    "v_exp_f32 v27, v27\n"
    "v_mfma_f32_16x16x32_bf16 v[124:127], a[172:175], a[124:127], 0\n"
    "v_cvt_pk_bf16_f32 v107, v26, v27\n"
    "v_mul_f32 v29, s52, v29\n"
    "v_mfma_f32_16x16x32_bf16 v[124:127], a[168:171], a[120:123], v[124:127]\n"
    "v_exp_f32 v28, v28\n"
    "v_mfma_f32_16x16x32_bf16 v[124:127], a[164:167], a[116:119], v[124:127]\n"
    "v_exp_f32 v29, v29\n"
    "v_mfma_f32_16x16x32_bf16 v[124:127], a[160:163], a[112:115], v[124:127]\n"
    "v_mul_f32 v30, s52, v30\n"
    "v_cvt_pk_bf16_f32 v110, v28, v29\n"
    "v_mfma_f32_16x16x32_bf16 v[124:127], a[204:207], a[108:111], v[124:127]\n"
    "v_exp_f32 v30, v30\n"
    "v_mfma_f32_16x16x32_bf16 v[124:127], a[200:203], a[104:107], v[124:127]\n"
    "v_mul_f32 v31, s52, v31\n"
    "v_mul_f32 v112, s52, v112\n"
    "v_mfma_f32_16x16x32_bf16 v[124:127], a[196:199], a[100:103], v[124:127]\n"
    "v_exp_f32 v31, v31\n"
    "v_mfma_f32_16x16x32_bf16 v[124:127], a[192:195], a[96:99], v[124:127]\n"
    "v_cvt_pk_bf16_f32 v111, v30, v31\n"
    "v_mul_f32 v113, s52, v113\n"
    "v_mfma_f32_16x16x32_bf16 a[240:243], v[12:15], v[80:83], a[240:243]\n"
    "v_exp_f32 v112, v112\n"
    "v_mfma_f32_16x16x32_bf16 a[244:247], v[12:15], v[84:87], a[244:247]\n"
    "s_waitcnt vmcnt(15) lgkmcnt(0)\n"
    "v_exp_f32 v113, v113\n"
    "v_mfma_f32_16x16x32_bf16 v[16:19], a[208:211], a[0:3], 0\n"
    "s_barrier\n"
    "v_mul_f32 v114, s52, v114\n"
    "v_cvt_pk_bf16_f32 v112, v112, v113\n"
    "v_mfma_f32_16x16x32_bf16 v[16:19], a[212:215], a[4:7], v[16:19]\n"
    "ds_read_b128 a[192:195], v0 offset:32768\n"
    "v_mul_f32 v115, s52, v115\n"
    "v_mfma_f32_16x16x32_bf16 v[16:19], a[216:219], a[8:11], v[16:19]\n"
    "s_add_u32 s57, s53, 0x34000\n"
    "v_exp_f32 v114, v114\n"
    "v_mfma_f32_16x16x32_bf16 v[16:19], a[220:223], a[12:15], v[16:19]\n"
    "s_add_u32 m0, s56, 0x4000\n"
    "v_exp_f32 v115, v115\n"
    "v_mfma_f32_16x16x32_bf16 v[16:19], a[224:227], a[16:19], v[16:19]\n"
    "ds_read_b128 a[196:199], v0 offset:33280\n"
    "v_cvt_pk_bf16_f32 v113, v114, v115\n"
    "v_mfma_f32_16x16x32_bf16 v[16:19], a[228:231], a[20:23], v[16:19]\n"
    "buffer_load_dwordx4 v3, s[36:39], s57 offen offset:0 lds\n"
    "v_mfma_f32_16x16x32_bf16 v[16:19], a[232:235], a[24:27], v[16:19]\n"
    "v_mul_f32 v116, s52, v116\n"
    "v_mul_f32 v117, s52, v117\n"
    "v_mfma_f32_16x16x32_bf16 v[16:19], a[236:239], a[28:31], v[16:19]\n"
    "ds_read_b128 a[200:203], v0 offset:33792\n"
    "v_mul_f32 v118, s52, v118\n"
    "v_mfma_f32_16x16x32_bf16 v[20:23], a[236:239], a[60:63], 0\n"
    "v_exp_f32 v116, v116\n"
    "v_mfma_f32_16x16x32_bf16 v[20:23], a[232:235], a[56:59], v[20:23]\n"
    "buffer_load_dword a177, v11, s[40:43], s88 offen\n"
    "v_mfma_f32_16x16x32_bf16 v[20:23], a[228:231], a[52:55], v[20:23]\n"
    "ds_read_b128 a[204:207], v0 offset:34304\n"
    "v_mul_f32 v119, s52, v119\n"
    "v_mfma_f32_16x16x32_bf16 v[20:23], a[224:227], a[48:51], v[20:23]\n"
    "s_add_u32 s88, s88, 0x2000\n"
    "v_exp_f32 v117, v117\n"
    "v_mfma_f32_16x16x32_bf16 v[20:23], a[220:223], a[44:47], v[20:23]\n"
    "s_add_u32 m0, s56, 0x4380\n"
    "v_cvt_pk_bf16_f32 v116, v116, v117\n"
    "v_mul_f32 v120, s52, v120\n"
    "v_mfma_f32_16x16x32_bf16 v[20:23], a[216:219], a[40:43], v[20:23]\n"
    "ds_read_b128 a[160:163], v0 offset:34816\n"
    "v_mul_f32 v121, s52, v121\n"
    "v_mfma_f32_16x16x32_bf16 v[20:23], a[212:215], a[36:39], v[20:23]\n"
    "buffer_load_dwordx4 v3, s[36:39], s57 offen offset:128 lds\n"
    "v_mfma_f32_16x16x32_bf16 v[20:23], a[208:211], a[32:35], v[20:23]\n"
    "v_exp_f32 v118, v118\n"
    "v_mfma_f32_16x16x32_bf16 v[24:27], a[208:211], a[64:67], 0\n"
    "ds_read_b128 a[164:167], v0 offset:35328\n"
    "v_mul_f32 v122, s52, v122\n"
    "v_mfma_f32_16x16x32_bf16 v[24:27], a[212:215], a[68:71], v[24:27]\n"
    "v_exp_f32 v119, v119\n"
    "v_mfma_f32_16x16x32_bf16 v[24:27], a[216:219], a[72:75], v[24:27]\n"
    "v_cvt_pk_bf16_f32 v117, v118, v119\n"
    "v_mul_f32 v123, s52, v123\n"
    "v_mfma_f32_16x16x32_bf16 v[24:27], a[220:223], a[76:79], v[24:27]\n"
    "ds_read_b128 a[168:171], v0 offset:35840\n"
    "v_mul_f32 v124, s52, v124\n"
    "v_mfma_f32_16x16x32_bf16 v[24:27], a[224:227], a[80:83], v[24:27]\n"
    "v_exp_f32 v120, v120\n"
    "v_mfma_f32_16x16x32_bf16 v[24:27], a[228:231], a[84:87], v[24:27]\n"
    "v_exp_f32 v121, v121\n"
    "v_mfma_f32_16x16x32_bf16 v[24:27], a[232:235], a[88:91], v[24:27]\n"
    "ds_read_b128 a[172:175], v0 offset:36352\n"
    "v_cvt_pk_bf16_f32 v120, v120, v121\n"
    "v_mfma_f32_16x16x32_bf16 v[24:27], a[236:239], a[92:95], v[24:27]\n"
    "s_add_u32 m0, s56, 0x4700\n"
    "v_exp_f32 v122, v122\n"
    "v_mfma_f32_16x16x32_bf16 v[28:31], a[236:239], a[124:127], 0\n"
    "buffer_load_dwordx4 v3, s[36:39], s57 offen offset:256 lds\n"
    "v_mfma_f32_16x16x32_bf16 v[28:31], a[232:235], a[120:123], v[28:31]\n"
    "v_exp_f32 v123, v123\n"
    "v_mfma_f32_16x16x32_bf16 v[28:31], a[228:231], a[116:119], v[28:31]\n"
    "v_cvt_pk_bf16_f32 v121, v122, v123\n"
    "v_mul_f32 v125, s52, v125\n"
    "v_mfma_f32_16x16x32_bf16 v[28:31], a[224:227], a[112:115], v[28:31]\n"
    "v_exp_f32 v124, v124\n"
    "v_mfma_f32_16x16x32_bf16 v[28:31], a[220:223], a[108:111], v[28:31]\n"
    "v_exp_f32 v125, v125\n"
    "v_mfma_f32_16x16x32_bf16 v[28:31], a[216:219], a[104:107], v[28:31]\n"
    "v_mul_f32 v126, s52, v126\n"
    "v_cvt_pk_bf16_f32 v124, v124, v125\n"
    "v_mfma_f32_16x16x32_bf16 v[28:31], a[212:215], a[100:103], v[28:31]\n"
    "v_exp_f32 v126, v126\n"
    "v_mfma_f32_16x16x32_bf16 v[28:31], a[208:211], a[96:99], v[28:31]\n"
    "v_mul_f32 v127, s52, v127\n"
    "v_mul_f32 v16, s52, v16\n"
    "v_mfma_f32_16x16x32_bf16 a[248:251], v[12:15], v[88:91], a[248:251]\n"
    "s_add_u32 m0, s56, 0x4a80\n"
    "v_exp_f32 v127, v127\n"
    "v_mfma_f32_16x16x32_bf16 a[252:255], v[12:15], v[92:95], a[252:255]\n"
    "s_waitcnt lgkmcnt(0)\n"
    "v_cvt_pk_bf16_f32 v125, v126, v127\n"
    "v_mul_f32 v17, s52, v17\n"
    "v_mfma_f32_16x16x32_bf16 v[128:131], a[192:195], a[0:3], 0\n"
    "buffer_load_dwordx4 v3, s[36:39], s57 offen offset:384 lds\n"
    "v_mfma_f32_16x16x32_bf16 v[128:131], a[196:199], a[4:7], v[128:131]\n"
    "ds_read_b128 a[208:211], v0 offset:40960\n"
    "v_mul_f32 v18, s52, v18\n"
    "v_mfma_f32_16x16x32_bf16 v[128:131], a[200:203], a[8:11], v[128:131]\n"
    "v_exp_f32 v16, v16\n"
    "v_mfma_f32_16x16x32_bf16 v[128:131], a[204:207], a[12:15], v[128:131]\n"
    "v_exp_f32 v17, v17\n"
    "v_mfma_f32_16x16x32_bf16 v[128:131], a[160:163], a[16:19], v[128:131]\n"
    "ds_read_b128 a[212:215], v0 offset:41472\n"
    "v_cvt_pk_bf16_f32 v114, v16, v17\n"
    "v_mfma_f32_16x16x32_bf16 v[128:131], a[164:167], a[20:23], v[128:131]\n"
    "v_exp_f32 v18, v18\n"
    "v_mfma_f32_16x16x32_bf16 v[128:131], a[168:171], a[24:27], v[128:131]\n"
    "v_mul_f32 v19, s52, v19\n"
    "v_mul_f32 v20, s52, v20\n"
    "v_mfma_f32_16x16x32_bf16 v[128:131], a[172:175], a[28:31], v[128:131]\n"
    "ds_read_b128 a[216:219], v0 offset:41984\n"
    "v_mul_f32 v21, s52, v21\n"
    "v_mfma_f32_16x16x32_bf16 v[132:135], a[172:175], a[60:63], 0\n"
    "s_add_u32 s57, s55, 0x2000\n"
    "v_exp_f32 v19, v19\n"
    "v_mfma_f32_16x16x32_bf16 v[132:135], a[168:171], a[56:59], v[132:135]\n"
    "s_add_u32 m0, s56, 0x18700\n"
    "v_cvt_pk_bf16_f32 v115, v18, v19\n"
    "v_mul_f32 v22, s52, v22\n"
    "v_mfma_f32_16x16x32_bf16 v[132:135], a[164:167], a[52:55], v[132:135]\n"
    "ds_read_b128 a[220:223], v0 offset:42496\n"
    "v_mul_f32 v23, s52, v23\n"
    "v_mfma_f32_16x16x32_bf16 v[132:135], a[160:163], a[48:51], v[132:135]\n"
    "buffer_load_dwordx4 v3, s[44:47], s57 offen offset:256 nt lds\n"
    "v_mfma_f32_16x16x32_bf16 v[132:135], a[204:207], a[44:47], v[132:135]\n"
    "v_exp_f32 v20, v20\n"
    "v_mfma_f32_16x16x32_bf16 v[132:135], a[200:203], a[40:43], v[132:135]\n"
    "ds_read_b128 a[224:227], v0 offset:43008\n"
    "v_mul_f32 v24, s52, v24\n"
    "v_mfma_f32_16x16x32_bf16 v[132:135], a[196:199], a[36:39], v[132:135]\n"
    "v_exp_f32 v21, v21\n"
    "v_mfma_f32_16x16x32_bf16 v[132:135], a[192:195], a[32:35], v[132:135]\n"
    "v_cvt_pk_bf16_f32 v118, v20, v21\n"
    "v_mul_f32 v25, s52, v25\n"
    "v_mfma_f32_16x16x32_bf16 v[136:139], a[192:195], a[64:67], 0\n"
    "ds_read_b128 a[228:231], v0 offset:43520\n"
    "v_mul_f32 v26, s52, v26\n"
    "v_mfma_f32_16x16x32_bf16 v[136:139], a[196:199], a[68:71], v[136:139]\n"
    "v_exp_f32 v22, v22\n"
    "v_mfma_f32_16x16x32_bf16 v[136:139], a[200:203], a[72:75], v[136:139]\n"
    "s_add_u32 m0, s56, 0x18a80\n"
    "v_exp_f32 v23, v23\n"
    "v_mfma_f32_16x16x32_bf16 v[136:139], a[204:207], a[76:79], v[136:139]\n"
    "ds_read_b128 a[232:235], v0 offset:44032\n"
    "v_cvt_pk_bf16_f32 v119, v22, v23\n"
    "v_mfma_f32_16x16x32_bf16 v[136:139], a[160:163], a[80:83], v[136:139]\n"
    "buffer_load_dwordx4 v3, s[44:47], s57 offen offset:384 nt lds\n"
    "v_mfma_f32_16x16x32_bf16 v[136:139], a[164:167], a[84:87], v[136:139]\n"
    "v_exp_f32 v24, v24\n"
    "v_mfma_f32_16x16x32_bf16 v[136:139], a[168:171], a[88:91], v[136:139]\n"
    "ds_read_b128 a[236:239], v0 offset:44544\n"
    "v_mul_f32 v27, s52, v27\n"
    "v_mfma_f32_16x16x32_bf16 v[136:139], a[172:175], a[92:95], v[136:139]\n"
    "v_exp_f32 v25, v25\n"
    "v_mfma_f32_16x16x32_bf16 v[140:143], a[172:175], a[124:127], 0\n"
    "v_cvt_pk_bf16_f32 v122, v24, v25\n"
    "v_mul_f32 v28, s52, v28\n"
    "v_mfma_f32_16x16x32_bf16 v[140:143], a[168:171], a[120:123], v[140:143]\n"
    "v_exp_f32 v26, v26\n"
    "v_mfma_f32_16x16x32_bf16 v[140:143], a[164:167], a[116:119], v[140:143]\n"
    "v_exp_f32 v27, v27\n"
    "v_mfma_f32_16x16x32_bf16 v[140:143], a[160:163], a[112:115], v[140:143]\n"
    "v_cvt_pk_bf16_f32 v123, v26, v27\n"
    "v_mul_f32 v29, s52, v29\n"
    "v_mfma_f32_16x16x32_bf16 v[140:143], a[204:207], a[108:111], v[140:143]\n"
    "v_exp_f32 v28, v28\n"
    "v_mfma_f32_16x16x32_bf16 v[140:143], a[200:203], a[104:107], v[140:143]\n"
    "v_exp_f32 v29, v29\n"
    "v_mfma_f32_16x16x32_bf16 v[140:143], a[196:199], a[100:103], v[140:143]\n"
    "v_mul_f32 v30, s52, v30\n"
    "v_cvt_pk_bf16_f32 v126, v28, v29\n"
    "v_mfma_f32_16x16x32_bf16 v[140:143], a[192:195], a[96:99], v[140:143]\n"
    "v_exp_f32 v30, v30\n"
    "v_mfma_f32_16x16x32_bf16 a[240:243], v[12:15], v[96:99], a[240:243]\n"
    "v_mul_f32 v31, s52, v31\n"
    "v_mul_f32 v128, s52, v128\n"
    "v_mfma_f32_16x16x32_bf16 a[244:247], v[12:15], v[100:103], a[244:247]\n"
    "s_waitcnt vmcnt(15) lgkmcnt(0)\n"
    "v_exp_f32 v31, v31\n"
    "v_mfma_f32_16x16x32_bf16 v[16:19], a[208:211], a[0:3], 0\n"
    "s_barrier\n"
    "v_cvt_pk_bf16_f32 v127, v30, v31\n"
    "v_mul_f32 v129, s52, v129\n"
    "v_mfma_f32_16x16x32_bf16 v[16:19], a[212:215], a[4:7], v[16:19]\n"
    "ds_read_b128 a[192:195], v0 offset:49152\n"
    "v_mul_f32 v130, s52, v130\n"
    "v_mfma_f32_16x16x32_bf16 v[16:19], a[216:219], a[8:11], v[16:19]\n"
    "s_add_u32 s57, s53, 0x38000\n"
    "v_exp_f32 v128, v128\n"
    "v_mfma_f32_16x16x32_bf16 v[16:19], a[220:223], a[12:15], v[16:19]\n"
    "s_add_u32 m0, s56, 0x8000\n"
    "v_exp_f32 v129, v129\n"
    "v_mfma_f32_16x16x32_bf16 v[16:19], a[224:227], a[16:19], v[16:19]\n"
    "ds_read_b128 a[196:199], v0 offset:49664\n"
    "v_cvt_pk_bf16_f32 v128, v128, v129\n"
    "v_mfma_f32_16x16x32_bf16 v[16:19], a[228:231], a[20:23], v[16:19]\n"
    "buffer_load_dwordx4 v3, s[36:39], s57 offen offset:0 lds\n"
    "v_mfma_f32_16x16x32_bf16 v[16:19], a[232:235], a[24:27], v[16:19]\n"
    "v_exp_f32 v130, v130\n"
    "v_mfma_f32_16x16x32_bf16 v[16:19], a[236:239], a[28:31], v[16:19]\n"
    "ds_read_b128 a[200:203], v0 offset:50176\n"
    "v_mul_f32 v131, s52, v131\n"
    "v_mfma_f32_16x16x32_bf16 v[20:23], a[236:239], a[60:63], 0\n"
    "v_exp_f32 v131, v131\n"
    "v_mfma_f32_16x16x32_bf16 v[20:23], a[232:235], a[56:59], v[20:23]\n"
    "v_cvt_pk_bf16_f32 v129, v130, v131\n"
    "v_mul_f32 v132, s52, v132\n"
    "v_mfma_f32_16x16x32_bf16 v[20:23], a[228:231], a[52:55], v[20:23]\n"
    "ds_read_b128 a[204:207], v0 offset:50688\n"
    "v_mul_f32 v133, s52, v133\n"
    "v_mfma_f32_16x16x32_bf16 v[20:23], a[224:227], a[48:51], v[20:23]\n"
    "v_exp_f32 v132, v132\n"
    "v_mfma_f32_16x16x32_bf16 v[20:23], a[220:223], a[44:47], v[20:23]\n"
    "s_add_u32 m0, s56, 0x8380\n"
    "v_exp_f32 v133, v133\n"
    "v_mfma_f32_16x16x32_bf16 v[20:23], a[216:219], a[40:43], v[20:23]\n"
    "ds_read_b128 a[160:163], v0 offset:51200\n"
    "v_mul_f32 v134, s52, v134\n"
    "v_mfma_f32_16x16x32_bf16 v[20:23], a[212:215], a[36:39], v[20:23]\n"
    "buffer_load_dwordx4 v3, s[36:39], s57 offen offset:128 lds\n"
    "v_mfma_f32_16x16x32_bf16 v[20:23], a[208:211], a[32:35], v[20:23]\n"
    "v_cvt_pk_bf16_f32 v132, v132, v133\n"
    "v_mul_f32 v135, s52, v135\n"
    "v_mfma_f32_16x16x32_bf16 v[24:27], a[208:211], a[64:67], 0\n"
    "ds_read_b128 a[164:167], v0 offset:51712\n"
    "v_mul_f32 v136, s52, v136\n"
    "v_mfma_f32_16x16x32_bf16 v[24:27], a[212:215], a[68:71], v[24:27]\n"
    "v_exp_f32 v134, v134\n"
    "v_mfma_f32_16x16x32_bf16 v[24:27], a[216:219], a[72:75], v[24:27]\n"
    "v_exp_f32 v135, v135\n"
    "v_mfma_f32_16x16x32_bf16 v[24:27], a[220:223], a[76:79], v[24:27]\n"
    "ds_read_b128 a[168:171], v0 offset:52224\n"
    "v_cvt_pk_bf16_f32 v133, v134, v135\n"
    "v_mfma_f32_16x16x32_bf16 v[24:27], a[224:227], a[80:83], v[24:27]\n"
    "v_exp_f32 v136, v136\n"
    "v_mfma_f32_16x16x32_bf16 v[24:27], a[228:231], a[84:87], v[24:27]\n"
    "v_mul_f32 v137, s52, v137\n"
    "v_mul_f32 v138, s52, v138\n"
    "v_mfma_f32_16x16x32_bf16 v[24:27], a[232:235], a[88:91], v[24:27]\n"
    "ds_read_b128 a[172:175], v0 offset:52736\n"
    "v_mul_f32 v139, s52, v139\n"
    "v_mfma_f32_16x16x32_bf16 v[24:27], a[236:239], a[92:95], v[24:27]\n"
    "s_add_u32 m0, s56, 0x8700\n"
    "v_exp_f32 v137, v137\n"
    "v_mfma_f32_16x16x32_bf16 v[28:31], a[236:239], a[124:127], 0\n"
    "buffer_load_dwordx4 v3, s[36:39], s57 offen offset:256 lds\n"
    "v_mfma_f32_16x16x32_bf16 v[28:31], a[232:235], a[120:123], v[28:31]\n"
    "v_cvt_pk_bf16_f32 v136, v136, v137\n"
    "v_mul_f32 v140, s52, v140\n"
    "v_mfma_f32_16x16x32_bf16 v[28:31], a[228:231], a[116:119], v[28:31]\n"
    "v_exp_f32 v138, v138\n"
    "v_mfma_f32_16x16x32_bf16 v[28:31], a[224:227], a[112:115], v[28:31]\n"
    "v_exp_f32 v139, v139\n"
    "v_mfma_f32_16x16x32_bf16 v[28:31], a[220:223], a[108:111], v[28:31]\n"
    "v_cvt_pk_bf16_f32 v137, v138, v139\n"
    "v_mul_f32 v141, s52, v141\n"
    "v_mfma_f32_16x16x32_bf16 v[28:31], a[216:219], a[104:107], v[28:31]\n"
    "v_exp_f32 v140, v140\n"
    "v_mfma_f32_16x16x32_bf16 v[28:31], a[212:215], a[100:103], v[28:31]\n"
    "v_exp_f32 v141, v141\n"
    "v_mfma_f32_16x16x32_bf16 v[28:31], a[208:211], a[96:99], v[28:31]\n"
    "v_mul_f32 v142, s52, v142\n"
    "v_cvt_pk_bf16_f32 v140, v140, v141\n"
    "v_mfma_f32_16x16x32_bf16 a[248:251], v[12:15], v[104:107], a[248:251]\n"
    "s_add_u32 m0, s56, 0x8a80\n"
    "v_exp_f32 v142, v142\n"
    "v_mfma_f32_16x16x32_bf16 a[252:255], v[12:15], v[108:111], a[252:255]\n"
    "s_waitcnt lgkmcnt(0)\n"
    "v_mul_f32 v143, s52, v143\n"
    "v_mul_f32 v16, s52, v16\n"
    "v_mfma_f32_16x16x32_bf16 v[144:147], a[192:195], a[0:3], 0\n"
    "buffer_load_dwordx4 v3, s[36:39], s57 offen offset:384 lds\n"
    "v_mfma_f32_16x16x32_bf16 v[144:147], a[196:199], a[4:7], v[144:147]\n"
    "ds_read_b128 a[208:211], v0 offset:57344\n"
    "v_mul_f32 v17, s52, v17\n"
    "v_mfma_f32_16x16x32_bf16 v[144:147], a[200:203], a[8:11], v[144:147]\n"
    "v_exp_f32 v143, v143\n"
    "v_mfma_f32_16x16x32_bf16 v[144:147], a[204:207], a[12:15], v[144:147]\n"
    "v_cvt_pk_bf16_f32 v141, v142, v143\n"
    "v_mul_f32 v18, s52, v18\n"
    "v_mfma_f32_16x16x32_bf16 v[144:147], a[160:163], a[16:19], v[144:147]\n"
    "ds_read_b128 a[212:215], v0 offset:57856\n"
    "v_mul_f32 v19, s52, v19\n"
    "v_mfma_f32_16x16x32_bf16 v[144:147], a[164:167], a[20:23], v[144:147]\n"
    "v_exp_f32 v16, v16\n"
    "v_mfma_f32_16x16x32_bf16 v[144:147], a[168:171], a[24:27], v[144:147]\n"
    "v_exp_f32 v17, v17\n"
    "v_mfma_f32_16x16x32_bf16 v[144:147], a[172:175], a[28:31], v[144:147]\n"
    "ds_read_b128 a[216:219], v0 offset:58368\n"
    "v_cvt_pk_bf16_f32 v130, v16, v17\n"
    "v_mfma_f32_16x16x32_bf16 v[148:151], a[172:175], a[60:63], 0\n"
    "s_add_u32 s57, s55, 0x12000\n"
    "v_exp_f32 v18, v18\n"
    "v_mfma_f32_16x16x32_bf16 v[148:151], a[168:171], a[56:59], v[148:151]\n"
    "s_add_u32 m0, s56, 0x1c000\n"
    "v_exp_f32 v19, v19\n"
    "v_mfma_f32_16x16x32_bf16 v[148:151], a[164:167], a[52:55], v[148:151]\n"
    "ds_read_b128 a[220:223], v0 offset:58880\n"
    "v_cvt_pk_bf16_f32 v131, v18, v19\n"
    "v_mfma_f32_16x16x32_bf16 v[148:151], a[160:163], a[48:51], v[148:151]\n"
    "buffer_load_dwordx4 v3, s[44:47], s57 offen offset:0 nt lds\n"
    "v_mfma_f32_16x16x32_bf16 v[148:151], a[204:207], a[44:47], v[148:151]\n"
    "v_mul_f32 v20, s52, v20\n"
    "v_mul_f32 v21, s52, v21\n"
    "v_mfma_f32_16x16x32_bf16 v[148:151], a[200:203], a[40:43], v[148:151]\n"
    "ds_read_b128 a[224:227], v0 offset:59392\n"
    "v_mul_f32 v22, s52, v22\n"
    "v_mfma_f32_16x16x32_bf16 v[148:151], a[196:199], a[36:39], v[148:151]\n"
    "v_exp_f32 v20, v20\n"
    "v_mfma_f32_16x16x32_bf16 v[148:151], a[192:195], a[32:35], v[148:151]\n"
    "v_exp_f32 v21, v21\n"
    "v_mfma_f32_16x16x32_bf16 v[152:155], a[192:195], a[64:67], 0\n"
    "ds_read_b128 a[228:231], v0 offset:59904\n"
    "v_cvt_pk_bf16_f32 v134, v20, v21\n"
    "v_mfma_f32_16x16x32_bf16 v[152:155], a[196:199], a[68:71], v[152:155]\n"
    "v_exp_f32 v22, v22\n"
    "v_mfma_f32_16x16x32_bf16 v[152:155], a[200:203], a[72:75], v[152:155]\n"
    "s_add_u32 m0, s56, 0x1c380\n"
    "v_mul_f32 v23, s52, v23\n"
    "v_mul_f32 v24, s52, v24\n"
    "v_mfma_f32_16x16x32_bf16 v[152:155], a[204:207], a[76:79], v[152:155]\n"
    "ds_read_b128 a[232:235], v0 offset:60416\n"
    "v_mul_f32 v25, s52, v25\n"
    "v_mfma_f32_16x16x32_bf16 v[152:155], a[160:163], a[80:83], v[152:155]\n"
    "buffer_load_dwordx4 v3, s[44:47], s57 offen offset:128 nt lds\n"
    "v_mfma_f32_16x16x32_bf16 v[152:155], a[164:167], a[84:87], v[152:155]\n"
    "v_exp_f32 v23, v23\n"
    "v_mfma_f32_16x16x32_bf16 v[152:155], a[168:171], a[88:91], v[152:155]\n"
    "ds_read_b128 a[236:239], v0 offset:60928\n"
    "v_cvt_pk_bf16_f32 v135, v22, v23\n"
    "v_mfma_f32_16x16x32_bf16 v[152:155], a[172:175], a[92:95], v[152:155]\n"
    "v_exp_f32 v24, v24\n"
    "v_mfma_f32_16x16x32_bf16 v[156:159], a[172:175], a[124:127], 0\n"
    "v_exp_f32 v25, v25\n"
    "v_mfma_f32_16x16x32_bf16 v[156:159], a[168:171], a[120:123], v[156:159]\n"
    "v_mul_f32 v26, s52, v26\n"
    "v_cvt_pk_bf16_f32 v138, v24, v25\n"
    "v_mfma_f32_16x16x32_bf16 v[156:159], a[164:167], a[116:119], v[156:159]\n"
    "v_exp_f32 v26, v26\n"
    "v_mfma_f32_16x16x32_bf16 v[156:159], a[160:163], a[112:115], v[156:159]\n"
    "v_mul_f32 v27, s52, v27\n"
    "v_mul_f32 v28, s52, v28\n"
    "v_mfma_f32_16x16x32_bf16 v[156:159], a[204:207], a[108:111], v[156:159]\n"
    "v_exp_f32 v27, v27\n"
    "v_mfma_f32_16x16x32_bf16 v[156:159], a[200:203], a[104:107], v[156:159]\n"
    "v_cvt_pk_bf16_f32 v139, v26, v27\n"
    "v_mul_f32 v29, s52, v29\n"
    "v_mfma_f32_16x16x32_bf16 v[156:159], a[196:199], a[100:103], v[156:159]\n"
    "v_exp_f32 v28, v28\n"
    "v_mfma_f32_16x16x32_bf16 v[156:159], a[192:195], a[96:99], v[156:159]\n"
    "v_exp_f32 v29, v29\n"
    "v_mfma_f32_16x16x32_bf16 a[240:243], v[12:15], v[112:115], a[240:243]\n"
    "v_mul_f32 v30, s52, v30\n"
    "v_cvt_pk_bf16_f32 v142, v28, v29\n"
    "v_mfma_f32_16x16x32_bf16 a[244:247], v[12:15], v[116:119], a[244:247]\n"
    "s_waitcnt vmcnt(15) lgkmcnt(0)\n"
    "v_exp_f32 v30, v30\n"
    "v_mfma_f32_16x16x32_bf16 v[16:19], a[208:211], a[0:3], 0\n"
    "s_barrier\n"
    "v_mul_f32 v31, s52, v31\n"
    "v_mul_f32 v144, s52, v144\n"
    "v_mfma_f32_16x16x32_bf16 v[16:19], a[212:215], a[4:7], v[16:19]\n"
    "ds_read_b128 a[192:195], v0 offset:0\n"
    "v_mul_f32 v145, s52, v145\n"
    "v_mfma_f32_16x16x32_bf16 v[16:19], a[216:219], a[8:11], v[16:19]\n"
    "s_add_u32 s57, s53, 0x3c000\n"
    "v_exp_f32 v31, v31\n"
    "v_mfma_f32_16x16x32_bf16 v[16:19], a[220:223], a[12:15], v[16:19]\n"
    "s_add_u32 m0, s56, 0xc000\n"
    "v_cvt_pk_bf16_f32 v143, v30, v31\n"
    "v_mul_f32 v146, s52, v146\n"
    "v_mfma_f32_16x16x32_bf16 v[16:19], a[224:227], a[16:19], v[16:19]\n"
    "ds_read_b128 a[196:199], v0 offset:512\n"
    "v_mul_f32 v147, s52, v147\n"
    "v_mfma_f32_16x16x32_bf16 v[16:19], a[228:231], a[20:23], v[16:19]\n"
    "buffer_load_dwordx4 v3, s[36:39], s57 offen offset:0 lds\n"
    "v_mfma_f32_16x16x32_bf16 v[16:19], a[232:235], a[24:27], v[16:19]\n"
    "v_exp_f32 v144, v144\n"
    "v_mfma_f32_16x16x32_bf16 v[16:19], a[236:239], a[28:31], v[16:19]\n"
    "ds_read_b128 a[200:203], v0 offset:1024\n"
    "v_mul_f32 v148, s52, v148\n"
    "v_mfma_f32_16x16x32_bf16 v[20:23], a[236:239], a[60:63], 0\n"
    "v_exp_f32 v145, v145\n"
    "v_mfma_f32_16x16x32_bf16 v[20:23], a[232:235], a[56:59], v[20:23]\n"
    "v_cvt_pk_bf16_f32 v144, v144, v145\n"
    "v_mul_f32 v149, s52, v149\n"
    "v_mfma_f32_16x16x32_bf16 v[20:23], a[228:231], a[52:55], v[20:23]\n"
    "ds_read_b128 a[204:207], v0 offset:1536\n"
    "v_mul_f32 v150, s52, v150\n"
    "v_mfma_f32_16x16x32_bf16 v[20:23], a[224:227], a[48:51], v[20:23]\n"
    "v_exp_f32 v146, v146\n"
    "v_mfma_f32_16x16x32_bf16 v[20:23], a[220:223], a[44:47], v[20:23]\n"
    "s_add_u32 m0, s56, 0xc380\n"
    "v_exp_f32 v147, v147\n"
    "v_mfma_f32_16x16x32_bf16 v[20:23], a[216:219], a[40:43], v[20:23]\n"
    "ds_read_b128 a[160:163], v0 offset:2048\n"
    "v_cvt_pk_bf16_f32 v145, v146, v147\n"
    "v_mfma_f32_16x16x32_bf16 v[20:23], a[212:215], a[36:39], v[20:23]\n"
    "buffer_load_dwordx4 v3, s[36:39], s57 offen offset:128 lds\n"
    "v_mfma_f32_16x16x32_bf16 v[20:23], a[208:211], a[32:35], v[20:23]\n"
    "v_exp_f32 v148, v148\n"
    "v_mfma_f32_16x16x32_bf16 v[24:27], a[208:211], a[64:67], 0\n"
    "ds_read_b128 a[164:167], v0 offset:2560\n"
    "v_mul_f32 v151, s52, v151\n"
    "v_mfma_f32_16x16x32_bf16 v[24:27], a[212:215], a[68:71], v[24:27]\n"
    "v_exp_f32 v149, v149\n"
    "v_mfma_f32_16x16x32_bf16 v[24:27], a[216:219], a[72:75], v[24:27]\n"
    "v_cvt_pk_bf16_f32 v148, v148, v149\n"
    "v_mul_f32 v152, s52, v152\n"
    "v_mfma_f32_16x16x32_bf16 v[24:27], a[220:223], a[76:79], v[24:27]\n"
    "ds_read_b128 a[168:171], v0 offset:3072\n"
    "v_mul_f32 v153, s52, v153\n"
    "v_mfma_f32_16x16x32_bf16 v[24:27], a[224:227], a[80:83], v[24:27]\n"
    "v_exp_f32 v150, v150\n"
    "v_mfma_f32_16x16x32_bf16 v[24:27], a[228:231], a[84:87], v[24:27]\n"
    "v_exp_f32 v151, v151\n"
    "v_mfma_f32_16x16x32_bf16 v[24:27], a[232:235], a[88:91], v[24:27]\n"
    "ds_read_b128 a[172:175], v0 offset:3584\n"
    "v_cvt_pk_bf16_f32 v149, v150, v151\n"
    "v_mfma_f32_16x16x32_bf16 v[24:27], a[236:239], a[92:95], v[24:27]\n"
    "s_add_u32 m0, s56, 0xc700\n"
    "v_exp_f32 v152, v152\n"
    "v_mfma_f32_16x16x32_bf16 v[28:31], a[236:239], a[124:127], 0\n"
    "buffer_load_dwordx4 v3, s[36:39], s57 offen offset:256 lds\n"
    "v_mfma_f32_16x16x32_bf16 v[28:31], a[232:235], a[120:123], v[28:31]\n"
    "v_exp_f32 v153, v153\n"
    "v_mfma_f32_16x16x32_bf16 v[28:31], a[228:231], a[116:119], v[28:31]\n"
    "v_mul_f32 v154, s52, v154\n"
    "v_cvt_pk_bf16_f32 v152, v152, v153\n"
    "v_mfma_f32_16x16x32_bf16 v[28:31], a[224:227], a[112:115], v[28:31]\n"
    "v_exp_f32 v154, v154\n"
    "v_mfma_f32_16x16x32_bf16 v[28:31], a[220:223], a[108:111], v[28:31]\n"
    "v_mul_f32 v155, s52, v155\n"
    "v_mul_f32 v156, s52, v156\n"
    "v_mfma_f32_16x16x32_bf16 v[28:31], a[216:219], a[104:107], v[28:31]\n"
    "v_exp_f32 v155, v155\n"
    "v_mfma_f32_16x16x32_bf16 v[28:31], a[212:215], a[100:103], v[28:31]\n"
    "v_cvt_pk_bf16_f32 v153, v154, v155\n"
    "v_mul_f32 v157, s52, v157\n"
    "v_mfma_f32_16x16x32_bf16 v[28:31], a[208:211], a[96:99], v[28:31]\n"
    "v_exp_f32 v156, v156\n"
    "v_mfma_f32_16x16x32_bf16 a[248:251], v[12:15], v[120:123], a[248:251]\n"
    "s_add_u32 m0, s56, 0xca80\n"
    "v_exp_f32 v157, v157\n"
    "v_mfma_f32_16x16x32_bf16 a[252:255], v[12:15], v[124:127], a[252:255]\n"
    "s_waitcnt lgkmcnt(0)\n"
    "v_mul_f32 v158, s52, v158\n"
    "v_cvt_pk_bf16_f32 v156, v156, v157\n"
    "v_mfma_f32_16x16x32_bf16 v[192:195], a[192:195], a[0:3], 0\n"
    "buffer_load_dwordx4 v3, s[36:39], s57 offen offset:384 lds\n"
    "v_mfma_f32_16x16x32_bf16 v[192:195], a[196:199], a[4:7], v[192:195]\n"
    "ds_read_b128 a[208:211], v0 offset:8192\n"
    "v_mul_f32 v159, s52, v159\n"
    "v_mfma_f32_16x16x32_bf16 v[192:195], a[200:203], a[8:11], v[192:195]\n"
    "v_exp_f32 v158, v158\n"
    "v_mfma_f32_16x16x32_bf16 v[192:195], a[204:207], a[12:15], v[192:195]\n"
    "v_exp_f32 v159, v159\n"
    "v_mfma_f32_16x16x32_bf16 v[192:195], a[160:163], a[16:19], v[192:195]\n"
    "ds_read_b128 a[212:215], v0 offset:8704\n"
    "v_cvt_pk_bf16_f32 v157, v158, v159\n"
    "v_mfma_f32_16x16x32_bf16 v[192:195], a[164:167], a[20:23], v[192:195]\n"
    "buffer_load_dword a178, v11, s[40:43], s88 offen\n"
    "v_mfma_f32_16x16x32_bf16 v[192:195], a[168:171], a[24:27], v[192:195]\n"
    "v_mul_f32 v16, s52, v16\n"
    "v_mul_f32 v17, s52, v17\n"
    "v_mfma_f32_16x16x32_bf16 v[192:195], a[172:175], a[28:31], v[192:195]\n"
    "ds_read_b128 a[216:219], v0 offset:9216\n"
    "v_mul_f32 v18, s52, v18\n"
    "v_mfma_f32_16x16x32_bf16 v[196:199], a[172:175], a[60:63], 0\n"
    "s_add_u32 s57, s55, 0x12000\n"
    "v_exp_f32 v16, v16\n"
    "v_mfma_f32_16x16x32_bf16 v[196:199], a[168:171], a[56:59], v[196:199]\n"
    "s_add_u32 m0, s56, 0x1c700\n"
    "v_exp_f32 v17, v17\n"
    "v_mfma_f32_16x16x32_bf16 v[196:199], a[164:167], a[52:55], v[196:199]\n"
    "ds_read_b128 a[220:223], v0 offset:9728\n"
    "v_cvt_pk_bf16_f32 v146, v16, v17\n"
    "v_mfma_f32_16x16x32_bf16 v[196:199], a[160:163], a[48:51], v[196:199]\n"
    "buffer_load_dwordx4 v3, s[44:47], s57 offen offset:256 nt lds\n"
    "v_mfma_f32_16x16x32_bf16 v[196:199], a[204:207], a[44:47], v[196:199]\n"
    "v_exp_f32 v18, v18\n"
    "v_mfma_f32_16x16x32_bf16 v[196:199], a[200:203], a[40:43], v[196:199]\n"
    "ds_read_b128 a[224:227], v0 offset:10240\n"
    "v_mul_f32 v19, s52, v19\n"
    "v_mfma_f32_16x16x32_bf16 v[196:199], a[196:199], a[36:39], v[196:199]\n"
    "v_exp_f32 v19, v19\n"
    "v_mfma_f32_16x16x32_bf16 v[196:199], a[192:195], a[32:35], v[196:199]\n"
    "v_cvt_pk_bf16_f32 v147, v18, v19\n"
    "v_mul_f32 v20, s52, v20\n"
    "v_mfma_f32_16x16x32_bf16 v[200:203], a[192:195], a[64:67], 0\n"
    "ds_read_b128 a[228:231], v0 offset:10752\n"
    "v_mul_f32 v21, s52, v21\n"
    "v_mfma_f32_16x16x32_bf16 v[200:203], a[196:199], a[68:71], v[200:203]\n"
    "v_exp_f32 v20, v20\n"
    "v_mfma_f32_16x16x32_bf16 v[200:203], a[200:203], a[72:75], v[200:203]\n"
    "s_add_u32 m0, s56, 0x1ca80\n"
    "v_exp_f32 v21, v21\n"
    "v_mfma_f32_16x16x32_bf16 v[200:203], a[204:207], a[76:79], v[200:203]\n"
    "ds_read_b128 a[232:235], v0 offset:11264\n"
    "v_mul_f32 v22, s52, v22\n"
    "v_mfma_f32_16x16x32_bf16 v[200:203], a[160:163], a[80:83], v[200:203]\n"
    "buffer_load_dwordx4 v3, s[44:47], s57 offen offset:384 nt lds\n"
    "L_pad7_%=:\n"
    "s_cbranch_execz L_ret1_%=\n"
    "v_mfma_f32_16x16x32_bf16 v[200:203], a[164:167], a[84:87], v[200:203]\n"
    "v_cvt_pk_bf16_f32 v150, v20, v21\n"
    "v_mul_f32 v23, s52, v23\n"
    "v_mfma_f32_16x16x32_bf16 v[200:203], a[168:171], a[88:91], v[200:203]\n"
    "ds_read_b128 a[236:239], v0 offset:11776\n"
    "v_mul_f32 v24, s52, v24\n"
    "v_mfma_f32_16x16x32_bf16 v[200:203], a[172:175], a[92:95], v[200:203]\n"
    "v_exp_f32 v22, v22\n"
    "v_mfma_f32_16x16x32_bf16 v[204:207], a[172:175], a[124:127], 0\n"
    "v_exp_f32 v23, v23\n"
    "v_mfma_f32_16x16x32_bf16 v[204:207], a[168:171], a[120:123], v[204:207]\n"
    "v_cvt_pk_bf16_f32 v151, v22, v23\n"
    "v_mul_f32 v25, s52, v25\n"
    "v_mfma_f32_16x16x32_bf16 v[204:207], a[164:167], a[116:119], v[204:207]\n"
    "v_exp_f32 v24, v24\n"
    "v_mfma_f32_16x16x32_bf16 v[204:207], a[160:163], a[112:115], v[204:207]\n"
    "v_exp_f32 v25, v25\n"
    "v_mfma_f32_16x16x32_bf16 v[204:207], a[204:207], a[108:111], v[204:207]\n"
    "v_mul_f32 v26, s52, v26\n"
    "v_cvt_pk_bf16_f32 v154, v24, v25\n"
    "v_mfma_f32_16x16x32_bf16 v[204:207], a[200:203], a[104:107], v[204:207]\n"
    "v_exp_f32 v26, v26\n"
    "v_mfma_f32_16x16x32_bf16 v[204:207], a[196:199], a[100:103], v[204:207]\n"
    "v_mul_f32 v27, s52, v27\n"
    "v_mul_f32 v28, s52, v28\n"
    "v_mfma_f32_16x16x32_bf16 v[204:207], a[192:195], a[96:99], v[204:207]\n"
    "v_exp_f32 v27, v27\n"
    "v_mfma_f32_16x16x32_bf16 a[240:243], v[12:15], v[128:131], a[240:243]\n"
    "v_cvt_pk_bf16_f32 v155, v26, v27\n"
    "v_mul_f32 v29, s52, v29\n"
    "v_mfma_f32_16x16x32_bf16 a[244:247], v[12:15], v[132:135], a[244:247]\n"
    "s_waitcnt vmcnt(15) lgkmcnt(0)\n"
    "v_exp_f32 v28, v28\n"
    "v_mfma_f32_16x16x32_bf16 v[16:19], a[208:211], a[0:3], 0\n"
    "s_barrier\n"
    "v_exp_f32 v29, v29\n"
    "v_mfma_f32_16x16x32_bf16 v[16:19], a[212:215], a[4:7], v[16:19]\n"
    "ds_read_b128 a[192:195], v0 offset:16384\n"
    "v_mul_f32 v30, s52, v30\n"
    "v_mfma_f32_16x16x32_bf16 v[16:19], a[216:219], a[8:11], v[16:19]\n"
    "s_xor_b32 s57, s54, 0x0\n"
    "v_cvt_pk_bf16_f32 v158, v28, v29\n"
    "v_mul_f32 v31, s52, v31\n"
    "v_mfma_f32_16x16x32_bf16 v[16:19], a[220:223], a[12:15], v[16:19]\n"
    "s_add_u32 m0, s56, 0x0\n"
    "v_exp_f32 v30, v30\n"
    "v_mfma_f32_16x16x32_bf16 v[16:19], a[224:227], a[16:19], v[16:19]\n"
    "ds_read_b128 a[196:199], v0 offset:16896\n"
    "v_mul_f32 v192, s52, v192\n"
    "v_mfma_f32_16x16x32_bf16 v[16:19], a[228:231], a[20:23], v[16:19]\n"
    "buffer_load_dwordx4 v4, s[40:43], s57 offen offset:0 lds\n"
    "v_mfma_f32_16x16x32_bf16 v[16:19], a[232:235], a[24:27], v[16:19]\n"
    "v_exp_f32 v31, v31\n"
    "v_mfma_f32_16x16x32_bf16 v[16:19], a[236:239], a[28:31], v[16:19]\n"
    "ds_read_b128 a[200:203], v0 offset:17408\n"
    "v_cvt_pk_bf16_f32 v159, v30, v31\n"
    "v_mfma_f32_16x16x32_bf16 v[20:23], a[236:239], a[60:63], 0\n"
    "v_exp_f32 v192, v192\n"
    "v_mfma_f32_16x16x32_bf16 v[20:23], a[232:235], a[56:59], v[20:23]\n"
    "v_mul_f32 v193, s52, v193\n"
    "v_mul_f32 v194, s52, v194\n"
    "v_mfma_f32_16x16x32_bf16 v[20:23], a[228:231], a[52:55], v[20:23]\n"
    "ds_read_b128 a[204:207], v0 offset:17920\n"
    "v_mul_f32 v195, s52, v195\n"
    "v_mfma_f32_16x16x32_bf16 v[20:23], a[224:227], a[48:51], v[20:23]\n"
    "s_xor_b32 s57, s54, 0x1000\n"
    "v_exp_f32 v193, v193\n"
    "v_mfma_f32_16x16x32_bf16 v[20:23], a[220:223], a[44:47], v[20:23]\n"
    "s_add_u32 m0, s56, 0x400\n"
    "v_cvt_pk_bf16_f32 v192, v192, v193\n"
    "v_mul_f32 v196, s52, v196\n"
    "v_mfma_f32_16x16x32_bf16 v[20:23], a[216:219], a[40:43], v[20:23]\n"
    "ds_read_b128 a[160:163], v0 offset:18432\n"
    "v_mul_f32 v197, s52, v197\n"
    "v_mfma_f32_16x16x32_bf16 v[20:23], a[212:215], a[36:39], v[20:23]\n"
    "buffer_load_dwordx4 v4, s[40:43], s57 offen offset:0 lds\n"
    "v_mfma_f32_16x16x32_bf16 v[20:23], a[208:211], a[32:35], v[20:23]\n"
    "v_exp_f32 v194, v194\n"
    "v_mfma_f32_16x16x32_bf16 v[24:27], a[208:211], a[64:67], 0\n"
    "ds_read_b128 a[164:167], v0 offset:18944\n"
    "v_mul_f32 v198, s52, v198\n"
    "v_mfma_f32_16x16x32_bf16 v[24:27], a[212:215], a[68:71], v[24:27]\n"
    "v_exp_f32 v195, v195\n"
    "v_mfma_f32_16x16x32_bf16 v[24:27], a[216:219], a[72:75], v[24:27]\n"
    "v_cvt_pk_bf16_f32 v193, v194, v195\n"
    "v_mul_f32 v199, s52, v199\n"
    "v_mfma_f32_16x16x32_bf16 v[24:27], a[220:223], a[76:79], v[24:27]\n"
    "ds_read_b128 a[168:171], v0 offset:19456\n"
    "v_mul_f32 v200, s52, v200\n"
    "v_mfma_f32_16x16x32_bf16 v[24:27], a[224:227], a[80:83], v[24:27]\n"
    "v_exp_f32 v196, v196\n"
    "v_mfma_f32_16x16x32_bf16 v[24:27], a[228:231], a[84:87], v[24:27]\n"
    "s_xor_b32 s57, s54, 0x2000\n"
    "v_exp_f32 v197, v197\n"
    "v_mfma_f32_16x16x32_bf16 v[24:27], a[232:235], a[88:91], v[24:27]\n"
    "ds_read_b128 a[172:175], v0 offset:19968\n"
    "v_cvt_pk_bf16_f32 v196, v196, v197\n"
    "v_mfma_f32_16x16x32_bf16 v[24:27], a[236:239], a[92:95], v[24:27]\n"
    "s_add_u32 m0, s56, 0x800\n"
    "v_exp_f32 v198, v198\n"
    "v_mfma_f32_16x16x32_bf16 v[28:31], a[236:239], a[124:127], 0\n"
    "buffer_load_dwordx4 v4, s[40:43], s57 offen offset:0 lds\n"
    "v_mfma_f32_16x16x32_bf16 v[28:31], a[232:235], a[120:123], v[28:31]\n"
    "v_exp_f32 v199, v199\n"
    "v_mfma_f32_16x16x32_bf16 v[28:31], a[228:231], a[116:119], v[28:31]\n"
    "v_cvt_pk_bf16_f32 v197, v198, v199\n"
    "v_mul_f32 v201, s52, v201\n"
    "v_mfma_f32_16x16x32_bf16 v[28:31], a[224:227], a[112:115], v[28:31]\n"
    "v_exp_f32 v200, v200\n"
    "v_mfma_f32_16x16x32_bf16 v[28:31], a[220:223], a[108:111], v[28:31]\n"
    "v_exp_f32 v201, v201\n"
    "v_mfma_f32_16x16x32_bf16 v[28:31], a[216:219], a[104:107], v[28:31]\n"
    "v_mul_f32 v202, s52, v202\n"
    "v_cvt_pk_bf16_f32 v200, v200, v201\n"
    "v_mfma_f32_16x16x32_bf16 v[28:31], a[212:215], a[100:103], v[28:31]\n"
    "v_exp_f32 v202, v202\n"
    "v_mfma_f32_16x16x32_bf16 v[28:31], a[208:211], a[96:99], v[28:31]\n"
    "s_xor_b32 s57, s54, 0x3000\n"
    "v_mul_f32 v203, s52, v203\n"
    "v_mul_f32 v204, s52, v204\n"
    "v_mfma_f32_16x16x32_bf16 a[248:251], v[12:15], v[136:139], a[248:251]\n"
    "s_add_u32 m0, s56, 0xc00\n"
    "v_exp_f32 v203, v203\n"
    "v_mfma_f32_16x16x32_bf16 a[252:255], v[12:15], v[140:143], a[252:255]\n"
    "s_waitcnt lgkmcnt(0)\n"
    "v_cvt_pk_bf16_f32 v201, v202, v203\n"
    "v_mul_f32 v205, s52, v205\n"
    "v_mfma_f32_16x16x32_bf16 v[208:211], a[192:195], a[0:3], 0\n"
    "buffer_load_dwordx4 v4, s[40:43], s57 offen offset:0 lds\n"
    "v_mfma_f32_16x16x32_bf16 v[208:211], a[196:199], a[4:7], v[208:211]\n"
    "ds_read_b128 a[208:211], v0 offset:24576\n"
    "v_mul_f32 v206, s52, v206\n"
    "v_mfma_f32_16x16x32_bf16 v[208:211], a[200:203], a[8:11], v[208:211]\n"
    "s_and_b32 s64, s72, 0x180\n"
    "v_exp_f32 v204, v204\n"
    "v_mfma_f32_16x16x32_bf16 v[208:211], a[204:207], a[12:15], v[208:211]\n"
    "s_add_u32 s64, s64, s85\n"
    "v_exp_f32 v205, v205\n"
    "v_mfma_f32_16x16x32_bf16 v[208:211], a[160:163], a[16:19], v[208:211]\n"
    "ds_read_b128 a[212:215], v0 offset:25088\n"
    "v_cvt_pk_bf16_f32 v204, v204, v205\n"
    "v_mfma_f32_16x16x32_bf16 v[208:211], a[164:167], a[20:23], v[208:211]\n"
    "s_add_u32 s64, s64, s76\n"
    "v_exp_f32 v206, v206\n"
    "v_mfma_f32_16x16x32_bf16 v[208:211], a[168:171], a[24:27], v[208:211]\n"
    "s_add_u32 s65, s64, 0x1000\n"
    "v_mul_f32 v207, s52, v207\n"
    "v_mul_f32 v16, s52, v16\n"
    "v_mfma_f32_16x16x32_bf16 v[208:211], a[172:175], a[28:31], v[208:211]\n"
    "ds_read_b128 a[216:219], v0 offset:25600\n"
    "v_mul_f32 v17, s52, v17\n"
    "v_mfma_f32_16x16x32_bf16 v[212:215], a[172:175], a[60:63], 0\n"
    "s_add_u32 s57, s55, 0x4000\n"
    "v_exp_f32 v207, v207\n"
    "v_mfma_f32_16x16x32_bf16 v[212:215], a[168:171], a[56:59], v[212:215]\n"
    "s_add_u32 m0, s56, 0x20000\n"
    "v_cvt_pk_bf16_f32 v205, v206, v207\n"
    "v_mul_f32 v18, s52, v18\n"
    "v_mfma_f32_16x16x32_bf16 v[212:215], a[164:167], a[52:55], v[212:215]\n"
    "ds_read_b128 a[220:223], v0 offset:26112\n"
    "v_mul_f32 v19, s52, v19\n"
    "v_mfma_f32_16x16x32_bf16 v[212:215], a[160:163], a[48:51], v[212:215]\n"
    "buffer_load_dwordx4 v3, s[44:47], s57 offen offset:0 nt lds\n"
    "v_mfma_f32_16x16x32_bf16 v[212:215], a[204:207], a[44:47], v[212:215]\n"
    "s_add_u32 s66, s64, 0x2000\n"
    "v_exp_f32 v16, v16\n"
    "v_mfma_f32_16x16x32_bf16 v[212:215], a[200:203], a[40:43], v[212:215]\n"
    "ds_read_b128 a[224:227], v0 offset:26624\n"
    "v_mul_f32 v20, s52, v20\n"
    "v_mfma_f32_16x16x32_bf16 v[212:215], a[196:199], a[36:39], v[212:215]\n"
    "s_add_u32 s67, s64, 0x3000\n"
    "v_exp_f32 v17, v17\n"
    "v_mfma_f32_16x16x32_bf16 v[212:215], a[192:195], a[32:35], v[212:215]\n"
    "s_add_u32 s68, s64, 0x4000\n"
    "v_cvt_pk_bf16_f32 v194, v16, v17\n"
    "v_mul_f32 v21, s52, v21\n"
    "v_mfma_f32_16x16x32_bf16 v[216:219], a[192:195], a[64:67], 0\n"
    "ds_read_b128 a[228:231], v0 offset:27136\n"
    "v_mul_f32 v22, s52, v22\n"
    "v_mfma_f32_16x16x32_bf16 v[216:219], a[196:199], a[68:71], v[216:219]\n"
    "s_add_u32 s69, s64, 0x5000\n"
    "v_exp_f32 v18, v18\n"
    "v_mfma_f32_16x16x32_bf16 v[216:219], a[200:203], a[72:75], v[216:219]\n"
    "s_add_u32 m0, s56, 0x20380\n"
    "v_exp_f32 v19, v19\n"
    "v_mfma_f32_16x16x32_bf16 v[216:219], a[204:207], a[76:79], v[216:219]\n"
    "ds_read_b128 a[232:235], v0 offset:27648\n"
    "v_cvt_pk_bf16_f32 v195, v18, v19\n"
    "v_mfma_f32_16x16x32_bf16 v[216:219], a[160:163], a[80:83], v[216:219]\n"
    "buffer_load_dwordx4 v3, s[44:47], s57 offen offset:128 nt lds\n"
    "v_mfma_f32_16x16x32_bf16 v[216:219], a[164:167], a[84:87], v[216:219]\n"
    "s_add_u32 s70, s64, 0x6000\n"
    "v_exp_f32 v20, v20\n"
    "v_mfma_f32_16x16x32_bf16 v[216:219], a[168:171], a[88:91], v[216:219]\n"
    "ds_read_b128 a[236:239], v0 offset:28160\n"
    "v_mul_f32 v23, s52, v23\n"
    "v_mfma_f32_16x16x32_bf16 v[216:219], a[172:175], a[92:95], v[216:219]\n"
    "s_add_u32 s71, s64, 0x7000\n"
    "v_exp_f32 v21, v21\n"
    "v_mfma_f32_16x16x32_bf16 v[220:223], a[172:175], a[124:127], 0\n"
    "s_mov_b32 s50, s80\n"
    "v_cvt_pk_bf16_f32 v198, v20, v21\n"
    "v_mul_f32 v24, s52, v24\n"
    "v_mfma_f32_16x16x32_bf16 v[220:223], a[168:171], a[120:123], v[220:223]\n"
    "v_exp_f32 v22, v22\n"
    "v_mfma_f32_16x16x32_bf16 v[220:223], a[164:167], a[116:119], v[220:223]\n"
    "v_exp_f32 v23, v23\n"
    "v_mfma_f32_16x16x32_bf16 v[220:223], a[160:163], a[112:115], v[220:223]\n"
    "v_cvt_pk_bf16_f32 v199, v22, v23\n"
    "v_mul_f32 v25, s52, v25\n"
    "v_mfma_f32_16x16x32_bf16 v[220:223], a[204:207], a[108:111], v[220:223]\n"
    "v_exp_f32 v24, v24\n"
    "v_mfma_f32_16x16x32_bf16 v[220:223], a[200:203], a[104:107], v[220:223]\n"
    "v_exp_f32 v25, v25\n"
    "v_mfma_f32_16x16x32_bf16 v[220:223], a[196:199], a[100:103], v[220:223]\n"
    "v_mul_f32 v26, s52, v26\n"
    "v_cvt_pk_bf16_f32 v202, v24, v25\n"
    "v_mfma_f32_16x16x32_bf16 v[220:223], a[192:195], a[96:99], v[220:223]\n"
    "v_exp_f32 v26, v26\n"
    "v_mfma_f32_16x16x32_bf16 a[240:243], v[12:15], v[144:147], a[240:243]\n"
    "v_mul_f32 v27, s52, v27\n"
    "v_mul_f32 v28, s52, v28\n"
    "v_mfma_f32_16x16x32_bf16 a[244:247], v[12:15], v[148:151], a[244:247]\n"
    "s_waitcnt vmcnt(15) lgkmcnt(0)\n"
    "v_exp_f32 v27, v27\n"
    "v_mfma_f32_16x16x32_bf16 v[16:19], a[208:211], a[0:3], 0\n"
    "s_barrier\n"
    "v_cvt_pk_bf16_f32 v203, v26, v27\n"
    "v_mul_f32 v29, s52, v29\n"
    "v_mfma_f32_16x16x32_bf16 v[16:19], a[212:215], a[4:7], v[16:19]\n"
    "ds_read_b128 a[192:195], v0 offset:32768\n"
    "v_mul_f32 v30, s52, v30\n"
    "v_mfma_f32_16x16x32_bf16 v[16:19], a[216:219], a[8:11], v[16:19]\n"
    "s_xor_b32 s57, s54, 0x10000\n"
    "v_exp_f32 v28, v28\n"
    "v_mfma_f32_16x16x32_bf16 v[16:19], a[220:223], a[12:15], v[16:19]\n"
    "s_add_u32 m0, s56, 0x4000\n"
    "v_exp_f32 v29, v29\n"
    "v_mfma_f32_16x16x32_bf16 v[16:19], a[224:227], a[16:19], v[16:19]\n"
    "ds_read_b128 a[196:199], v0 offset:33280\n"
    "v_cvt_pk_bf16_f32 v206, v28, v29\n"
    "v_mfma_f32_16x16x32_bf16 v[16:19], a[228:231], a[20:23], v[16:19]\n"
    "buffer_load_dwordx4 v4, s[40:43], s57 offen offset:0 lds\n"
    "v_mfma_f32_16x16x32_bf16 v[16:19], a[232:235], a[24:27], v[16:19]\n"
    "v_exp_f32 v30, v30\n"
    "v_mfma_f32_16x16x32_bf16 v[16:19], a[236:239], a[28:31], v[16:19]\n"
    "ds_read_b128 a[200:203], v0 offset:33792\n"
    "v_mul_f32 v31, s52, v31\n"
    "v_mfma_f32_16x16x32_bf16 v[20:23], a[236:239], a[60:63], 0\n"
    "v_exp_f32 v31, v31\n"
    "v_mfma_f32_16x16x32_bf16 v[20:23], a[232:235], a[56:59], v[20:23]\n"
    "v_cvt_pk_bf16_f32 v207, v30, v31\n"
    "v_mul_f32 v208, s52, v208\n"
    "v_mfma_f32_16x16x32_bf16 v[20:23], a[228:231], a[52:55], v[20:23]\n"
    "ds_read_b128 a[204:207], v0 offset:34304\n"
    "v_mul_f32 v209, s52, v209\n"
    "v_mfma_f32_16x16x32_bf16 v[20:23], a[224:227], a[48:51], v[20:23]\n"
    "s_xor_b32 s57, s54, 0x11000\n"
    "v_exp_f32 v208, v208\n"
    "v_mfma_f32_16x16x32_bf16 v[20:23], a[220:223], a[44:47], v[20:23]\n"
    "s_add_u32 m0, s56, 0x4400\n"
    "v_exp_f32 v209, v209\n"
    "v_mfma_f32_16x16x32_bf16 v[20:23], a[216:219], a[40:43], v[20:23]\n"
    "ds_read_b128 a[160:163], v0 offset:34816\n"
    "v_mul_f32 v210, s52, v210\n"
    "v_mfma_f32_16x16x32_bf16 v[20:23], a[212:215], a[36:39], v[20:23]\n"
    "buffer_load_dwordx4 v4, s[40:43], s57 offen offset:0 lds\n"
    "v_mfma_f32_16x16x32_bf16 v[20:23], a[208:211], a[32:35], v[20:23]\n"
    "v_cvt_pk_bf16_f32 v208, v208, v209\n"
    "v_mul_f32 v211, s52, v211\n"
    "v_mfma_f32_16x16x32_bf16 v[24:27], a[208:211], a[64:67], 0\n"
    "ds_read_b128 a[164:167], v0 offset:35328\n"
    "v_mul_f32 v212, s52, v212\n"
    "v_mfma_f32_16x16x32_bf16 v[24:27], a[212:215], a[68:71], v[24:27]\n"
    "v_exp_f32 v210, v210\n"
    "v_mfma_f32_16x16x32_bf16 v[24:27], a[216:219], a[72:75], v[24:27]\n"
    "v_exp_f32 v211, v211\n"
    "v_mfma_f32_16x16x32_bf16 v[24:27], a[220:223], a[76:79], v[24:27]\n"
    "ds_read_b128 a[168:171], v0 offset:35840\n"
    "v_cvt_pk_bf16_f32 v209, v210, v211\n"
    "v_mfma_f32_16x16x32_bf16 v[24:27], a[224:227], a[80:83], v[24:27]\n"
    "v_exp_f32 v212, v212\n"
    "v_mfma_f32_16x16x32_bf16 v[24:27], a[228:231], a[84:87], v[24:27]\n"
    "s_xor_b32 s57, s54, 0x12000\n"
    "v_mul_f32 v213, s52, v213\n"
    "v_mul_f32 v214, s52, v214\n"
    "v_mfma_f32_16x16x32_bf16 v[24:27], a[232:235], a[88:91], v[24:27]\n"
    "ds_read_b128 a[172:175], v0 offset:36352\n"
    "v_mul_f32 v215, s52, v215\n"
    "v_mfma_f32_16x16x32_bf16 v[24:27], a[236:239], a[92:95], v[24:27]\n"
    "s_add_u32 m0, s56, 0x4800\n"
    "v_exp_f32 v213, v213\n"
    "v_mfma_f32_16x16x32_bf16 v[28:31], a[236:239], a[124:127], 0\n"
    "buffer_load_dwordx4 v4, s[40:43], s57 offen offset:0 lds\n"
    "v_mfma_f32_16x16x32_bf16 v[28:31], a[232:235], a[120:123], v[28:31]\n"
    "v_cvt_pk_bf16_f32 v212, v212, v213\n"
    "v_mul_f32 v216, s52, v216\n"
    "v_mfma_f32_16x16x32_bf16 v[28:31], a[228:231], a[116:119], v[28:31]\n"
    "v_exp_f32 v214, v214\n"
    "v_mfma_f32_16x16x32_bf16 v[28:31], a[224:227], a[112:115], v[28:31]\n"
    "v_exp_f32 v215, v215\n"
    "v_mfma_f32_16x16x32_bf16 v[28:31], a[220:223], a[108:111], v[28:31]\n"
    "v_cvt_pk_bf16_f32 v213, v214, v215\n"
    "v_mul_f32 v217, s52, v217\n"
    "v_mfma_f32_16x16x32_bf16 v[28:31], a[216:219], a[104:107], v[28:31]\n"
    "v_exp_f32 v216, v216\n"
    "v_mfma_f32_16x16x32_bf16 v[28:31], a[212:215], a[100:103], v[28:31]\n"
    "v_exp_f32 v217, v217\n"
    "v_mfma_f32_16x16x32_bf16 v[28:31], a[208:211], a[96:99], v[28:31]\n"
    "s_xor_b32 s57, s54, 0x13000\n"
    "v_mul_f32 v218, s52, v218\n"
    "v_cvt_pk_bf16_f32 v216, v216, v217\n"
    "v_mfma_f32_16x16x32_bf16 a[248:251], v[12:15], v[152:155], a[248:251]\n"
    "s_add_u32 m0, s56, 0x4c00\n"
    "v_exp_f32 v218, v218\n"
    "v_mfma_f32_16x16x32_bf16 a[252:255], v[12:15], v[156:159], a[252:255]\n"
    "s_waitcnt lgkmcnt(0)\n"
    "v_mul_f32 v219, s52, v219\n"
    "v_mul_f32 v220, s52, v220\n"
    "v_mfma_f32_16x16x32_bf16 v[224:227], a[192:195], a[0:3], 0\n"
    "buffer_load_dwordx4 v4, s[40:43], s57 offen offset:0 lds\n"
    "v_mfma_f32_16x16x32_bf16 v[224:227], a[196:199], a[4:7], v[224:227]\n"
    "ds_read_b128 a[208:211], v0 offset:40960\n"
    "v_mul_f32 v221, s52, v221\n"
    "v_mfma_f32_16x16x32_bf16 v[224:227], a[200:203], a[8:11], v[224:227]\n"
    "v_exp_f32 v219, v219\n"
    "v_mfma_f32_16x16x32_bf16 v[224:227], a[204:207], a[12:15], v[224:227]\n"
    "v_cvt_pk_bf16_f32 v217, v218, v219\n"
    "v_mul_f32 v222, s52, v222\n"
    "v_mfma_f32_16x16x32_bf16 v[224:227], a[160:163], a[16:19], v[224:227]\n"
    "ds_read_b128 a[212:215], v0 offset:41472\n"
    "v_mul_f32 v223, s52, v223\n"
    "v_mfma_f32_16x16x32_bf16 v[224:227], a[164:167], a[20:23], v[224:227]\n"
    "v_exp_f32 v220, v220\n"
    "v_mfma_f32_16x16x32_bf16 v[224:227], a[168:171], a[24:27], v[224:227]\n"
    "v_exp_f32 v221, v221\n"
    "v_mfma_f32_16x16x32_bf16 v[224:227], a[172:175], a[28:31], v[224:227]\n"
    "ds_read_b128 a[216:219], v0 offset:41984\n"
    "v_cvt_pk_bf16_f32 v220, v220, v221\n"
    "v_mfma_f32_16x16x32_bf16 v[228:231], a[172:175], a[60:63], 0\n"
    "s_add_u32 s57, s55, 0x4000\n"
    "v_exp_f32 v222, v222\n"
    "v_mfma_f32_16x16x32_bf16 v[228:231], a[168:171], a[56:59], v[228:231]\n"
    "s_add_u32 m0, s56, 0x20700\n"
    "v_exp_f32 v223, v223\n"
    "v_mfma_f32_16x16x32_bf16 v[228:231], a[164:167], a[52:55], v[228:231]\n"
    "ds_read_b128 a[220:223], v0 offset:42496\n"
    "v_cvt_pk_bf16_f32 v221, v222, v223\n"
    "v_mfma_f32_16x16x32_bf16 v[228:231], a[160:163], a[48:51], v[228:231]\n"
    "buffer_load_dwordx4 v3, s[44:47], s57 offen offset:256 nt lds\n"
    "v_mfma_f32_16x16x32_bf16 v[228:231], a[204:207], a[44:47], v[228:231]\n"
    "v_mul_f32 v16, s52, v16\n"
    "v_mul_f32 v17, s52, v17\n"
    "v_mfma_f32_16x16x32_bf16 v[228:231], a[200:203], a[40:43], v[228:231]\n"
    "ds_read_b128 a[224:227], v0 offset:43008\n"
    "v_mul_f32 v18, s52, v18\n"
    "v_mfma_f32_16x16x32_bf16 v[228:231], a[196:199], a[36:39], v[228:231]\n"
    "v_exp_f32 v16, v16\n"
    "v_mfma_f32_16x16x32_bf16 v[228:231], a[192:195], a[32:35], v[228:231]\n"
    "v_exp_f32 v17, v17\n"
    "v_mfma_f32_16x16x32_bf16 v[232:235], a[192:195], a[64:67], 0\n"
    "ds_read_b128 a[228:231], v0 offset:43520\n"
    "v_cvt_pk_bf16_f32 v210, v16, v17\n"
    "v_mfma_f32_16x16x32_bf16 v[232:235], a[196:199], a[68:71], v[232:235]\n"
    "v_exp_f32 v18, v18\n"
    "v_mfma_f32_16x16x32_bf16 v[232:235], a[200:203], a[72:75], v[232:235]\n"
    "s_add_u32 m0, s56, 0x20a80\n"
    "v_mul_f32 v19, s52, v19\n"
    "v_mul_f32 v20, s52, v20\n"
    "v_mfma_f32_16x16x32_bf16 v[232:235], a[204:207], a[76:79], v[232:235]\n"
    "ds_read_b128 a[232:235], v0 offset:44032\n"
    "v_mul_f32 v21, s52, v21\n"
    "v_mfma_f32_16x16x32_bf16 v[232:235], a[160:163], a[80:83], v[232:235]\n"
    "buffer_load_dwordx4 v3, s[44:47], s57 offen offset:384 nt lds\n"
    "v_mfma_f32_16x16x32_bf16 v[232:235], a[164:167], a[84:87], v[232:235]\n"
    "v_exp_f32 v19, v19\n"
    "v_mfma_f32_16x16x32_bf16 v[232:235], a[168:171], a[88:91], v[232:235]\n"
    "ds_read_b128 a[236:239], v0 offset:44544\n"
    "v_cvt_pk_bf16_f32 v211, v18, v19\n"
    "v_mfma_f32_16x16x32_bf16 v[232:235], a[172:175], a[92:95], v[232:235]\n"
    "v_exp_f32 v20, v20\n"
    "v_mfma_f32_16x16x32_bf16 v[236:239], a[172:175], a[124:127], 0\n"
    "v_exp_f32 v21, v21\n"
    "v_mfma_f32_16x16x32_bf16 v[236:239], a[168:171], a[120:123], v[236:239]\n"
    "v_mul_f32 v22, s52, v22\n"
    "v_cvt_pk_bf16_f32 v214, v20, v21\n"
    "v_mfma_f32_16x16x32_bf16 v[236:239], a[164:167], a[116:119], v[236:239]\n"
    "v_exp_f32 v22, v22\n"
    "v_mfma_f32_16x16x32_bf16 v[236:239], a[160:163], a[112:115], v[236:239]\n"
    "v_mul_f32 v23, s52, v23\n"
    "v_mul_f32 v24, s52, v24\n"
    "v_mfma_f32_16x16x32_bf16 v[236:239], a[204:207], a[108:111], v[236:239]\n"
    "v_exp_f32 v23, v23\n"
    "v_mfma_f32_16x16x32_bf16 v[236:239], a[200:203], a[104:107], v[236:239]\n"
    "v_cvt_pk_bf16_f32 v215, v22, v23\n"
    "v_mul_f32 v25, s52, v25\n"
    "v_mfma_f32_16x16x32_bf16 v[236:239], a[196:199], a[100:103], v[236:239]\n"
    "v_exp_f32 v24, v24\n"
    "v_mfma_f32_16x16x32_bf16 v[236:239], a[192:195], a[96:99], v[236:239]\n"
    "v_exp_f32 v25, v25\n"
    "v_mfma_f32_16x16x32_bf16 a[240:243], v[12:15], v[192:195], a[240:243]\n"
    "v_mul_f32 v26, s52, v26\n"
    "v_cvt_pk_bf16_f32 v218, v24, v25\n"
    "v_mfma_f32_16x16x32_bf16 a[244:247], v[12:15], v[196:199], a[244:247]\n"
    "s_waitcnt vmcnt(15) lgkmcnt(0)\n"
    "v_exp_f32 v26, v26\n"
    "v_mfma_f32_16x16x32_bf16 v[16:19], a[208:211], a[0:3], 0\n"
    "s_barrier\n"
    "v_mul_f32 v27, s52, v27\n"
    "v_mul_f32 v28, s52, v28\n"
    "v_mfma_f32_16x16x32_bf16 v[16:19], a[212:215], a[4:7], v[16:19]\n"
    "ds_read_b128 a[192:195], v0 offset:49152\n"
    "v_mul_f32 v29, s52, v29\n"
    "v_mfma_f32_16x16x32_bf16 v[16:19], a[216:219], a[8:11], v[16:19]\n"
    "s_xor_b32 s57, s54, 0x20000\n"
    "v_exp_f32 v27, v27\n"
    "v_mfma_f32_16x16x32_bf16 v[16:19], a[220:223], a[12:15], v[16:19]\n"
    "s_add_u32 m0, s56, 0x8000\n"
    "v_cvt_pk_bf16_f32 v219, v26, v27\n"
    "v_mul_f32 v30, s52, v30\n"
    "v_mfma_f32_16x16x32_bf16 v[16:19], a[224:227], a[16:19], v[16:19]\n"
    "ds_read_b128 a[196:199], v0 offset:49664\n"
    "v_mul_f32 v31, s52, v31\n"
    "v_mfma_f32_16x16x32_bf16 v[16:19], a[228:231], a[20:23], v[16:19]\n"
    "buffer_load_dwordx4 v4, s[40:43], s57 offen offset:0 lds\n"
    "v_mfma_f32_16x16x32_bf16 v[16:19], a[232:235], a[24:27], v[16:19]\n"
    "v_exp_f32 v28, v28\n"
    "v_mfma_f32_16x16x32_bf16 v[16:19], a[236:239], a[28:31], v[16:19]\n"
    "ds_read_b128 a[200:203], v0 offset:50176\n"
    "v_mul_f32 v224, s52, v224\n"
    "v_mfma_f32_16x16x32_bf16 v[20:23], a[236:239], a[60:63], 0\n"
    "v_exp_f32 v29, v29\n"
    "v_mfma_f32_16x16x32_bf16 v[20:23], a[232:235], a[56:59], v[20:23]\n"
    "v_cvt_pk_bf16_f32 v222, v28, v29\n"
    "v_mul_f32 v225, s52, v225\n"
    "v_mfma_f32_16x16x32_bf16 v[20:23], a[228:231], a[52:55], v[20:23]\n"
    "ds_read_b128 a[204:207], v0 offset:50688\n"
    "v_mul_f32 v226, s52, v226\n"
    "v_mfma_f32_16x16x32_bf16 v[20:23], a[224:227], a[48:51], v[20:23]\n"
    "s_xor_b32 s57, s54, 0x21000\n"
    "v_exp_f32 v30, v30\n"
    "v_mfma_f32_16x16x32_bf16 v[20:23], a[220:223], a[44:47], v[20:23]\n"
    "s_add_u32 m0, s56, 0x8400\n"
    "v_exp_f32 v31, v31\n"
    "v_mfma_f32_16x16x32_bf16 v[20:23], a[216:219], a[40:43], v[20:23]\n"
    "ds_read_b128 a[160:163], v0 offset:51200\n"
    "v_cvt_pk_bf16_f32 v223, v30, v31\n"
    "v_mfma_f32_16x16x32_bf16 v[20:23], a[212:215], a[36:39], v[20:23]\n"
    "buffer_load_dwordx4 v4, s[40:43], s57 offen offset:0 lds\n"
    "v_mfma_f32_16x16x32_bf16 v[20:23], a[208:211], a[32:35], v[20:23]\n"
    "v_exp_f32 v224, v224\n"
    "v_mfma_f32_16x16x32_bf16 v[24:27], a[208:211], a[64:67], 0\n"
    "ds_read_b128 a[164:167], v0 offset:51712\n"
    "v_mul_f32 v227, s52, v227\n"
    "v_mfma_f32_16x16x32_bf16 v[24:27], a[212:215], a[68:71], v[24:27]\n"
    "v_exp_f32 v225, v225\n"
    "v_mfma_f32_16x16x32_bf16 v[24:27], a[216:219], a[72:75], v[24:27]\n"
    "v_cvt_pk_bf16_f32 v224, v224, v225\n"
    "v_mul_f32 v228, s52, v228\n"
    "v_mfma_f32_16x16x32_bf16 v[24:27], a[220:223], a[76:79], v[24:27]\n"
    "ds_read_b128 a[168:171], v0 offset:52224\n"
    "v_mul_f32 v229, s52, v229\n"
    "v_mfma_f32_16x16x32_bf16 v[24:27], a[224:227], a[80:83], v[24:27]\n"
    "v_exp_f32 v226, v226\n"
    "v_mfma_f32_16x16x32_bf16 v[24:27], a[228:231], a[84:87], v[24:27]\n"
    "s_xor_b32 s57, s54, 0x22000\n"
    "v_exp_f32 v227, v227\n"
    "v_mfma_f32_16x16x32_bf16 v[24:27], a[232:235], a[88:91], v[24:27]\n"
    "ds_read_b128 a[172:175], v0 offset:52736\n"
    "v_cvt_pk_bf16_f32 v225, v226, v227\n"
    "v_mfma_f32_16x16x32_bf16 v[24:27], a[236:239], a[92:95], v[24:27]\n"
    "s_add_u32 m0, s56, 0x8800\n"
    "v_exp_f32 v228, v228\n"
    "v_mfma_f32_16x16x32_bf16 v[28:31], a[236:239], a[124:127], 0\n"
    "buffer_load_dwordx4 v4, s[40:43], s57 offen offset:0 lds\n"
    "v_mfma_f32_16x16x32_bf16 v[28:31], a[232:235], a[120:123], v[28:31]\n"
    "v_exp_f32 v229, v229\n"
    "v_mfma_f32_16x16x32_bf16 v[28:31], a[228:231], a[116:119], v[28:31]\n"
    "v_mul_f32 v230, s52, v230\n"
    "v_cvt_pk_bf16_f32 v228, v228, v229\n"
    "v_mfma_f32_16x16x32_bf16 v[28:31], a[224:227], a[112:115], v[28:31]\n"
    "v_exp_f32 v230, v230\n"
    "v_mfma_f32_16x16x32_bf16 v[28:31], a[220:223], a[108:111], v[28:31]\n"
    "v_mul_f32 v231, s52, v231\n"
    "v_mul_f32 v232, s52, v232\n"
    "v_mfma_f32_16x16x32_bf16 v[28:31], a[216:219], a[104:107], v[28:31]\n"
    "v_exp_f32 v231, v231\n"
    "v_mfma_f32_16x16x32_bf16 v[28:31], a[212:215], a[100:103], v[28:31]\n"
    "v_cvt_pk_bf16_f32 v229, v230, v231\n"
    "v_mul_f32 v233, s52, v233\n"
    "v_mfma_f32_16x16x32_bf16 v[28:31], a[208:211], a[96:99], v[28:31]\n"
    "s_xor_b32 s57, s54, 0x23000\n"
    "v_exp_f32 v232, v232\n"
    "v_mfma_f32_16x16x32_bf16 a[248:251], v[12:15], v[200:203], a[248:251]\n"
    "s_add_u32 m0, s56, 0x8c00\n"
    "v_exp_f32 v233, v233\n"
    "v_mfma_f32_16x16x32_bf16 a[252:255], v[12:15], v[204:207], a[252:255]\n"
    "s_waitcnt lgkmcnt(0)\n"
    "v_mul_f32 v234, s52, v234\n"
    "v_cvt_pk_bf16_f32 v232, v232, v233\n"
    "v_mfma_f32_16x16x32_bf16 v[240:243], a[192:195], a[0:3], 0\n"
    "buffer_load_dwordx4 v4, s[40:43], s57 offen offset:0 lds\n"
    "v_mfma_f32_16x16x32_bf16 v[240:243], a[196:199], a[4:7], v[240:243]\n"
    "ds_read_b128 a[208:211], v0 offset:57344\n"
    "v_mul_f32 v235, s52, v235\n"
    "v_mfma_f32_16x16x32_bf16 v[240:243], a[200:203], a[8:11], v[240:243]\n"
    "v_exp_f32 v234, v234\n"
    "v_mfma_f32_16x16x32_bf16 v[240:243], a[204:207], a[12:15], v[240:243]\n"
    "v_exp_f32 v235, v235\n"
    "v_mfma_f32_16x16x32_bf16 v[240:243], a[160:163], a[16:19], v[240:243]\n"
    "ds_read_b128 a[212:215], v0 offset:57856\n"
    "v_cvt_pk_bf16_f32 v233, v234, v235\n"
    "v_mfma_f32_16x16x32_bf16 v[240:243], a[164:167], a[20:23], v[240:243]\n"
    "v_mul_f32 v236, s52, v236\n"
    "v_mul_f32 v237, s52, v237\n"
    "v_mfma_f32_16x16x32_bf16 v[240:243], a[168:171], a[24:27], v[240:243]\n"
    "v_exp_f32 v236, v236\n"
    "v_mfma_f32_16x16x32_bf16 v[240:243], a[172:175], a[28:31], v[240:243]\n"
    "ds_read_b128 a[216:219], v0 offset:58368\n"
    "v_mul_f32 v238, s52, v238\n"
    "v_mfma_f32_16x16x32_bf16 v[244:247], a[172:175], a[60:63], 0\n"
    "s_add_u32 s57, s55, 0x14000\n"
    "v_exp_f32 v237, v237\n"
    "v_mfma_f32_16x16x32_bf16 v[244:247], a[168:171], a[56:59], v[244:247]\n"
    "s_add_u32 m0, s56, 0x24000\n"
    "v_cvt_pk_bf16_f32 v236, v236, v237\n"
    "v_mul_f32 v239, s52, v239\n"
    "v_mfma_f32_16x16x32_bf16 v[244:247], a[164:167], a[52:55], v[244:247]\n"
    "ds_read_b128 a[220:223], v0 offset:58880\n"
    "v_mul_f32 v16, s52, v16\n"
    "v_mfma_f32_16x16x32_bf16 v[244:247], a[160:163], a[48:51], v[244:247]\n"
    "buffer_load_dwordx4 v3, s[44:47], s57 offen offset:0 nt lds\n"
    "v_mfma_f32_16x16x32_bf16 v[244:247], a[204:207], a[44:47], v[244:247]\n"
    "v_exp_f32 v238, v238\n"
    "v_mfma_f32_16x16x32_bf16 v[244:247], a[200:203], a[40:43], v[244:247]\n"
    "ds_read_b128 a[224:227], v0 offset:59392\n"
    "v_mul_f32 v17, s52, v17\n"
    "v_mfma_f32_16x16x32_bf16 v[244:247], a[196:199], a[36:39], v[244:247]\n"
    "v_exp_f32 v239, v239\n"
    "v_mfma_f32_16x16x32_bf16 v[244:247], a[192:195], a[32:35], v[244:247]\n"
    "v_cvt_pk_bf16_f32 v237, v238, v239\n"
    "v_mul_f32 v18, s52, v18\n"
    "v_mfma_f32_16x16x32_bf16 v[248:251], a[192:195], a[64:67], 0\n"
    "ds_read_b128 a[228:231], v0 offset:59904\n"
    "v_mul_f32 v19, s52, v19\n"
    "v_mfma_f32_16x16x32_bf16 v[248:251], a[196:199], a[68:71], v[248:251]\n"
    "v_exp_f32 v16, v16\n"
    "v_mfma_f32_16x16x32_bf16 v[248:251], a[200:203], a[72:75], v[248:251]\n"
    "s_add_u32 m0, s56, 0x24380\n"
    "v_exp_f32 v17, v17\n"
    "v_mfma_f32_16x16x32_bf16 v[248:251], a[204:207], a[76:79], v[248:251]\n"
    "ds_read_b128 a[232:235], v0 offset:60416\n"
    "v_cvt_pk_bf16_f32 v226, v16, v17\n"
    "v_mfma_f32_16x16x32_bf16 v[248:251], a[160:163], a[80:83], v[248:251]\n"
    "buffer_load_dwordx4 v3, s[44:47], s57 offen offset:128 nt lds\n"
    "v_mfma_f32_16x16x32_bf16 v[248:251], a[164:167], a[84:87], v[248:251]\n"
    "v_exp_f32 v18, v18\n"
    "v_mfma_f32_16x16x32_bf16 v[248:251], a[168:171], a[88:91], v[248:251]\n"
    "ds_read_b128 a[236:239], v0 offset:60928\n"
    "v_mul_f32 v20, s52, v20\n"
    "v_mfma_f32_16x16x32_bf16 v[248:251], a[172:175], a[92:95], v[248:251]\n"
    "v_exp_f32 v19, v19\n"
    "v_mfma_f32_16x16x32_bf16 v[252:255], a[172:175], a[124:127], 0\n"
    "v_cvt_pk_bf16_f32 v227, v18, v19\n"
    "v_mul_f32 v21, s52, v21\n"
    "v_mfma_f32_16x16x32_bf16 v[252:255], a[168:171], a[120:123], v[252:255]\n"
    "v_exp_f32 v20, v20\n"
    "v_mfma_f32_16x16x32_bf16 v[252:255], a[164:167], a[116:119], v[252:255]\n"
    "v_exp_f32 v21, v21\n"
    "v_mfma_f32_16x16x32_bf16 v[252:255], a[160:163], a[112:115], v[252:255]\n"
    "v_mul_f32 v22, s52, v22\n"
    "v_cvt_pk_bf16_f32 v230, v20, v21\n"
    "v_mfma_f32_16x16x32_bf16 v[252:255], a[204:207], a[108:111], v[252:255]\n"
    "v_exp_f32 v22, v22\n"
    "v_mfma_f32_16x16x32_bf16 v[252:255], a[200:203], a[104:107], v[252:255]\n"
    "v_mul_f32 v23, s52, v23\n"
    "v_mul_f32 v24, s52, v24\n"
    "v_mfma_f32_16x16x32_bf16 v[252:255], a[196:199], a[100:103], v[252:255]\n"
    "v_exp_f32 v23, v23\n"
    "v_mfma_f32_16x16x32_bf16 v[252:255], a[192:195], a[96:99], v[252:255]\n"
    "v_cvt_pk_bf16_f32 v231, v22, v23\n"
    "v_mul_f32 v25, s52, v25\n"
    "v_mfma_f32_16x16x32_bf16 a[240:243], v[12:15], v[208:211], a[240:243]\n"
    "v_exp_f32 v24, v24\n"
    "v_mfma_f32_16x16x32_bf16 a[244:247], v[12:15], v[212:215], a[244:247]\n"
    "s_waitcnt vmcnt(14) lgkmcnt(0)\n"
    "v_exp_f32 v25, v25\n"
    "v_mfma_f32_16x16x32_bf16 v[16:19], a[208:211], a[0:3], 0\n"
    "s_barrier\n"
    "v_mul_f32 v26, s52, v26\n"
    "v_cvt_pk_bf16_f32 v234, v24, v25\n"
    "v_mfma_f32_16x16x32_bf16 v[16:19], a[212:215], a[4:7], v[16:19]\n"
    "ds_read_b64_tr_b16 a[160:161], v1 offset:0\n"
    "v_mul_f32 v27, s52, v27\n"
    "v_mfma_f32_16x16x32_bf16 v[16:19], a[216:219], a[8:11], v[16:19]\n"
    "s_xor_b32 s57, s54, 0x30000\n"
    "v_exp_f32 v26, v26\n"
    "v_mfma_f32_16x16x32_bf16 v[16:19], a[220:223], a[12:15], v[16:19]\n"
    "s_add_u32 m0, s56, 0xc000\n"
    "v_exp_f32 v27, v27\n"
    "v_mfma_f32_16x16x32_bf16 v[16:19], a[224:227], a[16:19], v[16:19]\n"
    "ds_read_b64_tr_b16 a[162:163], v1 offset:2048\n"
    "v_cvt_pk_bf16_f32 v235, v26, v27\n"
    "v_mfma_f32_16x16x32_bf16 v[16:19], a[228:231], a[20:23], v[16:19]\n"
    "buffer_load_dwordx4 v4, s[40:43], s57 offen offset:0 lds\n"
    "v_mfma_f32_16x16x32_bf16 v[16:19], a[232:235], a[24:27], v[16:19]\n"
    "v_mul_f32 v28, s52, v28\n"
    "v_mul_f32 v29, s52, v29\n"
    "v_mfma_f32_16x16x32_bf16 v[16:19], a[236:239], a[28:31], v[16:19]\n"
    "ds_read_b64_tr_b16 a[164:165], v1 offset:4096\n"
    "v_mul_f32 v30, s52, v30\n"
    "v_mfma_f32_16x16x32_bf16 v[20:23], a[236:239], a[60:63], 0\n"
    "s_xor_b32 s57, s54, 0x31000\n"
    "v_exp_f32 v28, v28\n"
    "v_mfma_f32_16x16x32_bf16 v[20:23], a[232:235], a[56:59], v[20:23]\n"
    "s_add_u32 m0, s56, 0xc400\n"
    "v_exp_f32 v29, v29\n"
    "v_mfma_f32_16x16x32_bf16 v[20:23], a[228:231], a[52:55], v[20:23]\n"
    "ds_read_b64_tr_b16 a[166:167], v1 offset:6144\n"
    "v_cvt_pk_bf16_f32 v238, v28, v29\n"
    "v_mfma_f32_16x16x32_bf16 v[20:23], a[224:227], a[48:51], v[20:23]\n"
    "buffer_load_dwordx4 v4, s[40:43], s57 offen offset:0 lds\n"
    "v_mfma_f32_16x16x32_bf16 v[20:23], a[220:223], a[44:47], v[20:23]\n"
    "L_pad8_%=:\n"
    "s_cbranch_execz L_ret2_%=\n"
    "v_exp_f32 v30, v30\n"
    "v_mfma_f32_16x16x32_bf16 v[20:23], a[216:219], a[40:43], v[20:23]\n"
    "ds_read_b64_tr_b16 a[168:169], v1 offset:8192\n"
    "v_mul_f32 v31, s52, v31\n"
    "v_mfma_f32_16x16x32_bf16 v[20:23], a[212:215], a[36:39], v[20:23]\n"
    "s_xor_b32 s57, s54, 0x32000\n"
    "v_exp_f32 v31, v31\n"
    "v_mfma_f32_16x16x32_bf16 v[20:23], a[208:211], a[32:35], v[20:23]\n"
    "s_add_u32 m0, s56, 0xc800\n"
    "v_cvt_pk_bf16_f32 v239, v30, v31\n"
    "v_mul_f32 v240, s52, v240\n"
    "v_mfma_f32_16x16x32_bf16 v[24:27], a[208:211], a[64:67], 0\n"
    "ds_read_b64_tr_b16 a[170:171], v1 offset:10240\n"
    "v_mul_f32 v241, s52, v241\n"
    "v_mfma_f32_16x16x32_bf16 v[24:27], a[212:215], a[68:71], v[24:27]\n"
    "buffer_load_dwordx4 v4, s[40:43], s57 offen offset:0 lds\n"
    "v_mfma_f32_16x16x32_bf16 v[24:27], a[216:219], a[72:75], v[24:27]\n"
    "v_exp_f32 v240, v240\n"
    "v_mfma_f32_16x16x32_bf16 v[24:27], a[220:223], a[76:79], v[24:27]\n"
    "ds_read_b64_tr_b16 a[172:173], v1 offset:12288\n"
    "v_mul_f32 v242, s52, v242\n"
    "v_mfma_f32_16x16x32_bf16 v[24:27], a[224:227], a[80:83], v[24:27]\n"
    "s_xor_b32 s57, s54, 0x33000\n"
    "v_exp_f32 v241, v241\n"
    "v_mfma_f32_16x16x32_bf16 v[24:27], a[228:231], a[84:87], v[24:27]\n"
    "s_add_u32 m0, s56, 0xcc00\n"
    "v_cvt_pk_bf16_f32 v240, v240, v241\n"
    "v_mul_f32 v243, s52, v243\n"
    "v_mfma_f32_16x16x32_bf16 v[24:27], a[232:235], a[88:91], v[24:27]\n"
    "ds_read_b64_tr_b16 a[174:175], v1 offset:14336\n"
    "v_mul_f32 v244, s52, v244\n"
    "v_mfma_f32_16x16x32_bf16 v[24:27], a[236:239], a[92:95], v[24:27]\n"
    "buffer_load_dwordx4 v4, s[40:43], s57 offen offset:0 lds\n"
    "v_mfma_f32_16x16x32_bf16 v[28:31], a[236:239], a[124:127], 0\n"
    "v_exp_f32 v242, v242\n"
    "v_mfma_f32_16x16x32_bf16 v[28:31], a[232:235], a[120:123], v[28:31]\n"
    "v_exp_f32 v243, v243\n"
    "v_mfma_f32_16x16x32_bf16 v[28:31], a[228:231], a[116:119], v[28:31]\n"
    "v_cvt_pk_bf16_f32 v241, v242, v243\n"
    "v_mul_f32 v245, s52, v245\n"
    "v_mfma_f32_16x16x32_bf16 v[28:31], a[224:227], a[112:115], v[28:31]\n"
    "s_add_u32 s57, s55, 0x14000\n"
    "v_exp_f32 v244, v244\n"
    "v_mfma_f32_16x16x32_bf16 v[28:31], a[220:223], a[108:111], v[28:31]\n"
    "s_add_u32 m0, s56, 0x24700\n"
    "v_exp_f32 v245, v245\n"
    "v_mfma_f32_16x16x32_bf16 v[28:31], a[216:219], a[104:107], v[28:31]\n"
    "buffer_load_dwordx4 v3, s[44:47], s57 offen offset:256 nt lds\n"
    "v_mfma_f32_16x16x32_bf16 v[28:31], a[212:215], a[100:103], v[28:31]\n"
    "v_mul_f32 v246, s52, v246\n"
    "v_cvt_pk_bf16_f32 v244, v244, v245\n"
    "v_mfma_f32_16x16x32_bf16 v[28:31], a[208:211], a[96:99], v[28:31]\n"
    "v_exp_f32 v246, v246\n"
    "v_mfma_f32_16x16x32_bf16 a[248:251], v[12:15], v[216:219], a[248:251]\n"
    "v_mul_f32 v247, s52, v247\n"
    "v_mul_f32 v248, s52, v248\n"
    "v_mfma_f32_16x16x32_bf16 a[252:255], v[12:15], v[220:223], a[252:255]\n"
    "s_waitcnt lgkmcnt(0)\n"
    "s_add_u32 m0, s56, 0x24a80\n"
    "v_mul_f32 v249, s52, v249\n"
    "v_mfma_f32_16x16x32_bf16 a[192:195], a[160:163], a[128:131], 0\n"
    "v_exp_f32 v247, v247\n"
    "v_mfma_f32_16x16x32_bf16 a[192:195], a[164:167], a[136:139], a[192:195]\n"
    "ds_read_b64_tr_b16 a[176:177], v2 offset:0\n"
    "v_cvt_pk_bf16_f32 v245, v246, v247\n"
    "v_mfma_f32_16x16x32_bf16 a[192:195], a[168:171], a[144:147], a[192:195]\n"
    "ds_read_b64_tr_b16 a[178:179], v2 offset:2048\n"
    "v_mul_f32 v250, s52, v250\n"
    "v_mfma_f32_16x16x32_bf16 a[192:195], a[172:175], a[152:155], a[192:195]\n"
    "ds_read_b64_tr_b16 a[180:181], v2 offset:4096\n"
    "v_mul_f32 v251, s52, v251\n"
    "v_mfma_f32_16x16x32_bf16 a[196:199], a[172:175], v[184:187], 0\n"
    "ds_read_b64_tr_b16 a[182:183], v2 offset:6144\n"
    "v_mul_f32 v252, s52, v252\n"
    "v_mfma_f32_16x16x32_bf16 a[196:199], a[168:171], v[176:179], a[196:199]\n"
    "ds_read_b64_tr_b16 a[184:185], v2 offset:8192\n"
    "v_mul_f32 v253, s52, v253\n"
    "v_mfma_f32_16x16x32_bf16 a[196:199], a[164:167], v[168:171], a[196:199]\n"
    "ds_read_b64_tr_b16 a[186:187], v2 offset:10240\n"
    "v_mul_f32 v254, s52, v254\n"
    "v_mfma_f32_16x16x32_bf16 a[196:199], a[160:163], v[160:163], a[196:199]\n"
    "ds_read_b64_tr_b16 a[188:189], v2 offset:12288\n"
    "v_mul_f32 v255, s52, v255\n"
    "v_mfma_f32_16x16x32_bf16 a[200:203], a[160:163], a[132:135], 0\n"
    "ds_read_b64_tr_b16 a[190:191], v2 offset:14336\n"
    "v_mul_f32 v16, s52, v16\n"
    "v_mfma_f32_16x16x32_bf16 a[200:203], a[164:167], a[140:143], a[200:203]\n"
    "buffer_load_dwordx4 v3, s[44:47], s57 offen offset:384 nt lds\n"
    "v_mfma_f32_16x16x32_bf16 a[200:203], a[168:171], a[148:151], a[200:203]\n"
    "v_exp_f32 v248, v248\n"
    "v_mfma_f32_16x16x32_bf16 a[200:203], a[172:175], a[156:159], a[200:203]\n"
    "v_exp_f32 v249, v249\n"
    "v_mfma_f32_16x16x32_bf16 a[204:207], a[172:175], v[188:191], 0\n"
    "v_cvt_pk_bf16_f32 v248, v248, v249\n"
    "v_mul_f32 v17, s52, v17\n"
    "v_mfma_f32_16x16x32_bf16 a[204:207], a[168:171], v[180:183], a[204:207]\n"
    "v_exp_f32 v250, v250\n"
    "v_mfma_f32_16x16x32_bf16 a[204:207], a[164:167], v[172:175], a[204:207]\n"
    "v_exp_f32 v251, v251\n"
    "v_mfma_f32_16x16x32_bf16 a[204:207], a[160:163], v[164:167], a[204:207]\n"
    "v_cvt_pk_bf16_f32 v249, v250, v251\n"
    "v_mul_f32 v18, s52, v18\n"
    "v_mfma_f32_16x16x32_bf16 a[240:243], v[12:15], v[224:227], a[240:243]\n"
    "s_waitcnt lgkmcnt(0)\n"
    "s_xor_b32 s57, s54, 0x80\n"
    "v_mul_f32 v19, s52, v19\n"
    "v_mfma_f32_16x16x32_bf16 a[220:223], a[176:179], v[164:167], 0\n"
    "v_exp_f32 v252, v252\n"
    "v_mfma_f32_16x16x32_bf16 a[220:223], a[180:183], v[172:175], a[220:223]\n"
    "ds_read_b64_tr_b16 a[160:161], v1 offset:512\n"
    "v_mul_f32 v20, s52, v20\n"
    "v_mfma_f32_16x16x32_bf16 a[220:223], a[184:187], v[180:183], a[220:223]\n"
    "ds_read_b64_tr_b16 a[162:163], v1 offset:2560\n"
    "v_mul_f32 v21, s52, v21\n"
    "v_mfma_f32_16x16x32_bf16 a[220:223], a[188:191], v[188:191], a[220:223]\n"
    "ds_read_b64_tr_b16 a[164:165], v1 offset:4608\n"
    "v_mul_f32 v22, s52, v22\n"
    "v_mfma_f32_16x16x32_bf16 a[216:219], a[188:191], a[156:159], 0\n"
    "ds_read_b64_tr_b16 a[166:167], v1 offset:6656\n"
    "v_mul_f32 v23, s52, v23\n"
    "v_mfma_f32_16x16x32_bf16 a[216:219], a[184:187], a[148:151], a[216:219]\n"
    "ds_read_b64_tr_b16 a[168:169], v1 offset:8704\n"
    "v_mul_f32 v24, s52, v24\n"
    "v_mfma_f32_16x16x32_bf16 a[216:219], a[180:183], a[140:143], a[216:219]\n"
    "ds_read_b64_tr_b16 a[170:171], v1 offset:10752\n"
    "v_mul_f32 v25, s52, v25\n"
    "v_mfma_f32_16x16x32_bf16 a[216:219], a[176:179], a[132:135], a[216:219]\n"
    "ds_read_b64_tr_b16 a[172:173], v1 offset:12800\n"
    "v_mul_f32 v26, s52, v26\n"
    "v_mfma_f32_16x16x32_bf16 a[212:215], a[176:179], v[160:163], 0\n"
    "ds_read_b64_tr_b16 a[174:175], v1 offset:14848\n"
    "v_mul_f32 v27, s52, v27\n"
    "v_mfma_f32_16x16x32_bf16 a[212:215], a[180:183], v[168:171], a[212:215]\n"
    "v_exp_f32 v253, v253\n"
    "v_mfma_f32_16x16x32_bf16 a[212:215], a[184:187], v[176:179], a[212:215]\n"
    "v_cvt_pk_bf16_f32 v252, v252, v253\n"
    "v_mul_f32 v28, s52, v28\n"
    "v_mfma_f32_16x16x32_bf16 a[212:215], a[188:191], v[184:187], a[212:215]\n"
    "v_exp_f32 v254, v254\n"
    "v_mfma_f32_16x16x32_bf16 a[208:211], a[188:191], a[152:155], 0\n"
    "v_exp_f32 v255, v255\n"
    "v_mfma_f32_16x16x32_bf16 a[208:211], a[184:187], a[144:147], a[208:211]\n"
    "v_cvt_pk_bf16_f32 v253, v254, v255\n"
    "v_mul_f32 v29, s52, v29\n"
    "v_mfma_f32_16x16x32_bf16 a[208:211], a[180:183], a[136:139], a[208:211]\n"
    "v_exp_f32 v16, v16\n"
    "v_mfma_f32_16x16x32_bf16 a[208:211], a[176:179], a[128:131], a[208:211]\n"
    "v_exp_f32 v17, v17\n"
    "v_mfma_f32_16x16x32_bf16 a[244:247], v[12:15], v[228:231], a[244:247]\n"
    "s_waitcnt lgkmcnt(0)\n"
    "s_add_u32 m0, s56, 0x0\n"
    "v_cvt_pk_bf16_f32 v242, v16, v17\n"
    "v_mfma_f32_16x16x32_bf16 a[224:227], a[160:163], a[128:131], 0\n"
    "v_exp_f32 v18, v18\n"
    "v_mfma_f32_16x16x32_bf16 a[224:227], a[164:167], a[136:139], a[224:227]\n"
    "ds_read_b64_tr_b16 a[176:177], v2 offset:512\n"
    "v_mul_f32 v30, s52, v30\n"
    "v_mfma_f32_16x16x32_bf16 a[224:227], a[168:171], a[144:147], a[224:227]\n"
    "ds_read_b64_tr_b16 a[178:179], v2 offset:2560\n"
    "v_mul_f32 v31, s52, v31\n"
    "v_mfma_f32_16x16x32_bf16 a[224:227], a[172:175], a[152:155], a[224:227]\n"
    "ds_read_b64_tr_b16 a[180:181], v2 offset:4608\n"
    "v_mfma_f32_16x16x32_bf16 a[228:231], a[172:175], v[184:187], 0\n"
    "ds_read_b64_tr_b16 a[182:183], v2 offset:6656\n"
    "v_mfma_f32_16x16x32_bf16 a[228:231], a[168:171], v[176:179], a[228:231]\n"
    "ds_read_b64_tr_b16 a[184:185], v2 offset:8704\n"
    "v_mfma_f32_16x16x32_bf16 a[228:231], a[164:167], v[168:171], a[228:231]\n"
    "ds_read_b64_tr_b16 a[186:187], v2 offset:10752\n"
    "v_mfma_f32_16x16x32_bf16 a[228:231], a[160:163], v[160:163], a[228:231]\n"
    "ds_read_b64_tr_b16 a[188:189], v2 offset:12800\n"
    "v_mfma_f32_16x16x32_bf16 a[232:235], a[160:163], a[132:135], 0\n"
    "ds_read_b64_tr_b16 a[190:191], v2 offset:14848\n"
    "v_mfma_f32_16x16x32_bf16 a[232:235], a[164:167], a[140:143], a[232:235]\n"
    "buffer_load_dwordx4 a[96:99], v5, s[44:47], s63 offen offset:0 nt\n"
    "v_mfma_f32_16x16x32_bf16 a[232:235], a[168:171], a[148:151], a[232:235]\n"
    "buffer_load_dwordx4 a[100:103], v5, s[44:47], s63 offen offset:64 nt\n"
    "v_mfma_f32_16x16x32_bf16 a[232:235], a[172:175], a[156:159], a[232:235]\n"
    "v_exp_f32 v19, v19\n"
    "v_mfma_f32_16x16x32_bf16 a[236:239], a[172:175], v[188:191], 0\n"
    "v_exp_f32 v20, v20\n"
    "v_cvt_pk_bf16_f32 v243, v18, v19\n"
    "v_mfma_f32_16x16x32_bf16 a[236:239], a[168:171], v[180:183], a[236:239]\n"
    "v_exp_f32 v21, v21\n"
    "v_mfma_f32_16x16x32_bf16 a[236:239], a[164:167], v[172:175], a[236:239]\n"
    "v_exp_f32 v22, v22\n"
    "v_cvt_pk_bf16_f32 v246, v20, v21\n"
    "v_mfma_f32_16x16x32_bf16 a[236:239], a[160:163], v[164:167], a[236:239]\n"
    "v_exp_f32 v23, v23\n"
    "v_mfma_f32_16x16x32_bf16 a[248:251], v[12:15], v[232:235], a[248:251]\n"
    "s_waitcnt vmcnt(16) lgkmcnt(0)\n"
    "v_mfma_f32_16x16x32_bf16 a[76:79], a[176:179], v[164:167], 0\n"
    "s_barrier\n"
    "v_mfma_f32_16x16x32_bf16 a[76:79], a[180:183], v[172:175], a[76:79]\n"
    "ds_read_b64_tr_b16 a[160:161], v1 offset:16384\n"
    "v_mfma_f32_16x16x32_bf16 a[76:79], a[184:187], v[180:183], a[76:79]\n"
    "ds_read_b64_tr_b16 a[162:163], v1 offset:18432\n"
    "v_cvt_pk_bf16_f32 v247, v22, v23\n"
    "v_mfma_f32_16x16x32_bf16 a[76:79], a[188:191], v[188:191], a[76:79]\n"
    "ds_read_b64_tr_b16 a[164:165], v1 offset:20480\n"
    "v_mfma_f32_16x16x32_bf16 a[72:75], a[188:191], a[156:159], 0\n"
    "ds_read_b64_tr_b16 a[166:167], v1 offset:22528\n"
    "v_mfma_f32_16x16x32_bf16 a[72:75], a[184:187], a[148:151], a[72:75]\n"
    "ds_read_b64_tr_b16 a[168:169], v1 offset:24576\n"
    "v_mfma_f32_16x16x32_bf16 a[72:75], a[180:183], a[140:143], a[72:75]\n"
    "ds_read_b64_tr_b16 a[170:171], v1 offset:26624\n"
    "v_mfma_f32_16x16x32_bf16 a[72:75], a[176:179], a[132:135], a[72:75]\n"
    "ds_read_b64_tr_b16 a[172:173], v1 offset:28672\n"
    "v_mfma_f32_16x16x32_bf16 a[68:71], a[176:179], v[160:163], 0\n"
    "ds_read_b64_tr_b16 a[174:175], v1 offset:30720\n"
    "v_mfma_f32_16x16x32_bf16 a[68:71], a[180:183], v[168:171], a[68:71]\n"
    "buffer_load_dwordx4 v4, s[40:43], s57 offen offset:0 lds\n"
    "v_mfma_f32_16x16x32_bf16 a[68:71], a[184:187], v[176:179], a[68:71]\n"
    "v_exp_f32 v24, v24\n"
    "v_mfma_f32_16x16x32_bf16 a[68:71], a[188:191], v[184:187], a[68:71]\n"
    "v_exp_f32 v25, v25\n"
    "v_mfma_f32_16x16x32_bf16 a[64:67], a[188:191], a[152:155], 0\n"
    "s_add_u32 m0, s56, 0x400\n"
    "s_xor_b32 s57, s54, 0x1080\n"
    "v_cvt_pk_bf16_f32 v250, v24, v25\n"
    "v_mfma_f32_16x16x32_bf16 a[64:67], a[184:187], a[144:147], a[64:67]\n"
    "buffer_load_dwordx4 v4, s[40:43], s57 offen offset:0 lds\n"
    "v_mfma_f32_16x16x32_bf16 a[64:67], a[180:183], a[136:139], a[64:67]\n"
    "v_exp_f32 v26, v26\n"
    "v_mfma_f32_16x16x32_bf16 a[64:67], a[176:179], a[128:131], a[64:67]\n"
    "v_exp_f32 v27, v27\n"
    "v_mfma_f32_16x16x32_bf16 a[252:255], v[12:15], v[236:239], a[252:255]\n"
    "s_waitcnt lgkmcnt(0)\n"
    "s_add_u32 m0, s56, 0x800\n"
    "v_cvt_pk_bf16_f32 v251, v26, v27\n"
    "v_mfma_f32_16x16x32_bf16 a[192:195], a[160:163], v[32:35], a[192:195]\n"
    "s_xor_b32 s57, s54, 0x2080\n"
    "v_exp_f32 v28, v28\n"
    "v_mfma_f32_16x16x32_bf16 a[192:195], a[164:167], v[48:51], a[192:195]\n"
    "ds_read_b64_tr_b16 a[176:177], v2 offset:16384\n"
    "v_mfma_f32_16x16x32_bf16 a[192:195], a[168:171], v[64:67], a[192:195]\n"
    "ds_read_b64_tr_b16 a[178:179], v2 offset:18432\n"
    "v_mfma_f32_16x16x32_bf16 a[192:195], a[172:175], v[80:83], a[192:195]\n"
    "ds_read_b64_tr_b16 a[180:181], v2 offset:20480\n"
    "v_mfma_f32_16x16x32_bf16 a[196:199], a[172:175], v[84:87], a[196:199]\n"
    "ds_read_b64_tr_b16 a[182:183], v2 offset:22528\n"
    "v_mfma_f32_16x16x32_bf16 a[196:199], a[168:171], v[68:71], a[196:199]\n"
    "ds_read_b64_tr_b16 a[184:185], v2 offset:24576\n"
    "v_mfma_f32_16x16x32_bf16 a[196:199], a[164:167], v[52:55], a[196:199]\n"
    "ds_read_b64_tr_b16 a[186:187], v2 offset:26624\n"
    "v_mfma_f32_16x16x32_bf16 a[196:199], a[160:163], v[36:39], a[196:199]\n"
    "ds_read_b64_tr_b16 a[188:189], v2 offset:28672\n"
    "v_mfma_f32_16x16x32_bf16 a[200:203], a[160:163], v[40:43], a[200:203]\n"
    "ds_read_b64_tr_b16 a[190:191], v2 offset:30720\n"
    "v_mfma_f32_16x16x32_bf16 a[200:203], a[164:167], v[56:59], a[200:203]\n"
    "buffer_load_dwordx4 v4, s[40:43], s57 offen offset:0 lds\n"
    "v_mfma_f32_16x16x32_bf16 a[200:203], a[168:171], v[72:75], a[200:203]\n"
    "v_exp_f32 v29, v29\n"
    "v_mfma_f32_16x16x32_bf16 a[200:203], a[172:175], v[88:91], a[200:203]\n"
    "v_cvt_pk_bf16_f32 v254, v28, v29\n"
    "v_accvgpr_write_b32 a0, v96\n"
    "v_mfma_f32_16x16x32_bf16 a[204:207], a[172:175], v[92:95], a[204:207]\n"
    "s_add_u32 m0, s56, 0xc00\n"
    "s_xor_b32 s57, s54, 0x3080\n"
    "v_accvgpr_write_b32 a1, v97\n"
    "v_mfma_f32_16x16x32_bf16 a[204:207], a[168:171], v[76:79], a[204:207]\n"
    "buffer_load_dwordx4 v4, s[40:43], s57 offen offset:0 lds\n"
    "v_mfma_f32_16x16x32_bf16 a[204:207], a[164:167], v[60:63], a[204:207]\n"
    "v_exp_f32 v30, v30\n"
    "v_mfma_f32_16x16x32_bf16 a[204:207], a[160:163], v[44:47], a[204:207]\n"
    "v_exp_f32 v31, v31\n"
    "v_mfma_f32_16x16x32_bf16 a[240:243], v[12:15], v[240:243], a[240:243]\n"
    "s_waitcnt lgkmcnt(0)\n"
    "s_xor_b32 s57, s54, 0x20080\n"
    "v_cvt_pk_bf16_f32 v255, v30, v31\n"
    "v_mfma_f32_16x16x32_bf16 a[220:223], a[176:179], v[44:47], a[220:223]\n"
    "v_accvgpr_write_b32 a2, v98\n"
    "v_accvgpr_write_b32 a3, v99\n"
    "v_mfma_f32_16x16x32_bf16 a[220:223], a[180:183], v[60:63], a[220:223]\n"
    "ds_read_b64_tr_b16 a[160:161], v1 offset:16896\n"
    "v_accvgpr_write_b32 a4, v100\n"
    "v_mfma_f32_16x16x32_bf16 a[220:223], a[184:187], v[76:79], a[220:223]\n"
    "ds_read_b64_tr_b16 a[162:163], v1 offset:18944\n"
    "v_accvgpr_write_b32 a5, v101\n"
    "v_mfma_f32_16x16x32_bf16 a[220:223], a[188:191], v[92:95], a[220:223]\n"
    "ds_read_b64_tr_b16 a[164:165], v1 offset:20992\n"
    "v_accvgpr_read_b32 v8, a240\n"
    "v_mfma_f32_16x16x32_bf16 a[216:219], a[188:191], v[88:91], a[216:219]\n"
    "ds_read_b64_tr_b16 a[166:167], v1 offset:23040\n"
    "v_accvgpr_write_b32 a6, v102\n"
    "v_mfma_f32_16x16x32_bf16 a[216:219], a[184:187], v[72:75], a[216:219]\n"
    "ds_read_b64_tr_b16 a[168:169], v1 offset:25088\n"
    "v_accvgpr_write_b32 a7, v103\n"
    "v_mfma_f32_16x16x32_bf16 a[216:219], a[180:183], v[56:59], a[216:219]\n"
    "ds_read_b64_tr_b16 a[170:171], v1 offset:27136\n"
    "v_accvgpr_write_b32 a8, v104\n"
    "v_mfma_f32_16x16x32_bf16 a[216:219], a[176:179], v[40:43], a[216:219]\n"
    "ds_read_b64_tr_b16 a[172:173], v1 offset:29184\n"
    "v_accvgpr_write_b32 a9, v105\n"
    "v_mfma_f32_16x16x32_bf16 a[212:215], a[176:179], v[36:39], a[212:215]\n"
    "ds_read_b64_tr_b16 a[174:175], v1 offset:31232\n"
    "v_accvgpr_write_b32 a10, v106\n"
    "v_mfma_f32_16x16x32_bf16 a[212:215], a[180:183], v[52:55], a[212:215]\n"
    "buffer_load_dwordx4 a[104:107], v5, s[44:47], s63 offen offset:128 nt\n"
    "v_mfma_f32_16x16x32_bf16 a[212:215], a[184:187], v[68:71], a[212:215]\n"
    "buffer_load_dwordx4 a[108:111], v5, s[44:47], s63 offen offset:192 nt\n"
    "v_mfma_f32_16x16x32_bf16 a[212:215], a[188:191], v[84:87], a[212:215]\n"
    "v_rcp_f32 v8, v8\n"
    "v_mfma_f32_16x16x32_bf16 a[208:211], a[188:191], v[80:83], a[208:211]\n"
    "v_accvgpr_write_b32 a11, v107\n"
    "v_accvgpr_write_b32 a12, v108\n"
    "v_mfma_f32_16x16x32_bf16 a[208:211], a[184:187], v[64:67], a[208:211]\n"
    "v_accvgpr_write_b32 a13, v109\n"
    "v_mfma_f32_16x16x32_bf16 a[208:211], a[180:183], v[48:51], a[208:211]\n"
    "v_accvgpr_write_b32 a14, v110\n"
    "v_mfma_f32_16x16x32_bf16 a[208:211], a[176:179], v[32:35], a[208:211]\n"
    "v_accvgpr_write_b32 a15, v111\n"
    "v_mfma_f32_16x16x32_bf16 a[244:247], v[12:15], v[244:247], a[244:247]\n"
    "s_waitcnt lgkmcnt(0)\n"
    "s_add_u32 m0, s56, 0x4000\n"
    "v_mfma_f32_16x16x32_bf16 a[224:227], a[160:163], v[32:35], a[224:227]\n"
    "v_mfma_f32_16x16x32_bf16 a[224:227], a[164:167], v[48:51], a[224:227]\n"
    "ds_read_b64_tr_b16 a[176:177], v2 offset:16896\n"
    "v_accvgpr_write_b32 a16, v112\n"
    "v_mfma_f32_16x16x32_bf16 a[224:227], a[168:171], v[64:67], a[224:227]\n"
    "ds_read_b64_tr_b16 a[178:179], v2 offset:18944\n"
    "v_accvgpr_write_b32 a17, v113\n"
    "v_mfma_f32_16x16x32_bf16 a[224:227], a[172:175], v[80:83], a[224:227]\n"
    "ds_read_b64_tr_b16 a[180:181], v2 offset:20992\n"
    "v_accvgpr_read_b32 v9, a244\n"
    "v_mfma_f32_16x16x32_bf16 a[228:231], a[172:175], v[84:87], a[228:231]\n"
    "ds_read_b64_tr_b16 a[182:183], v2 offset:23040\n"
    "v_accvgpr_write_b32 a18, v114\n"
    "v_mfma_f32_16x16x32_bf16 a[228:231], a[168:171], v[68:71], a[228:231]\n"
    "ds_read_b64_tr_b16 a[184:185], v2 offset:25088\n"
    "v_accvgpr_write_b32 a19, v115\n"
    "v_mfma_f32_16x16x32_bf16 a[228:231], a[164:167], v[52:55], a[228:231]\n"
    "ds_read_b64_tr_b16 a[186:187], v2 offset:27136\n"
    "v_mfma_f32_16x16x32_bf16 a[228:231], a[160:163], v[36:39], a[228:231]\n"
    "ds_read_b64_tr_b16 a[188:189], v2 offset:29184\n"
    "v_accvgpr_write_b32 a20, v116\n"
    "v_mfma_f32_16x16x32_bf16 a[232:235], a[160:163], v[40:43], a[232:235]\n"
    "ds_read_b64_tr_b16 a[190:191], v2 offset:31232\n"
    "v_accvgpr_write_b32 a21, v117\n"
    "v_mfma_f32_16x16x32_bf16 a[232:235], a[164:167], v[56:59], a[232:235]\n"
    "v_rcp_f32 v9, v9\n"
    "v_mfma_f32_16x16x32_bf16 a[232:235], a[168:171], v[72:75], a[232:235]\n"
    "v_accvgpr_write_b32 a22, v118\n"
    "v_accvgpr_write_b32 a23, v119\n"
    "v_mfma_f32_16x16x32_bf16 a[232:235], a[172:175], v[88:91], a[232:235]\n"
    "v_mfma_f32_16x16x32_bf16 a[236:239], a[172:175], v[92:95], a[236:239]\n"
    "v_mfma_f32_16x16x32_bf16 a[236:239], a[168:171], v[76:79], a[236:239]\n"
    "v_accvgpr_write_b32 a24, v120\n"
    "v_mfma_f32_16x16x32_bf16 a[236:239], a[164:167], v[60:63], a[236:239]\n"
    "v_accvgpr_write_b32 a25, v121\n"
    "v_mfma_f32_16x16x32_bf16 a[236:239], a[160:163], v[44:47], a[236:239]\n"
    "v_accvgpr_write_b32 a26, v122\n"
    "v_mfma_f32_16x16x32_bf16 a[248:251], v[12:15], v[248:251], a[248:251]\n"
    "s_waitcnt vmcnt(16) lgkmcnt(0)\n"
    "v_mfma_f32_16x16x32_bf16 a[76:79], a[176:179], v[44:47], a[76:79]\n"
    "s_barrier\n"
    "v_mfma_f32_16x16x32_bf16 a[76:79], a[180:183], v[60:63], a[76:79]\n"
    "ds_read_b64_tr_b16 a[160:161], v1 offset:32768\n"
    "v_mfma_f32_16x16x32_bf16 a[76:79], a[184:187], v[76:79], a[76:79]\n"
    "ds_read_b64_tr_b16 a[162:163], v1 offset:34816\n"
    "v_accvgpr_write_b32 a27, v123\n"
    "v_mfma_f32_16x16x32_bf16 a[76:79], a[188:191], v[92:95], a[76:79]\n"
    "ds_read_b64_tr_b16 a[164:165], v1 offset:36864\n"
    "v_accvgpr_read_b32 v10, a248\n"
    "v_mfma_f32_16x16x32_bf16 a[72:75], a[188:191], v[88:91], a[72:75]\n"
    "ds_read_b64_tr_b16 a[166:167], v1 offset:38912\n"
    "v_accvgpr_write_b32 a28, v124\n"
    "v_mfma_f32_16x16x32_bf16 a[72:75], a[184:187], v[72:75], a[72:75]\n"
    "ds_read_b64_tr_b16 a[168:169], v1 offset:40960\n"
    "v_accvgpr_write_b32 a29, v125\n"
    "v_mfma_f32_16x16x32_bf16 a[72:75], a[180:183], v[56:59], a[72:75]\n"
    "ds_read_b64_tr_b16 a[170:171], v1 offset:43008\n"
    "v_accvgpr_write_b32 a30, v126\n"
    "v_mfma_f32_16x16x32_bf16 a[72:75], a[176:179], v[40:43], a[72:75]\n"
    "ds_read_b64_tr_b16 a[172:173], v1 offset:45056\n"
    "v_accvgpr_write_b32 a31, v127\n"
    "v_mfma_f32_16x16x32_bf16 a[68:71], a[176:179], v[36:39], a[68:71]\n"
    "ds_read_b64_tr_b16 a[174:175], v1 offset:47104\n"
    "v_accvgpr_write_b32 a32, v128\n"
    "v_mfma_f32_16x16x32_bf16 a[68:71], a[180:183], v[52:55], a[68:71]\n"
    "buffer_load_dwordx4 v4, s[40:43], s57 offen offset:0 lds\n"
    "v_mfma_f32_16x16x32_bf16 a[68:71], a[184:187], v[68:71], a[68:71]\n"
    "v_rcp_f32 v10, v10\n"
    "v_mfma_f32_16x16x32_bf16 a[68:71], a[188:191], v[84:87], a[68:71]\n"
    "v_accvgpr_write_b32 a33, v129\n"
    "v_accvgpr_write_b32 a34, v130\n"
    "v_mfma_f32_16x16x32_bf16 a[64:67], a[188:191], v[80:83], a[64:67]\n"
    "s_add_u32 m0, s56, 0x4400\n"
    "s_xor_b32 s57, s54, 0x21080\n"
    "v_accvgpr_write_b32 a35, v131\n"
    "v_mfma_f32_16x16x32_bf16 a[64:67], a[184:187], v[64:67], a[64:67]\n"
    "buffer_load_dwordx4 v4, s[40:43], s57 offen offset:0 lds\n"
    "v_mfma_f32_16x16x32_bf16 a[64:67], a[180:183], v[48:51], a[64:67]\n"
    "v_accvgpr_write_b32 a36, v132\n"
    "v_mfma_f32_16x16x32_bf16 a[64:67], a[176:179], v[32:35], a[64:67]\n"
    "v_accvgpr_write_b32 a37, v133\n"
    "v_mfma_f32_16x16x32_bf16 a[252:255], v[12:15], v[252:255], a[252:255]\n"
    "s_waitcnt lgkmcnt(0)\n"
    "s_add_u32 m0, s56, 0x4800\n"
    "v_accvgpr_write_b32 a38, v134\n"
    "v_mfma_f32_16x16x32_bf16 a[192:195], a[160:163], v[96:99], a[192:195]\n"
    "s_xor_b32 s57, s54, 0x22080\n"
    "v_accvgpr_write_b32 a39, v135\n"
    "v_mfma_f32_16x16x32_bf16 a[192:195], a[164:167], v[112:115], a[192:195]\n"
    "ds_read_b64_tr_b16 a[176:177], v2 offset:32768\n"
    "v_mfma_f32_16x16x32_bf16 a[192:195], a[168:171], v[128:131], a[192:195]\n"
    "ds_read_b64_tr_b16 a[178:179], v2 offset:34816\n"
    "v_mfma_f32_16x16x32_bf16 a[192:195], a[172:175], v[144:147], a[192:195]\n"
    "ds_read_b64_tr_b16 a[180:181], v2 offset:36864\n"
    "v_accvgpr_read_b32 v11, a252\n"
    "v_mfma_f32_16x16x32_bf16 a[196:199], a[172:175], v[148:151], a[196:199]\n"
    "ds_read_b64_tr_b16 a[182:183], v2 offset:38912\n"
    "v_accvgpr_write_b32 a40, v136\n"
    "v_mfma_f32_16x16x32_bf16 a[196:199], a[168:171], v[132:135], a[196:199]\n"
    "ds_read_b64_tr_b16 a[184:185], v2 offset:40960\n"
    "v_accvgpr_write_b32 a41, v137\n"
    "v_mfma_f32_16x16x32_bf16 a[196:199], a[164:167], v[116:119], a[196:199]\n"
    "ds_read_b64_tr_b16 a[186:187], v2 offset:43008\n"
    "v_accvgpr_write_b32 a42, v138\n"
    "v_mfma_f32_16x16x32_bf16 a[196:199], a[160:163], v[100:103], a[196:199]\n"
    "ds_read_b64_tr_b16 a[188:189], v2 offset:45056\n"
    "v_accvgpr_write_b32 a43, v139\n"
    "v_mfma_f32_16x16x32_bf16 a[200:203], a[160:163], v[104:107], a[200:203]\n"
    "ds_read_b64_tr_b16 a[190:191], v2 offset:47104\n"
    "v_mfma_f32_16x16x32_bf16 a[200:203], a[164:167], v[120:123], a[200:203]\n"
    "buffer_load_dwordx4 v4, s[40:43], s57 offen offset:0 lds\n"
    "v_mfma_f32_16x16x32_bf16 a[200:203], a[168:171], v[136:139], a[200:203]\n"
    "v_rcp_f32 v11, v11\n"
    "v_mfma_f32_16x16x32_bf16 a[200:203], a[172:175], v[152:155], a[200:203]\n"
    "v_accvgpr_write_b32 a44, v140\n"
    "v_accvgpr_write_b32 a45, v141\n"
    "v_mfma_f32_16x16x32_bf16 a[204:207], a[172:175], v[156:159], a[204:207]\n"
    "s_add_u32 m0, s56, 0x4c00\n"
    "s_xor_b32 s57, s54, 0x23080\n"
    "v_accvgpr_write_b32 a46, v142\n"
    "v_mfma_f32_16x16x32_bf16 a[204:207], a[168:171], v[140:143], a[204:207]\n"
    "buffer_load_dwordx4 v4, s[40:43], s57 offen offset:0 lds\n"
    "v_mfma_f32_16x16x32_bf16 a[204:207], a[164:167], v[124:127], a[204:207]\n"
    "v_accvgpr_write_b32 a47, v143\n"
    "v_mfma_f32_16x16x32_bf16 a[204:207], a[160:163], v[108:111], a[204:207]\n"
    "s_waitcnt lgkmcnt(0)\n"
    "s_xor_b32 s57, s54, 0x30080\n"
    "v_accvgpr_write_b32 a48, v144\n"
    "v_mfma_f32_16x16x32_bf16 a[220:223], a[176:179], v[108:111], a[220:223]\n"
    "v_accvgpr_write_b32 a49, v145\n"
    "v_mfma_f32_16x16x32_bf16 a[220:223], a[180:183], v[124:127], a[220:223]\n"
    "ds_read_b64_tr_b16 a[160:161], v1 offset:33280\n"
    "v_accvgpr_write_b32 a50, v146\n"
    "v_mfma_f32_16x16x32_bf16 a[220:223], a[184:187], v[140:143], a[220:223]\n"
    "ds_read_b64_tr_b16 a[162:163], v1 offset:35328\n"
    "v_accvgpr_write_b32 a51, v147\n"
    "v_mfma_f32_16x16x32_bf16 a[220:223], a[188:191], v[156:159], a[220:223]\n"
    "ds_read_b64_tr_b16 a[164:165], v1 offset:37376\n"
    "v_mfma_f32_16x16x32_bf16 a[216:219], a[188:191], v[152:155], a[216:219]\n"
    "ds_read_b64_tr_b16 a[166:167], v1 offset:39424\n"
    "v_mfma_f32_16x16x32_bf16 a[216:219], a[184:187], v[136:139], a[216:219]\n"
    "ds_read_b64_tr_b16 a[168:169], v1 offset:41472\n"
    "v_accvgpr_write_b32 a52, v148\n"
    "v_mfma_f32_16x16x32_bf16 a[216:219], a[180:183], v[120:123], a[216:219]\n"
    "ds_read_b64_tr_b16 a[170:171], v1 offset:43520\n"
    "v_accvgpr_write_b32 a53, v149\n"
    "v_mfma_f32_16x16x32_bf16 a[216:219], a[176:179], v[104:107], a[216:219]\n"
    "ds_read_b64_tr_b16 a[172:173], v1 offset:45568\n"
    "v_accvgpr_write_b32 a54, v150\n"
    "v_mfma_f32_16x16x32_bf16 a[212:215], a[176:179], v[100:103], a[212:215]\n"
    "ds_read_b64_tr_b16 a[174:175], v1 offset:47616\n"
    "v_accvgpr_write_b32 a55, v151\n"
    "v_mfma_f32_16x16x32_bf16 a[212:215], a[180:183], v[116:119], a[212:215]\n"
    "buffer_load_dwordx4 a[112:115], v5, s[44:47], s63 offen offset:256 nt\n"
    "v_mfma_f32_16x16x32_bf16 a[212:215], a[184:187], v[132:135], a[212:215]\n"
    "buffer_load_dwordx4 a[116:119], v5, s[44:47], s63 offen offset:320 nt\n"
    "v_mfma_f32_16x16x32_bf16 a[212:215], a[188:191], v[148:151], a[212:215]\n"
    "v_accvgpr_write_b32 a56, v152\n"
    "v_accvgpr_write_b32 a240, v80\n"
    "v_mfma_f32_16x16x32_bf16 a[208:211], a[188:191], v[144:147], a[208:211]\n"
    "v_accvgpr_write_b32 a57, v153\n"
    "v_accvgpr_write_b32 a241, v81\n"
    "v_mfma_f32_16x16x32_bf16 a[208:211], a[184:187], v[128:131], a[208:211]\n"
    "v_accvgpr_write_b32 a58, v154\n"
    "v_accvgpr_write_b32 a242, v82\n"
    "v_mfma_f32_16x16x32_bf16 a[208:211], a[180:183], v[112:115], a[208:211]\n"
    "v_accvgpr_write_b32 a59, v155\n"
    "v_accvgpr_write_b32 a243, v83\n"
    "v_mfma_f32_16x16x32_bf16 a[208:211], a[176:179], v[96:99], a[208:211]\n"
    "s_waitcnt lgkmcnt(0)\n"
    "s_add_u32 m0, s56, 0x8000\n"
    "v_accvgpr_write_b32 a244, v84\n"
    "v_mfma_f32_16x16x32_bf16 a[224:227], a[160:163], v[96:99], a[224:227]\n"
    "v_accvgpr_write_b32 a245, v85\n"
    "v_accvgpr_write_b32 a246, v86\n"
    "v_mfma_f32_16x16x32_bf16 a[224:227], a[164:167], v[112:115], a[224:227]\n"
    "ds_read_b64_tr_b16 a[176:177], v2 offset:33280\n"
    "v_accvgpr_write_b32 a60, v156\n"
    "v_mfma_f32_16x16x32_bf16 a[224:227], a[168:171], v[128:131], a[224:227]\n"
    "ds_read_b64_tr_b16 a[178:179], v2 offset:35328\n"
    "v_accvgpr_write_b32 a61, v157\n"
    "v_mfma_f32_16x16x32_bf16 a[224:227], a[172:175], v[144:147], a[224:227]\n"
    "ds_read_b64_tr_b16 a[180:181], v2 offset:37376\n"
    "v_accvgpr_write_b32 a62, v158\n"
    "v_mfma_f32_16x16x32_bf16 a[228:231], a[172:175], v[148:151], a[228:231]\n"
    "ds_read_b64_tr_b16 a[182:183], v2 offset:39424\n"
    "v_accvgpr_write_b32 a63, v159\n"
    "v_mfma_f32_16x16x32_bf16 a[228:231], a[168:171], v[132:135], a[228:231]\n"
    "ds_read_b64_tr_b16 a[184:185], v2 offset:41472\n"
    "v_accvgpr_write_b32 a247, v87\n"
    "v_mfma_f32_16x16x32_bf16 a[228:231], a[164:167], v[116:119], a[228:231]\n"
    "ds_read_b64_tr_b16 a[186:187], v2 offset:43520\n"
    "v_accvgpr_write_b32 a248, v88\n"
    "v_mfma_f32_16x16x32_bf16 a[228:231], a[160:163], v[100:103], a[228:231]\n"
    "ds_read_b64_tr_b16 a[188:189], v2 offset:45568\n"
    "v_accvgpr_write_b32 a249, v89\n"
    "v_mfma_f32_16x16x32_bf16 a[232:235], a[160:163], v[104:107], a[232:235]\n"
    "ds_read_b64_tr_b16 a[190:191], v2 offset:47616\n"
    "v_accvgpr_write_b32 a250, v90\n"
    "v_mfma_f32_16x16x32_bf16 a[232:235], a[164:167], v[120:123], a[232:235]\n"
    "v_accvgpr_write_b32 a251, v91\n"
    "v_accvgpr_write_b32 a252, v92\n"
    "v_mfma_f32_16x16x32_bf16 a[232:235], a[168:171], v[136:139], a[232:235]\n"
    "v_accvgpr_write_b32 a253, v93\n"
    "v_accvgpr_write_b32 a254, v94\n"
    "v_mfma_f32_16x16x32_bf16 a[232:235], a[172:175], v[152:155], a[232:235]\n"
    "v_accvgpr_write_b32 a255, v95\n"
    "v_mfma_f32_16x16x32_bf16 a[236:239], a[172:175], v[156:159], a[236:239]\n"
    "v_mfma_f32_16x16x32_bf16 a[236:239], a[168:171], v[140:143], a[236:239]\n"
    "v_mfma_f32_16x16x32_bf16 a[236:239], a[164:167], v[124:127], a[236:239]\n"
    "v_mfma_f32_16x16x32_bf16 a[236:239], a[160:163], v[108:111], a[236:239]\n"
    "s_waitcnt vmcnt(16) lgkmcnt(0)\n"
    "v_mfma_f32_16x16x32_bf16 a[76:79], a[176:179], v[108:111], a[76:79]\n"
    "s_barrier\n"
    "v_mfma_f32_16x16x32_bf16 a[76:79], a[180:183], v[124:127], a[76:79]\n"
    "ds_read_b64_tr_b16 a[160:161], v1 offset:49152\n"
    "v_mfma_f32_16x16x32_bf16 a[76:79], a[184:187], v[140:143], a[76:79]\n"
    "ds_read_b64_tr_b16 a[162:163], v1 offset:51200\n"
    "v_mfma_f32_16x16x32_bf16 a[76:79], a[188:191], v[156:159], a[76:79]\n"
    "ds_read_b64_tr_b16 a[164:165], v1 offset:53248\n"
    "v_mfma_f32_16x16x32_bf16 a[72:75], a[188:191], v[152:155], a[72:75]\n"
    "ds_read_b64_tr_b16 a[166:167], v1 offset:55296\n"
    "v_mfma_f32_16x16x32_bf16 a[72:75], a[184:187], v[136:139], a[72:75]\n"
    "ds_read_b64_tr_b16 a[168:169], v1 offset:57344\n"
    "v_mfma_f32_16x16x32_bf16 a[72:75], a[180:183], v[120:123], a[72:75]\n"
    "ds_read_b64_tr_b16 a[170:171], v1 offset:59392\n"
    "v_mfma_f32_16x16x32_bf16 a[72:75], a[176:179], v[104:107], a[72:75]\n"
    "ds_read_b64_tr_b16 a[172:173], v1 offset:61440\n"
    "v_mfma_f32_16x16x32_bf16 a[68:71], a[176:179], v[100:103], a[68:71]\n"
    "ds_read_b64_tr_b16 a[174:175], v1 offset:63488\n"
    "v_mfma_f32_16x16x32_bf16 a[68:71], a[180:183], v[116:119], a[68:71]\n"
    "buffer_load_dwordx4 v4, s[40:43], s57 offen offset:0 lds\n"
    "v_mfma_f32_16x16x32_bf16 a[68:71], a[184:187], v[132:135], a[68:71]\n"
    "v_mfma_f32_16x16x32_bf16 a[68:71], a[188:191], v[148:151], a[68:71]\n"
    "v_mfma_f32_16x16x32_bf16 a[64:67], a[188:191], v[144:147], a[64:67]\n"
    "s_add_u32 m0, s56, 0x8400\n"
    "s_xor_b32 s57, s54, 0x31080\n"
    "v_mfma_f32_16x16x32_bf16 a[64:67], a[184:187], v[128:131], a[64:67]\n"
    "buffer_load_dwordx4 v4, s[40:43], s57 offen offset:0 lds\n"
    "v_mfma_f32_16x16x32_bf16 a[64:67], a[180:183], v[112:115], a[64:67]\n"
    "L_pad9_%=:\n"
    "s_cbranch_execz L_ret2_%=\n"
    "v_mfma_f32_16x16x32_bf16 a[64:67], a[176:179], v[96:99], a[64:67]\n"
    "s_waitcnt lgkmcnt(0)\n"
    "s_add_u32 m0, s56, 0x8800\n"
    "v_mfma_f32_16x16x32_bf16 a[192:195], a[160:163], v[192:195], a[192:195]\n"
    "s_xor_b32 s57, s54, 0x32080\n"
    "v_mfma_f32_16x16x32_bf16 a[192:195], a[164:167], v[208:211], a[192:195]\n"
    "ds_read_b64_tr_b16 a[176:177], v2 offset:49152\n"
    "v_mfma_f32_16x16x32_bf16 a[192:195], a[168:171], v[224:227], a[192:195]\n"
    "ds_read_b64_tr_b16 a[178:179], v2 offset:51200\n"
    "v_mfma_f32_16x16x32_bf16 a[192:195], a[172:175], v[240:243], a[192:195]\n"
    "ds_read_b64_tr_b16 a[180:181], v2 offset:53248\n"
    "v_mfma_f32_16x16x32_bf16 a[196:199], a[172:175], v[244:247], a[196:199]\n"
    "ds_read_b64_tr_b16 a[182:183], v2 offset:55296\n"
    "v_mfma_f32_16x16x32_bf16 a[196:199], a[168:171], v[228:231], a[196:199]\n"
    "ds_read_b64_tr_b16 a[184:185], v2 offset:57344\n"
    "v_mfma_f32_16x16x32_bf16 a[196:199], a[164:167], v[212:215], a[196:199]\n"
    "ds_read_b64_tr_b16 a[186:187], v2 offset:59392\n"
    "s_nop 0\n"
    "v_accvgpr_read_b32 v16, a192\n"
    "v_mfma_f32_16x16x32_bf16 a[196:199], a[160:163], v[196:199], a[196:199]\n"
    "ds_read_b64_tr_b16 a[188:189], v2 offset:61440\n"
    "v_accvgpr_read_b32 v17, a193\n"
    "v_mfma_f32_16x16x32_bf16 a[200:203], a[160:163], v[200:203], a[200:203]\n"
    "ds_read_b64_tr_b16 a[190:191], v2 offset:63488\n"
    "v_accvgpr_read_b32 v18, a194\n"
    "v_mfma_f32_16x16x32_bf16 a[200:203], a[164:167], v[216:219], a[200:203]\n"
    "buffer_load_dwordx4 v4, s[40:43], s57 offen offset:0 lds\n"
    "v_mfma_f32_16x16x32_bf16 a[200:203], a[168:171], v[232:235], a[200:203]\n"
    "v_accvgpr_read_b32 v19, a195\n"
    "v_accvgpr_write_b32 a192, v32\n"
    "v_mfma_f32_16x16x32_bf16 a[200:203], a[172:175], v[248:251], a[200:203]\n"
    "v_accvgpr_write_b32 a193, v33\n"
    "v_accvgpr_write_b32 a194, v34\n"
    "v_mfma_f32_16x16x32_bf16 a[204:207], a[172:175], v[252:255], a[204:207]\n"
    "s_add_u32 m0, s56, 0x8c00\n"
    "s_xor_b32 s57, s54, 0x33080\n"
    "v_accvgpr_write_b32 a195, v35\n"
    "v_mfma_f32_16x16x32_bf16 a[204:207], a[168:171], v[236:239], a[204:207]\n"
    "buffer_load_dwordx4 v4, s[40:43], s57 offen offset:0 lds\n"
    "v_mfma_f32_16x16x32_bf16 a[204:207], a[164:167], v[220:223], a[204:207]\n"
    "v_mul_f32 v32, v8, v16\n"
    "v_mul_f32 v33, v8, v17\n"
    "v_mfma_f32_16x16x32_bf16 a[204:207], a[160:163], v[204:207], a[204:207]\n"
    "s_waitcnt lgkmcnt(0)\n"
    "s_xor_b32 s57, s54, 0x10080\n"
    "v_mul_f32 v34, v8, v18\n"
    "v_mfma_f32_16x16x32_bf16 a[220:223], a[176:179], v[204:207], a[220:223]\n"
    "v_mul_f32 v35, v8, v19\n"
    "v_cvt_pk_bf16_f32 v32, v32, v33\n"
    "v_mfma_f32_16x16x32_bf16 a[220:223], a[180:183], v[220:223], a[220:223]\n"
    "ds_read_b64_tr_b16 a[160:161], v1 offset:49664\n"
    "v_cvt_pk_bf16_f32 v33, v34, v35\n"
    "v_mfma_f32_16x16x32_bf16 a[220:223], a[184:187], v[236:239], a[220:223]\n"
    "ds_read_b64_tr_b16 a[162:163], v1 offset:51712\n"
    "v_accvgpr_read_b32 v20, a196\n"
    "v_mfma_f32_16x16x32_bf16 a[220:223], a[188:191], v[252:255], a[220:223]\n"
    "ds_read_b64_tr_b16 a[164:165], v1 offset:53760\n"
    "v_accvgpr_read_b32 v21, a197\n"
    "v_mfma_f32_16x16x32_bf16 a[216:219], a[188:191], v[248:251], a[216:219]\n"
    "ds_read_b64_tr_b16 a[166:167], v1 offset:55808\n"
    "v_accvgpr_read_b32 v22, a198\n"
    "v_mfma_f32_16x16x32_bf16 a[216:219], a[184:187], v[232:235], a[216:219]\n"
    "ds_read_b64_tr_b16 a[168:169], v1 offset:57856\n"
    "v_accvgpr_read_b32 v23, a199\n"
    "v_mfma_f32_16x16x32_bf16 a[216:219], a[180:183], v[216:219], a[216:219]\n"
    "ds_read_b64_tr_b16 a[170:171], v1 offset:59904\n"
    "v_accvgpr_write_b32 a196, v36\n"
    "v_mfma_f32_16x16x32_bf16 a[216:219], a[176:179], v[200:203], a[216:219]\n"
    "ds_read_b64_tr_b16 a[172:173], v1 offset:61952\n"
    "v_accvgpr_write_b32 a197, v37\n"
    "v_mfma_f32_16x16x32_bf16 a[212:215], a[176:179], v[196:199], a[212:215]\n"
    "ds_read_b64_tr_b16 a[174:175], v1 offset:64000\n"
    "v_accvgpr_write_b32 a198, v38\n"
    "v_mfma_f32_16x16x32_bf16 a[212:215], a[180:183], v[212:215], a[212:215]\n"
    "buffer_load_dwordx4 a[120:123], v5, s[44:47], s63 offen offset:384 nt\n"
    "v_mfma_f32_16x16x32_bf16 a[212:215], a[184:187], v[228:231], a[212:215]\n"
    "buffer_load_dwordx4 a[124:127], v5, s[44:47], s63 offen offset:448 nt\n"
    "v_mfma_f32_16x16x32_bf16 a[212:215], a[188:191], v[244:247], a[212:215]\n"
    "v_accvgpr_write_b32 a199, v39\n"
    "v_mul_f32 v36, v9, v20\n"
    "v_mfma_f32_16x16x32_bf16 a[208:211], a[188:191], v[240:243], a[208:211]\n"
    "v_mul_f32 v37, v9, v21\n"
    "v_mul_f32 v38, v9, v22\n"
    "v_mfma_f32_16x16x32_bf16 a[208:211], a[184:187], v[224:227], a[208:211]\n"
    "v_mul_f32 v39, v9, v23\n"
    "v_cvt_pk_bf16_f32 v36, v36, v37\n"
    "v_mfma_f32_16x16x32_bf16 a[208:211], a[180:183], v[208:211], a[208:211]\n"
    "v_accvgpr_read_b32 v20, a212\n"
    "v_accvgpr_read_b32 v21, a213\n"
    "v_mfma_f32_16x16x32_bf16 a[208:211], a[176:179], v[192:195], a[208:211]\n"
    "s_waitcnt lgkmcnt(0)\n"
    "s_add_u32 m0, s56, 0xc000\n"
    "v_accvgpr_read_b32 v22, a214\n"
    "v_mfma_f32_16x16x32_bf16 a[224:227], a[160:163], v[192:195], a[224:227]\n"
    "v_accvgpr_read_b32 v23, a215\n"
    "v_accvgpr_write_b32 a212, v52\n"
    "v_mfma_f32_16x16x32_bf16 a[224:227], a[164:167], v[208:211], a[224:227]\n"
    "ds_read_b64_tr_b16 a[176:177], v2 offset:49664\n"
    "v_accvgpr_write_b32 a213, v53\n"
    "v_mfma_f32_16x16x32_bf16 a[224:227], a[168:171], v[224:227], a[224:227]\n"
    "ds_read_b64_tr_b16 a[178:179], v2 offset:51712\n"
    "v_accvgpr_read_b32 v16, a208\n"
    "v_mfma_f32_16x16x32_bf16 a[224:227], a[172:175], v[240:243], a[224:227]\n"
    "ds_read_b64_tr_b16 a[180:181], v2 offset:53760\n"
    "v_accvgpr_read_b32 v17, a209\n"
    "v_mfma_f32_16x16x32_bf16 a[228:231], a[172:175], v[244:247], a[228:231]\n"
    "ds_read_b64_tr_b16 a[182:183], v2 offset:55808\n"
    "v_accvgpr_read_b32 v18, a210\n"
    "v_mfma_f32_16x16x32_bf16 a[228:231], a[168:171], v[228:231], a[228:231]\n"
    "ds_read_b64_tr_b16 a[184:185], v2 offset:57856\n"
    "v_accvgpr_read_b32 v19, a211\n"
    "v_mfma_f32_16x16x32_bf16 a[228:231], a[164:167], v[212:215], a[228:231]\n"
    "ds_read_b64_tr_b16 a[186:187], v2 offset:59904\n"
    "v_accvgpr_write_b32 a208, v48\n"
    "v_mfma_f32_16x16x32_bf16 a[228:231], a[160:163], v[196:199], a[228:231]\n"
    "ds_read_b64_tr_b16 a[188:189], v2 offset:61952\n"
    "v_accvgpr_write_b32 a209, v49\n"
    "v_mfma_f32_16x16x32_bf16 a[232:235], a[160:163], v[200:203], a[232:235]\n"
    "ds_read_b64_tr_b16 a[190:191], v2 offset:64000\n"
    "v_accvgpr_write_b32 a210, v50\n"
    "v_mfma_f32_16x16x32_bf16 a[232:235], a[164:167], v[216:219], a[232:235]\n"
    "v_accvgpr_write_b32 a211, v51\n"
    "v_mul_f32 v48, v8, v16\n"
    "v_mfma_f32_16x16x32_bf16 a[232:235], a[168:171], v[232:235], a[232:235]\n"
    "v_mul_f32 v49, v8, v17\n"
    "v_mul_f32 v50, v8, v18\n"
    "v_mfma_f32_16x16x32_bf16 a[232:235], a[172:175], v[248:251], a[232:235]\n"
    "v_mul_f32 v51, v8, v19\n"
    "v_accvgpr_read_b32 v16, a224\n"
    "v_mfma_f32_16x16x32_bf16 a[236:239], a[172:175], v[252:255], a[236:239]\n"
    "v_accvgpr_read_b32 v17, a225\n"
    "v_accvgpr_read_b32 v18, a226\n"
    "v_mfma_f32_16x16x32_bf16 a[236:239], a[168:171], v[236:239], a[236:239]\n"
    "v_accvgpr_read_b32 v19, a227\n"
    "v_accvgpr_write_b32 a224, v64\n"
    "v_mfma_f32_16x16x32_bf16 a[236:239], a[164:167], v[220:223], a[236:239]\n"
    "v_accvgpr_write_b32 a225, v65\n"
    "v_accvgpr_write_b32 a226, v66\n"
    "v_mfma_f32_16x16x32_bf16 a[236:239], a[160:163], v[204:207], a[236:239]\n"
    "s_waitcnt vmcnt(14) lgkmcnt(0)\n"
    "v_mfma_f32_16x16x32_bf16 a[76:79], a[176:179], v[204:207], a[76:79]\n"
    "s_barrier\n"
    "v_mfma_f32_16x16x32_bf16 a[76:79], a[180:183], v[220:223], a[76:79]\n"
    "ds_read_b64_tr_b16 a[160:161], v1 offset:0\n"
    "v_mfma_f32_16x16x32_bf16 a[76:79], a[184:187], v[236:239], a[76:79]\n"
    "ds_read_b64_tr_b16 a[162:163], v1 offset:2048\n"
    "v_accvgpr_write_b32 a227, v67\n"
    "v_mfma_f32_16x16x32_bf16 a[76:79], a[188:191], v[252:255], a[76:79]\n"
    "ds_read_b64_tr_b16 a[164:165], v1 offset:4096\n"
    "v_mul_f32 v64, v8, v16\n"
    "v_mfma_f32_16x16x32_bf16 a[72:75], a[188:191], v[248:251], a[72:75]\n"
    "ds_read_b64_tr_b16 a[166:167], v1 offset:6144\n"
    "v_mul_f32 v65, v8, v17\n"
    "v_mfma_f32_16x16x32_bf16 a[72:75], a[184:187], v[232:235], a[72:75]\n"
    "ds_read_b64_tr_b16 a[168:169], v1 offset:8192\n"
    "v_mul_f32 v66, v8, v18\n"
    "v_mfma_f32_16x16x32_bf16 a[72:75], a[180:183], v[216:219], a[72:75]\n"
    "ds_read_b64_tr_b16 a[170:171], v1 offset:10240\n"
    "v_mul_f32 v67, v8, v19\n"
    "v_mfma_f32_16x16x32_bf16 a[72:75], a[176:179], v[200:203], a[72:75]\n"
    "ds_read_b64_tr_b16 a[172:173], v1 offset:12288\n"
    "v_cvt_pk_bf16_f32 v64, v64, v65\n"
    "v_mfma_f32_16x16x32_bf16 a[68:71], a[176:179], v[196:199], a[68:71]\n"
    "ds_read_b64_tr_b16 a[174:175], v1 offset:14336\n"
    "v_cvt_pk_bf16_f32 v65, v66, v67\n"
    "v_mfma_f32_16x16x32_bf16 a[68:71], a[180:183], v[212:215], a[68:71]\n"
    "buffer_load_dwordx4 v4, s[40:43], s57 offen offset:0 lds\n"
    "v_mfma_f32_16x16x32_bf16 a[68:71], a[184:187], v[228:231], a[68:71]\n"
    "v_cvt_pk_bf16_f32 v34, v48, v49\n"
    "v_cvt_pk_bf16_f32 v35, v50, v51\n"
    "v_mfma_f32_16x16x32_bf16 a[68:71], a[188:191], v[244:247], a[68:71]\n"
    "v_permlane16_swap_b32 v32, v34\n"
    "v_mfma_f32_16x16x32_bf16 a[64:67], a[188:191], v[240:243], a[64:67]\n"
    "s_add_u32 m0, s56, 0xc400\n"
    "s_xor_b32 s57, s54, 0x11080\n"
    "v_mov_b32 v48, v32\n"
    "v_mfma_f32_16x16x32_bf16 a[64:67], a[184:187], v[224:227], a[64:67]\n"
    "buffer_load_dwordx4 v4, s[40:43], s57 offen offset:0 lds\n"
    "v_mfma_f32_16x16x32_bf16 a[64:67], a[180:183], v[208:211], a[64:67]\n"
    "v_permlane16_swap_b32 v33, v35\n"
    "v_mfma_f32_16x16x32_bf16 a[64:67], a[176:179], v[192:195], a[64:67]\n"
    "s_waitcnt lgkmcnt(0)\n"
    "s_add_u32 m0, s56, 0xc800\n"
    "v_mov_b32 v49, v33\n"
    "v_mfma_f32_16x16x32_bf16 v[96:99], a[160:163], a[128:131], 0\n"
    "s_xor_b32 s57, s54, 0x12080\n"
    "v_mov_b32 v50, v34\n"
    "v_mov_b32 v51, v35\n"
    "v_mfma_f32_16x16x32_bf16 v[96:99], a[164:167], a[136:139], v[96:99]\n"
    "ds_read_b64_tr_b16 a[176:177], v2 offset:0\n"
    "v_accvgpr_write_b32 a214, v54\n"
    "v_mfma_f32_16x16x32_bf16 v[96:99], a[168:171], a[144:147], v[96:99]\n"
    "ds_read_b64_tr_b16 a[178:179], v2 offset:2048\n"
    "v_accvgpr_read_b32 v80, a64\n"
    "v_mfma_f32_16x16x32_bf16 v[96:99], a[172:175], a[152:155], v[96:99]\n"
    "ds_read_b64_tr_b16 a[180:181], v2 offset:4096\n"
    "v_accvgpr_read_b32 v81, a65\n"
    "v_mfma_f32_16x16x32_bf16 v[100:103], a[172:175], v[184:187], 0\n"
    "ds_read_b64_tr_b16 a[182:183], v2 offset:6144\n"
    "v_accvgpr_read_b32 v82, a66\n"
    "v_mfma_f32_16x16x32_bf16 v[100:103], a[168:171], v[176:179], v[100:103]\n"
    "ds_read_b64_tr_b16 a[184:185], v2 offset:8192\n"
    "v_accvgpr_read_b32 v83, a67\n"
    "v_mfma_f32_16x16x32_bf16 v[100:103], a[164:167], v[168:171], v[100:103]\n"
    "ds_read_b64_tr_b16 a[186:187], v2 offset:10240\n"
    "v_mul_f32 v80, v8, v80\n"
    "v_mfma_f32_16x16x32_bf16 v[100:103], a[160:163], v[160:163], v[100:103]\n"
    "ds_read_b64_tr_b16 a[188:189], v2 offset:12288\n"
    "v_mul_f32 v81, v8, v81\n"
    "v_mfma_f32_16x16x32_bf16 v[104:107], a[160:163], a[132:135], 0\n"
    "ds_read_b64_tr_b16 a[190:191], v2 offset:14336\n"
    "v_mul_f32 v82, v8, v82\n"
    "v_mfma_f32_16x16x32_bf16 v[104:107], a[164:167], a[140:143], v[104:107]\n"
    "buffer_load_dwordx4 v4, s[40:43], s57 offen offset:0 lds\n"
    "v_mfma_f32_16x16x32_bf16 v[104:107], a[168:171], a[148:151], v[104:107]\n"
    "v_mul_f32 v83, v8, v83\n"
    "v_cvt_pk_bf16_f32 v66, v80, v81\n"
    "v_mfma_f32_16x16x32_bf16 v[104:107], a[172:175], a[156:159], v[104:107]\n"
    "v_cvt_pk_bf16_f32 v67, v82, v83\n"
    "v_accvgpr_write_b32 a215, v55\n"
    "v_mfma_f32_16x16x32_bf16 v[108:111], a[172:175], v[188:191], 0\n"
    "s_add_u32 m0, s56, 0xcc00\n"
    "s_xor_b32 s57, s54, 0x13080\n"
    "v_mul_f32 v52, v9, v20\n"
    "v_mfma_f32_16x16x32_bf16 v[108:111], a[168:171], v[180:183], v[108:111]\n"
    "buffer_load_dwordx4 v4, s[40:43], s57 offen offset:0 lds\n"
    "v_mfma_f32_16x16x32_bf16 v[108:111], a[164:167], v[172:175], v[108:111]\n"
    "v_permlane16_swap_b32 v64, v66\n"
    "v_mfma_f32_16x16x32_bf16 v[108:111], a[160:163], v[164:167], v[108:111]\n"
    "s_waitcnt lgkmcnt(0)\n"
    "s_xor_b32 s57, s54, 0x100\n"
    "v_mov_b32_dpp v32, v64 row_ror:8 row_mask:0xf bank_mask:0xc\n"
    "v_mfma_f32_16x16x32_bf16 v[124:127], a[176:179], v[164:167], 0\n"
    "v_permlane16_swap_b32 v65, v67\n"
    "v_mfma_f32_16x16x32_bf16 v[124:127], a[180:183], v[172:175], v[124:127]\n"
    "ds_read_b64_tr_b16 a[160:161], v1 offset:512\n"
    "v_mov_b32_dpp v33, v65 row_ror:8 row_mask:0xf bank_mask:0xc\n"
    "v_mfma_f32_16x16x32_bf16 v[124:127], a[184:187], v[180:183], v[124:127]\n"
    "ds_read_b64_tr_b16 a[162:163], v1 offset:2560\n"
    "v_mov_b32_dpp v34, v66 row_ror:8 row_mask:0xf bank_mask:0xc\n"
    "v_mfma_f32_16x16x32_bf16 v[124:127], a[188:191], v[188:191], v[124:127]\n"
    "ds_read_b64_tr_b16 a[164:165], v1 offset:4608\n"
    "v_mov_b32_dpp v35, v67 row_ror:8 row_mask:0xf bank_mask:0xc\n"
    "v_mfma_f32_16x16x32_bf16 v[120:123], a[188:191], a[156:159], 0\n"
    "ds_read_b64_tr_b16 a[166:167], v1 offset:6656\n"
    "v_mov_b32_dpp v64, v48 row_ror:8 row_mask:0xf bank_mask:0x3\n"
    "v_mfma_f32_16x16x32_bf16 v[120:123], a[184:187], a[148:151], v[120:123]\n"
    "ds_read_b64_tr_b16 a[168:169], v1 offset:8704\n"
    "v_mov_b32_dpp v65, v49 row_ror:8 row_mask:0xf bank_mask:0x3\n"
    "v_mfma_f32_16x16x32_bf16 v[120:123], a[180:183], a[140:143], v[120:123]\n"
    "ds_read_b64_tr_b16 a[170:171], v1 offset:10752\n"
    "v_mov_b32_dpp v66, v50 row_ror:8 row_mask:0xf bank_mask:0x3\n"
    "v_mfma_f32_16x16x32_bf16 v[120:123], a[176:179], a[132:135], v[120:123]\n"
    "ds_read_b64_tr_b16 a[172:173], v1 offset:12800\n"
    "v_mov_b32_dpp v67, v51 row_ror:8 row_mask:0xf bank_mask:0x3\n"
    "v_mfma_f32_16x16x32_bf16 v[116:119], a[176:179], v[160:163], 0\n"
    "ds_read_b64_tr_b16 a[174:175], v1 offset:14848\n"
    "v_mul_f32 v53, v9, v21\n"
    "v_mfma_f32_16x16x32_bf16 v[116:119], a[180:183], v[168:171], v[116:119]\n"
    "buffer_store_dwordx4 v[32:35], v6, s[48:51], s64 offen sc1\n"
    "v_mfma_f32_16x16x32_bf16 v[116:119], a[184:187], v[176:179], v[116:119]\n"
    "s_xor_b32 s64, s64, 0x80\n"
    "v_mul_f32 v54, v9, v22\n"
    "v_mul_f32 v55, v9, v23\n"
    "v_mfma_f32_16x16x32_bf16 v[116:119], a[188:191], v[184:187], v[116:119]\n"
    "v_accvgpr_read_b32 v20, a228\n"
    "v_accvgpr_read_b32 v21, a229\n"
    "v_mfma_f32_16x16x32_bf16 v[112:115], a[188:191], a[152:155], 0\n"
    "v_accvgpr_read_b32 v22, a230\n"
    "v_accvgpr_read_b32 v23, a231\n"
    "v_mfma_f32_16x16x32_bf16 v[112:115], a[184:187], a[144:147], v[112:115]\n"
    "v_accvgpr_write_b32 a228, v68\n"
    "v_accvgpr_write_b32 a229, v69\n"
    "v_mfma_f32_16x16x32_bf16 v[112:115], a[180:183], a[136:139], v[112:115]\n"
    "v_accvgpr_write_b32 a230, v70\n"
    "v_accvgpr_write_b32 a231, v71\n"
    "v_mfma_f32_16x16x32_bf16 v[112:115], a[176:179], a[128:131], v[112:115]\n"
    "s_waitcnt lgkmcnt(0)\n"
    "s_add_u32 m0, s56, 0x0\n"
    "v_accvgpr_read_b32 v84, a68\n"
    "v_mfma_f32_16x16x32_bf16 v[128:131], a[160:163], a[128:131], 0\n"
    "v_accvgpr_read_b32 v85, a69\n"
    "v_accvgpr_read_b32 v86, a70\n"
    "v_mfma_f32_16x16x32_bf16 v[128:131], a[164:167], a[136:139], v[128:131]\n"
    "ds_read_b64_tr_b16 a[176:177], v2 offset:512\n"
    "v_accvgpr_read_b32 v87, a71\n"
    "v_mfma_f32_16x16x32_bf16 v[128:131], a[168:171], a[144:147], v[128:131]\n"
    "ds_read_b64_tr_b16 a[178:179], v2 offset:2560\n"
    "v_mul_f32 v68, v9, v20\n"
    "v_mfma_f32_16x16x32_bf16 v[128:131], a[172:175], a[152:155], v[128:131]\n"
    "ds_read_b64_tr_b16 a[180:181], v2 offset:4608\n"
    "v_mul_f32 v69, v9, v21\n"
    "v_mfma_f32_16x16x32_bf16 v[132:135], a[172:175], v[184:187], 0\n"
    "ds_read_b64_tr_b16 a[182:183], v2 offset:6656\n"
    "v_mul_f32 v70, v9, v22\n"
    "v_mfma_f32_16x16x32_bf16 v[132:135], a[168:171], v[176:179], v[132:135]\n"
    "ds_read_b64_tr_b16 a[184:185], v2 offset:8704\n"
    "v_mul_f32 v71, v9, v23\n"
    "v_mfma_f32_16x16x32_bf16 v[132:135], a[164:167], v[168:171], v[132:135]\n"
    "ds_read_b64_tr_b16 a[186:187], v2 offset:10752\n"
    "v_mul_f32 v84, v9, v84\n"
    "v_mfma_f32_16x16x32_bf16 v[132:135], a[160:163], v[160:163], v[132:135]\n"
    "ds_read_b64_tr_b16 a[188:189], v2 offset:12800\n"
    "v_mul_f32 v85, v9, v85\n"
    "v_mfma_f32_16x16x32_bf16 v[136:139], a[160:163], a[132:135], 0\n"
    "ds_read_b64_tr_b16 a[190:191], v2 offset:14848\n"
    "v_mul_f32 v86, v9, v86\n"
    "v_mfma_f32_16x16x32_bf16 v[136:139], a[164:167], a[140:143], v[136:139]\n"
    "buffer_store_dwordx4 v[64:67], v6, s[48:51], s65 offen sc1\n"
    "v_mfma_f32_16x16x32_bf16 v[136:139], a[168:171], a[148:151], v[136:139]\n"
    "s_xor_b32 s65, s65, 0x80\n"
    "v_mul_f32 v87, v9, v87\n"
    "v_cvt_pk_bf16_f32 v68, v68, v69\n"
    "v_mfma_f32_16x16x32_bf16 v[136:139], a[172:175], a[156:159], v[136:139]\n"
    "v_cvt_pk_bf16_f32 v37, v38, v39\n"
    "v_cvt_pk_bf16_f32 v69, v70, v71\n"
    "v_mfma_f32_16x16x32_bf16 v[140:143], a[172:175], v[188:191], 0\n"
    "v_cvt_pk_bf16_f32 v38, v52, v53\n"
    "v_cvt_pk_bf16_f32 v39, v54, v55\n"
    "v_mfma_f32_16x16x32_bf16 v[140:143], a[168:171], v[180:183], v[140:143]\n"
    "v_cvt_pk_bf16_f32 v70, v84, v85\n"
    "v_cvt_pk_bf16_f32 v71, v86, v87\n"
    "v_mfma_f32_16x16x32_bf16 v[140:143], a[164:167], v[172:175], v[140:143]\n"
    "v_permlane16_swap_b32 v36, v38\n"
    "v_mfma_f32_16x16x32_bf16 v[140:143], a[160:163], v[164:167], v[140:143]\n"
    "s_waitcnt vmcnt(14) lgkmcnt(0)\n"
    "v_mfma_f32_16x16x32_bf16 v[156:159], a[176:179], v[164:167], 0\n"
    "s_barrier\n"
    "v_mfma_f32_16x16x32_bf16 v[156:159], a[180:183], v[172:175], v[156:159]\n"
    "ds_read_b64_tr_b16 a[160:161], v1 offset:16384\n"
    "v_mfma_f32_16x16x32_bf16 v[156:159], a[184:187], v[180:183], v[156:159]\n"
    "ds_read_b64_tr_b16 a[162:163], v1 offset:18432\n"
    "v_mov_b32 v52, v36\n"
    "v_mfma_f32_16x16x32_bf16 v[156:159], a[188:191], v[188:191], v[156:159]\n"
    "ds_read_b64_tr_b16 a[164:165], v1 offset:20480\n"
    "v_mov_b32 v54, v38\n"
    "v_mfma_f32_16x16x32_bf16 v[152:155], a[188:191], a[156:159], 0\n"
    "ds_read_b64_tr_b16 a[166:167], v1 offset:22528\n"
    "v_accvgpr_read_b32 v24, a200\n"
    "v_mfma_f32_16x16x32_bf16 v[152:155], a[184:187], a[148:151], v[152:155]\n"
    "ds_read_b64_tr_b16 a[168:169], v1 offset:24576\n"
    "v_accvgpr_read_b32 v25, a201\n"
    "v_mfma_f32_16x16x32_bf16 v[152:155], a[180:183], a[140:143], v[152:155]\n"
    "ds_read_b64_tr_b16 a[170:171], v1 offset:26624\n"
    "v_accvgpr_read_b32 v26, a202\n"
    "v_mfma_f32_16x16x32_bf16 v[152:155], a[176:179], a[132:135], v[152:155]\n"
    "ds_read_b64_tr_b16 a[172:173], v1 offset:28672\n"
    "v_accvgpr_read_b32 v27, a203\n"
    "v_mfma_f32_16x16x32_bf16 v[148:151], a[176:179], v[160:163], 0\n"
    "ds_read_b64_tr_b16 a[174:175], v1 offset:30720\n"
    "v_accvgpr_write_b32 a200, v40\n"
    "v_mfma_f32_16x16x32_bf16 v[148:151], a[180:183], v[168:171], v[148:151]\n"
    "buffer_load_dwordx4 v4, s[40:43], s57 offen offset:0 lds\n"
    "v_mfma_f32_16x16x32_bf16 v[148:151], a[184:187], v[176:179], v[148:151]\n"
    "v_permlane16_swap_b32 v68, v70\n"
    "v_mfma_f32_16x16x32_bf16 v[148:151], a[188:191], v[184:187], v[148:151]\n"
    "v_permlane16_swap_b32 v37, v39\n"
    "v_mfma_f32_16x16x32_bf16 v[144:147], a[188:191], a[152:155], 0\n"
    "s_add_u32 m0, s56, 0x400\n"
    "s_xor_b32 s57, s54, 0x1100\n"
    "v_mov_b32 v53, v37\n"
    "v_mfma_f32_16x16x32_bf16 v[144:147], a[184:187], a[144:147], v[144:147]\n"
    "buffer_load_dwordx4 v4, s[40:43], s57 offen offset:0 lds\n"
    "v_mfma_f32_16x16x32_bf16 v[144:147], a[180:183], a[136:139], v[144:147]\n"
    "v_permlane16_swap_b32 v69, v71\n"
    "v_mfma_f32_16x16x32_bf16 v[144:147], a[176:179], a[128:131], v[144:147]\n"
    "s_waitcnt lgkmcnt(0)\n"
    "s_add_u32 m0, s56, 0x800\n"
    "v_mov_b32 v55, v39\n"
    "v_mfma_f32_16x16x32_bf16 v[96:99], a[160:163], a[0:3], v[96:99]\n"
    "s_xor_b32 s57, s54, 0x2100\n"
    "v_mov_b32_dpp v36, v68 row_ror:8 row_mask:0xf bank_mask:0xc\n"
    "v_mov_b32_dpp v37, v69 row_ror:8 row_mask:0xf bank_mask:0xc\n"
    "v_mfma_f32_16x16x32_bf16 v[96:99], a[164:167], a[16:19], v[96:99]\n"
    "ds_read_b64_tr_b16 a[176:177], v2 offset:16384\n"
    "v_mov_b32_dpp v38, v70 row_ror:8 row_mask:0xf bank_mask:0xc\n"
    "v_mfma_f32_16x16x32_bf16 v[96:99], a[168:171], a[32:35], v[96:99]\n"
    "ds_read_b64_tr_b16 a[178:179], v2 offset:18432\n"
    "v_mov_b32_dpp v39, v71 row_ror:8 row_mask:0xf bank_mask:0xc\n"
    "v_mfma_f32_16x16x32_bf16 v[96:99], a[172:175], a[48:51], v[96:99]\n"
    "ds_read_b64_tr_b16 a[180:181], v2 offset:20480\n"
    "v_mov_b32_dpp v68, v52 row_ror:8 row_mask:0xf bank_mask:0x3\n"
    "v_mfma_f32_16x16x32_bf16 v[100:103], a[172:175], a[52:55], v[100:103]\n"
    "ds_read_b64_tr_b16 a[182:183], v2 offset:22528\n"
    "v_mov_b32_dpp v69, v53 row_ror:8 row_mask:0xf bank_mask:0x3\n"
    "v_mfma_f32_16x16x32_bf16 v[100:103], a[168:171], a[36:39], v[100:103]\n"
    "ds_read_b64_tr_b16 a[184:185], v2 offset:24576\n"
    "v_mov_b32_dpp v70, v54 row_ror:8 row_mask:0xf bank_mask:0x3\n"
    "v_mfma_f32_16x16x32_bf16 v[100:103], a[164:167], a[20:23], v[100:103]\n"
    "ds_read_b64_tr_b16 a[186:187], v2 offset:26624\n"
    "v_mov_b32_dpp v71, v55 row_ror:8 row_mask:0xf bank_mask:0x3\n"
    "v_mfma_f32_16x16x32_bf16 v[100:103], a[160:163], a[4:7], v[100:103]\n"
    "ds_read_b64_tr_b16 a[188:189], v2 offset:28672\n"
    "v_accvgpr_write_b32 a201, v41\n"
    "v_mfma_f32_16x16x32_bf16 v[104:107], a[160:163], a[8:11], v[104:107]\n"
    "ds_read_b64_tr_b16 a[190:191], v2 offset:30720\n"
    "v_accvgpr_write_b32 a202, v42\n"
    "v_mfma_f32_16x16x32_bf16 v[104:107], a[164:167], a[24:27], v[104:107]\n"
    "buffer_load_dwordx4 v4, s[40:43], s57 offen offset:0 lds\n"
    "v_mfma_f32_16x16x32_bf16 v[104:107], a[168:171], a[40:43], v[104:107]\n"
    "v_accvgpr_write_b32 a203, v43\n"
    "v_mul_f32 v40, v10, v24\n"
    "v_mfma_f32_16x16x32_bf16 v[104:107], a[172:175], a[56:59], v[104:107]\n"
    "v_mul_f32 v41, v10, v25\n"
    "v_mul_f32 v42, v10, v26\n"
    "v_mfma_f32_16x16x32_bf16 v[108:111], a[172:175], a[60:63], v[108:111]\n"
    "s_add_u32 m0, s56, 0xc00\n"
    "s_xor_b32 s57, s54, 0x3100\n"
    "v_mul_f32 v43, v10, v27\n"
    "v_mfma_f32_16x16x32_bf16 v[108:111], a[168:171], a[44:47], v[108:111]\n"
    "buffer_load_dwordx4 v4, s[40:43], s57 offen offset:0 lds\n"
    "v_mfma_f32_16x16x32_bf16 v[108:111], a[164:167], a[28:31], v[108:111]\n"
    "v_accvgpr_read_b32 v24, a216\n"
    "v_accvgpr_read_b32 v25, a217\n"
    "v_mfma_f32_16x16x32_bf16 v[108:111], a[160:163], a[12:15], v[108:111]\n"
    "s_waitcnt lgkmcnt(0)\n"
    "s_xor_b32 s57, s54, 0x10100\n"
    "v_accvgpr_read_b32 v26, a218\n"
    "v_mfma_f32_16x16x32_bf16 v[124:127], a[176:179], a[12:15], v[124:127]\n"
    "v_accvgpr_read_b32 v27, a219\n"
    "v_accvgpr_write_b32 a216, v56\n"
    "v_mfma_f32_16x16x32_bf16 v[124:127], a[180:183], a[28:31], v[124:127]\n"
    "ds_read_b64_tr_b16 a[160:161], v1 offset:16896\n"
    "v_accvgpr_write_b32 a217, v57\n"
    "v_mfma_f32_16x16x32_bf16 v[124:127], a[184:187], a[44:47], v[124:127]\n"
    "ds_read_b64_tr_b16 a[162:163], v1 offset:18944\n"
    "v_accvgpr_write_b32 a218, v58\n"
    "v_mfma_f32_16x16x32_bf16 v[124:127], a[188:191], a[60:63], v[124:127]\n"
    "ds_read_b64_tr_b16 a[164:165], v1 offset:20992\n"
    "v_accvgpr_write_b32 a219, v59\n"
    "v_mfma_f32_16x16x32_bf16 v[120:123], a[188:191], a[56:59], v[120:123]\n"
    "ds_read_b64_tr_b16 a[166:167], v1 offset:23040\n"
    "v_mul_f32 v56, v10, v24\n"
    "v_mfma_f32_16x16x32_bf16 v[120:123], a[184:187], a[40:43], v[120:123]\n"
    "ds_read_b64_tr_b16 a[168:169], v1 offset:25088\n"
    "v_mul_f32 v57, v10, v25\n"
    "v_mfma_f32_16x16x32_bf16 v[120:123], a[180:183], a[24:27], v[120:123]\n"
    "ds_read_b64_tr_b16 a[170:171], v1 offset:27136\n"
    "v_mul_f32 v58, v10, v26\n"
    "v_mfma_f32_16x16x32_bf16 v[120:123], a[176:179], a[8:11], v[120:123]\n"
    "ds_read_b64_tr_b16 a[172:173], v1 offset:29184\n"
    "v_mul_f32 v59, v10, v27\n"
    "v_mfma_f32_16x16x32_bf16 v[116:119], a[176:179], a[4:7], v[116:119]\n"
    "ds_read_b64_tr_b16 a[174:175], v1 offset:31232\n"
    "v_accvgpr_read_b32 v24, a232\n"
    "v_mfma_f32_16x16x32_bf16 v[116:119], a[180:183], a[20:23], v[116:119]\n"
    "buffer_store_dwordx4 v[36:39], v6, s[48:51], s66 offen sc1\n"
    "v_mfma_f32_16x16x32_bf16 v[116:119], a[184:187], a[36:39], v[116:119]\n"
    "s_xor_b32 s66, s66, 0x80\n"
    "v_accvgpr_read_b32 v25, a233\n"
    "v_accvgpr_read_b32 v26, a234\n"
    "v_mfma_f32_16x16x32_bf16 v[116:119], a[188:191], a[52:55], v[116:119]\n"
    "v_accvgpr_read_b32 v27, a235\n"
    "v_accvgpr_write_b32 a232, v72\n"
    "v_mfma_f32_16x16x32_bf16 v[112:115], a[188:191], a[48:51], v[112:115]\n"
    "v_accvgpr_write_b32 a233, v73\n"
    "v_accvgpr_write_b32 a234, v74\n"
    "v_mfma_f32_16x16x32_bf16 v[112:115], a[184:187], a[32:35], v[112:115]\n"
    "v_accvgpr_write_b32 a235, v75\n"
    "v_accvgpr_read_b32 v88, a72\n"
    "v_mfma_f32_16x16x32_bf16 v[112:115], a[180:183], a[16:19], v[112:115]\n"
    "v_accvgpr_read_b32 v89, a73\n"
    "v_accvgpr_read_b32 v90, a74\n"
    "v_mfma_f32_16x16x32_bf16 v[112:115], a[176:179], a[0:3], v[112:115]\n"
    "s_waitcnt lgkmcnt(0)\n"
    "s_add_u32 m0, s56, 0x4000\n"
    "v_accvgpr_read_b32 v91, a75\n"
    "v_mfma_f32_16x16x32_bf16 v[128:131], a[160:163], a[0:3], v[128:131]\n"
    "v_mul_f32 v72, v10, v24\n"
    "v_mul_f32 v73, v10, v25\n"
    "v_mfma_f32_16x16x32_bf16 v[128:131], a[164:167], a[16:19], v[128:131]\n"
    "ds_read_b64_tr_b16 a[176:177], v2 offset:16896\n"
    "v_mul_f32 v74, v10, v26\n"
    "v_mfma_f32_16x16x32_bf16 v[128:131], a[168:171], a[32:35], v[128:131]\n"
    "ds_read_b64_tr_b16 a[178:179], v2 offset:18944\n"
    "v_mul_f32 v75, v10, v27\n"
    "v_mfma_f32_16x16x32_bf16 v[128:131], a[172:175], a[48:51], v[128:131]\n"
    "ds_read_b64_tr_b16 a[180:181], v2 offset:20992\n"
    "v_mul_f32 v88, v10, v88\n"
    "v_mfma_f32_16x16x32_bf16 v[132:135], a[172:175], a[52:55], v[132:135]\n"
    "ds_read_b64_tr_b16 a[182:183], v2 offset:23040\n"
    "v_mul_f32 v89, v10, v89\n"
    "v_mfma_f32_16x16x32_bf16 v[132:135], a[168:171], a[36:39], v[132:135]\n"
    "ds_read_b64_tr_b16 a[184:185], v2 offset:25088\n"
    "v_mul_f32 v90, v10, v90\n"
    "v_mfma_f32_16x16x32_bf16 v[132:135], a[164:167], a[20:23], v[132:135]\n"
    "ds_read_b64_tr_b16 a[186:187], v2 offset:27136\n"
    "v_mul_f32 v91, v10, v91\n"
    "v_mfma_f32_16x16x32_bf16 v[132:135], a[160:163], a[4:7], v[132:135]\n"
    "ds_read_b64_tr_b16 a[188:189], v2 offset:29184\n"
    "v_cvt_pk_bf16_f32 v40, v40, v41\n"
    "v_mfma_f32_16x16x32_bf16 v[136:139], a[160:163], a[8:11], v[136:139]\n"
    "ds_read_b64_tr_b16 a[190:191], v2 offset:31232\n"
    "v_cvt_pk_bf16_f32 v72, v72, v73\n"
    "v_mfma_f32_16x16x32_bf16 v[136:139], a[164:167], a[24:27], v[136:139]\n"
    "buffer_store_dwordx4 v[68:71], v6, s[48:51], s67 offen sc1\n"
    "v_mfma_f32_16x16x32_bf16 v[136:139], a[168:171], a[40:43], v[136:139]\n"
    "s_xor_b32 s67, s67, 0x80\n"
    "v_cvt_pk_bf16_f32 v41, v42, v43\n"
    "v_cvt_pk_bf16_f32 v73, v74, v75\n"
    "v_mfma_f32_16x16x32_bf16 v[136:139], a[172:175], a[56:59], v[136:139]\n"
    "v_cvt_pk_bf16_f32 v42, v56, v57\n"
    "v_cvt_pk_bf16_f32 v43, v58, v59\n"
    "v_mfma_f32_16x16x32_bf16 v[140:143], a[172:175], a[60:63], v[140:143]\n"
    "v_cvt_pk_bf16_f32 v74, v88, v89\n"
    "v_cvt_pk_bf16_f32 v75, v90, v91\n"
    "v_mfma_f32_16x16x32_bf16 v[140:143], a[168:171], a[44:47], v[140:143]\n"
    "v_permlane16_swap_b32 v40, v42\n"
    "v_mfma_f32_16x16x32_bf16 v[140:143], a[164:167], a[28:31], v[140:143]\n"
    "v_permlane16_swap_b32 v72, v74\n"
    "v_mfma_f32_16x16x32_bf16 v[140:143], a[160:163], a[12:15], v[140:143]\n"
    "s_waitcnt vmcnt(14) lgkmcnt(0)\n"
    "v_mfma_f32_16x16x32_bf16 v[156:159], a[176:179], a[12:15], v[156:159]\n"
    "s_barrier\n"
    "v_mfma_f32_16x16x32_bf16 v[156:159], a[180:183], a[28:31], v[156:159]\n"
    "ds_read_b64_tr_b16 a[160:161], v1 offset:32768\n"
    "v_mfma_f32_16x16x32_bf16 v[156:159], a[184:187], a[44:47], v[156:159]\n"
    "ds_read_b64_tr_b16 a[162:163], v1 offset:34816\n"
    "v_mov_b32 v56, v40\n"
    "v_mfma_f32_16x16x32_bf16 v[156:159], a[188:191], a[60:63], v[156:159]\n"
    "ds_read_b64_tr_b16 a[164:165], v1 offset:36864\n"
    "v_mov_b32 v58, v42\n"
    "v_mfma_f32_16x16x32_bf16 v[152:155], a[188:191], a[56:59], v[152:155]\n"
    "ds_read_b64_tr_b16 a[166:167], v1 offset:38912\n"
    "v_mov_b32_dpp v40, v72 row_ror:8 row_mask:0xf bank_mask:0xc\n"
    "v_mfma_f32_16x16x32_bf16 v[152:155], a[184:187], a[40:43], v[152:155]\n"
    "ds_read_b64_tr_b16 a[168:169], v1 offset:40960\n"
    "v_mov_b32_dpp v42, v74 row_ror:8 row_mask:0xf bank_mask:0xc\n"
    "v_mfma_f32_16x16x32_bf16 v[152:155], a[180:183], a[24:27], v[152:155]\n"
    "ds_read_b64_tr_b16 a[170:171], v1 offset:43008\n"
    "v_mov_b32_dpp v72, v56 row_ror:8 row_mask:0xf bank_mask:0x3\n"
    "v_mfma_f32_16x16x32_bf16 v[152:155], a[176:179], a[8:11], v[152:155]\n"
    "ds_read_b64_tr_b16 a[172:173], v1 offset:45056\n"
    "v_mov_b32_dpp v74, v58 row_ror:8 row_mask:0xf bank_mask:0x3\n"
    "v_mfma_f32_16x16x32_bf16 v[148:151], a[176:179], a[4:7], v[148:151]\n"
    "ds_read_b64_tr_b16 a[174:175], v1 offset:47104\n"
    "v_accvgpr_read_b32 v28, a204\n"
    "v_mfma_f32_16x16x32_bf16 v[148:151], a[180:183], a[20:23], v[148:151]\n"
    "buffer_load_dwordx4 v4, s[40:43], s57 offen offset:0 lds\n"
    "v_mfma_f32_16x16x32_bf16 v[148:151], a[184:187], a[36:39], v[148:151]\n"
    "v_permlane16_swap_b32 v41, v43\n"
    "v_mfma_f32_16x16x32_bf16 v[148:151], a[188:191], a[52:55], v[148:151]\n"
    "v_permlane16_swap_b32 v73, v75\n"
    "v_mfma_f32_16x16x32_bf16 v[144:147], a[188:191], a[48:51], v[144:147]\n"
    "s_add_u32 m0, s56, 0x4400\n"
    "s_xor_b32 s57, s54, 0x11100\n"
    "v_mov_b32 v57, v41\n"
    "v_mfma_f32_16x16x32_bf16 v[144:147], a[184:187], a[32:35], v[144:147]\n"
    "buffer_load_dwordx4 v4, s[40:43], s57 offen offset:0 lds\n"
    "v_mfma_f32_16x16x32_bf16 v[144:147], a[180:183], a[16:19], v[144:147]\n"
    "v_mov_b32 v59, v43\n"
    "v_mov_b32_dpp v41, v73 row_ror:8 row_mask:0xf bank_mask:0xc\n"
    "v_mfma_f32_16x16x32_bf16 v[144:147], a[176:179], a[0:3], v[144:147]\n"
    "s_waitcnt lgkmcnt(0)\n"
    "s_add_u32 m0, s56, 0x4800\n"
    "v_mov_b32_dpp v43, v75 row_ror:8 row_mask:0xf bank_mask:0xc\n"
    "v_mfma_f32_16x16x32_bf16 v[96:99], a[160:163], v[192:195], v[96:99]\n"
    "s_xor_b32 s57, s54, 0x12100\n"
    "L_pad10_%=:\n"
    "s_cbranch_execz L_ret2_%=\n"
    "v_mov_b32_dpp v73, v57 row_ror:8 row_mask:0xf bank_mask:0x3\n"
    "v_mov_b32_dpp v75, v59 row_ror:8 row_mask:0xf bank_mask:0x3\n"
    "v_mfma_f32_16x16x32_bf16 v[96:99], a[164:167], v[208:211], v[96:99]\n"
    "ds_read_b64_tr_b16 a[176:177], v2 offset:32768\n"
    "v_accvgpr_read_b32 v29, a205\n"
    "v_mfma_f32_16x16x32_bf16 v[96:99], a[168:171], v[224:227], v[96:99]\n"
    "ds_read_b64_tr_b16 a[178:179], v2 offset:34816\n"
    "v_accvgpr_read_b32 v30, a206\n"
    "v_mfma_f32_16x16x32_bf16 v[96:99], a[172:175], v[240:243], v[96:99]\n"
    "ds_read_b64_tr_b16 a[180:181], v2 offset:36864\n"
    "v_accvgpr_read_b32 v31, a207\n"
    "v_mfma_f32_16x16x32_bf16 v[100:103], a[172:175], v[244:247], v[100:103]\n"
    "ds_read_b64_tr_b16 a[182:183], v2 offset:38912\n"
    "v_accvgpr_write_b32 a204, v44\n"
    "v_mfma_f32_16x16x32_bf16 v[100:103], a[168:171], v[228:231], v[100:103]\n"
    "ds_read_b64_tr_b16 a[184:185], v2 offset:40960\n"
    "v_accvgpr_write_b32 a205, v45\n"
    "v_mfma_f32_16x16x32_bf16 v[100:103], a[164:167], v[212:215], v[100:103]\n"
    "ds_read_b64_tr_b16 a[186:187], v2 offset:43008\n"
    "v_accvgpr_write_b32 a206, v46\n"
    "v_mfma_f32_16x16x32_bf16 v[100:103], a[160:163], v[196:199], v[100:103]\n"
    "ds_read_b64_tr_b16 a[188:189], v2 offset:45056\n"
    "v_accvgpr_write_b32 a207, v47\n"
    "v_mfma_f32_16x16x32_bf16 v[104:107], a[160:163], v[200:203], v[104:107]\n"
    "ds_read_b64_tr_b16 a[190:191], v2 offset:47104\n"
    "v_mul_f32 v44, v11, v28\n"
    "v_mfma_f32_16x16x32_bf16 v[104:107], a[164:167], v[216:219], v[104:107]\n"
    "buffer_load_dwordx4 v4, s[40:43], s57 offen offset:0 lds\n"
    "v_mfma_f32_16x16x32_bf16 v[104:107], a[168:171], v[232:235], v[104:107]\n"
    "s_add_u32 s53, s73, s90\n"
    "v_mul_f32 v45, v11, v29\n"
    "v_mul_f32 v46, v11, v30\n"
    "v_mfma_f32_16x16x32_bf16 v[104:107], a[172:175], v[248:251], v[104:107]\n"
    "s_andn2_b32 s53, s53, 0x180\n"
    "v_mul_f32 v47, v11, v31\n"
    "v_accvgpr_read_b32 v28, a220\n"
    "v_mfma_f32_16x16x32_bf16 v[108:111], a[172:175], v[252:255], v[108:111]\n"
    "s_add_u32 m0, s56, 0x4c00\n"
    "s_xor_b32 s57, s54, 0x13100\n"
    "v_accvgpr_read_b32 v29, a221\n"
    "v_mfma_f32_16x16x32_bf16 v[108:111], a[168:171], v[236:239], v[108:111]\n"
    "buffer_load_dwordx4 v4, s[40:43], s57 offen offset:0 lds\n"
    "v_mfma_f32_16x16x32_bf16 v[108:111], a[164:167], v[220:223], v[108:111]\n"
    "s_cmp_lg_u32 s81, 0\n"
    "s_cselect_b32 s38, s42, 0\n"
    "v_accvgpr_read_b32 v30, a222\n"
    "v_accvgpr_read_b32 v31, a223\n"
    "v_mfma_f32_16x16x32_bf16 v[108:111], a[160:163], v[204:207], v[108:111]\n"
    "s_waitcnt lgkmcnt(0)\n"
    "s_xor_b32 s57, s54, 0x20100\n"
    "v_accvgpr_write_b32 a220, v60\n"
    "v_mfma_f32_16x16x32_bf16 v[124:127], a[176:179], v[204:207], v[124:127]\n"
    "v_accvgpr_write_b32 a221, v61\n"
    "v_accvgpr_write_b32 a222, v62\n"
    "v_mfma_f32_16x16x32_bf16 v[124:127], a[180:183], v[220:223], v[124:127]\n"
    "ds_read_b64_tr_b16 a[160:161], v1 offset:33280\n"
    "v_accvgpr_write_b32 a223, v63\n"
    "v_mfma_f32_16x16x32_bf16 v[124:127], a[184:187], v[236:239], v[124:127]\n"
    "ds_read_b64_tr_b16 a[162:163], v1 offset:35328\n"
    "v_mul_f32 v60, v11, v28\n"
    "v_mfma_f32_16x16x32_bf16 v[124:127], a[188:191], v[252:255], v[124:127]\n"
    "ds_read_b64_tr_b16 a[164:165], v1 offset:37376\n"
    "v_mul_f32 v61, v11, v29\n"
    "v_mfma_f32_16x16x32_bf16 v[120:123], a[188:191], v[248:251], v[120:123]\n"
    "ds_read_b64_tr_b16 a[166:167], v1 offset:39424\n"
    "v_mul_f32 v62, v11, v30\n"
    "v_mfma_f32_16x16x32_bf16 v[120:123], a[184:187], v[232:235], v[120:123]\n"
    "ds_read_b64_tr_b16 a[168:169], v1 offset:41472\n"
    "v_mul_f32 v63, v11, v31\n"
    "v_mfma_f32_16x16x32_bf16 v[120:123], a[180:183], v[216:219], v[120:123]\n"
    "ds_read_b64_tr_b16 a[170:171], v1 offset:43520\n"
    "v_accvgpr_read_b32 v28, a236\n"
    "v_mfma_f32_16x16x32_bf16 v[120:123], a[176:179], v[200:203], v[120:123]\n"
    "ds_read_b64_tr_b16 a[172:173], v1 offset:45568\n"
    "v_accvgpr_read_b32 v29, a237\n"
    "v_mfma_f32_16x16x32_bf16 v[116:119], a[176:179], v[196:199], v[116:119]\n"
    "ds_read_b64_tr_b16 a[174:175], v1 offset:47616\n"
    "v_accvgpr_read_b32 v30, a238\n"
    "v_mfma_f32_16x16x32_bf16 v[116:119], a[180:183], v[212:215], v[116:119]\n"
    "buffer_store_dwordx4 v[40:43], v6, s[48:51], s68 offen sc1\n"
    "v_mfma_f32_16x16x32_bf16 v[116:119], a[184:187], v[228:231], v[116:119]\n"
    "s_xor_b32 s68, s68, 0x80\n"
    "v_accvgpr_read_b32 v31, a239\n"
    "v_accvgpr_write_b32 a236, v76\n"
    "v_mfma_f32_16x16x32_bf16 v[116:119], a[188:191], v[244:247], v[116:119]\n"
    "v_accvgpr_write_b32 a237, v77\n"
    "v_accvgpr_write_b32 a238, v78\n"
    "v_mfma_f32_16x16x32_bf16 v[112:115], a[188:191], v[240:243], v[112:115]\n"
    "v_accvgpr_write_b32 a239, v79\n"
    "v_accvgpr_read_b32 v92, a76\n"
    "v_mfma_f32_16x16x32_bf16 v[112:115], a[184:187], v[224:227], v[112:115]\n"
    "v_accvgpr_read_b32 v93, a77\n"
    "v_accvgpr_read_b32 v94, a78\n"
    "v_mfma_f32_16x16x32_bf16 v[112:115], a[180:183], v[208:211], v[112:115]\n"
    "v_accvgpr_read_b32 v95, a79\n"
    "v_mul_f32 v76, v11, v28\n"
    "v_mfma_f32_16x16x32_bf16 v[112:115], a[176:179], v[192:195], v[112:115]\n"
    "s_waitcnt lgkmcnt(0)\n"
    "s_add_u32 m0, s56, 0x8000\n"
    "v_mul_f32 v77, v11, v29\n"
    "v_mfma_f32_16x16x32_bf16 v[128:131], a[160:163], v[192:195], v[128:131]\n"
    "v_mul_f32 v78, v11, v30\n"
    "v_mul_f32 v79, v11, v31\n"
    "v_mfma_f32_16x16x32_bf16 v[128:131], a[164:167], v[208:211], v[128:131]\n"
    "ds_read_b64_tr_b16 a[176:177], v2 offset:33280\n"
    "v_mul_f32 v92, v11, v92\n"
    "v_mfma_f32_16x16x32_bf16 v[128:131], a[168:171], v[224:227], v[128:131]\n"
    "ds_read_b64_tr_b16 a[178:179], v2 offset:35328\n"
    "v_mul_f32 v93, v11, v93\n"
    "v_mfma_f32_16x16x32_bf16 v[128:131], a[172:175], v[240:243], v[128:131]\n"
    "ds_read_b64_tr_b16 a[180:181], v2 offset:37376\n"
    "v_mul_f32 v94, v11, v94\n"
    "v_mfma_f32_16x16x32_bf16 v[132:135], a[172:175], v[244:247], v[132:135]\n"
    "ds_read_b64_tr_b16 a[182:183], v2 offset:39424\n"
    "v_mul_f32 v95, v11, v95\n"
    "v_mfma_f32_16x16x32_bf16 v[132:135], a[168:171], v[228:231], v[132:135]\n"
    "ds_read_b64_tr_b16 a[184:185], v2 offset:41472\n"
    "v_cvt_pk_bf16_f32 v44, v44, v45\n"
    "v_mfma_f32_16x16x32_bf16 v[132:135], a[164:167], v[212:215], v[132:135]\n"
    "ds_read_b64_tr_b16 a[186:187], v2 offset:43520\n"
    "v_cvt_pk_bf16_f32 v76, v76, v77\n"
    "v_mfma_f32_16x16x32_bf16 v[132:135], a[160:163], v[196:199], v[132:135]\n"
    "ds_read_b64_tr_b16 a[188:189], v2 offset:45568\n"
    "v_cvt_pk_bf16_f32 v45, v46, v47\n"
    "v_mfma_f32_16x16x32_bf16 v[136:139], a[160:163], v[200:203], v[136:139]\n"
    "ds_read_b64_tr_b16 a[190:191], v2 offset:47616\n"
    "v_cvt_pk_bf16_f32 v77, v78, v79\n"
    "v_mfma_f32_16x16x32_bf16 v[136:139], a[164:167], v[216:219], v[136:139]\n"
    "buffer_store_dwordx4 v[72:75], v6, s[48:51], s69 offen sc1\n"
    "v_mfma_f32_16x16x32_bf16 v[136:139], a[168:171], v[232:235], v[136:139]\n"
    "s_xor_b32 s69, s69, 0x80\n"
    "v_cvt_pk_bf16_f32 v46, v60, v61\n"
    "v_cvt_pk_bf16_f32 v47, v62, v63\n"
    "v_mfma_f32_16x16x32_bf16 v[136:139], a[172:175], v[248:251], v[136:139]\n"
    "v_cvt_pk_bf16_f32 v78, v92, v93\n"
    "v_cvt_pk_bf16_f32 v79, v94, v95\n"
    "v_mfma_f32_16x16x32_bf16 v[140:143], a[172:175], v[252:255], v[140:143]\n"
    "v_permlane16_swap_b32 v44, v46\n"
    "v_mfma_f32_16x16x32_bf16 v[140:143], a[168:171], v[236:239], v[140:143]\n"
    "v_permlane16_swap_b32 v76, v78\n"
    "v_mfma_f32_16x16x32_bf16 v[140:143], a[164:167], v[220:223], v[140:143]\n"
    "v_permlane16_swap_b32 v45, v47\n"
    "v_mfma_f32_16x16x32_bf16 v[140:143], a[160:163], v[204:207], v[140:143]\n"
    "s_waitcnt vmcnt(14) lgkmcnt(0)\n"
    "v_mfma_f32_16x16x32_bf16 v[156:159], a[176:179], v[204:207], v[156:159]\n"
    "s_barrier\n"
    "v_mfma_f32_16x16x32_bf16 v[156:159], a[180:183], v[220:223], v[156:159]\n"
    "ds_read_b64_tr_b16 a[160:161], v1 offset:49152\n"
    "v_mfma_f32_16x16x32_bf16 v[156:159], a[184:187], v[236:239], v[156:159]\n"
    "ds_read_b64_tr_b16 a[162:163], v1 offset:51200\n"
    "v_mov_b32 v60, v44\n"
    "v_mfma_f32_16x16x32_bf16 v[156:159], a[188:191], v[252:255], v[156:159]\n"
    "ds_read_b64_tr_b16 a[164:165], v1 offset:53248\n"
    "v_mov_b32 v61, v45\n"
    "v_mfma_f32_16x16x32_bf16 v[152:155], a[188:191], v[248:251], v[152:155]\n"
    "ds_read_b64_tr_b16 a[166:167], v1 offset:55296\n"
    "v_mov_b32 v62, v46\n"
    "v_mfma_f32_16x16x32_bf16 v[152:155], a[184:187], v[232:235], v[152:155]\n"
    "ds_read_b64_tr_b16 a[168:169], v1 offset:57344\n"
    "v_mov_b32 v63, v47\n"
    "v_mfma_f32_16x16x32_bf16 v[152:155], a[180:183], v[216:219], v[152:155]\n"
    "ds_read_b64_tr_b16 a[170:171], v1 offset:59392\n"
    "v_mov_b32_dpp v44, v76 row_ror:8 row_mask:0xf bank_mask:0xc\n"
    "v_mfma_f32_16x16x32_bf16 v[152:155], a[176:179], v[200:203], v[152:155]\n"
    "ds_read_b64_tr_b16 a[172:173], v1 offset:61440\n"
    "v_mov_b32_dpp v46, v78 row_ror:8 row_mask:0xf bank_mask:0xc\n"
    "v_mfma_f32_16x16x32_bf16 v[148:151], a[176:179], v[196:199], v[148:151]\n"
    "ds_read_b64_tr_b16 a[174:175], v1 offset:63488\n"
    "v_mov_b32_dpp v76, v60 row_ror:8 row_mask:0xf bank_mask:0x3\n"
    "v_mfma_f32_16x16x32_bf16 v[148:151], a[180:183], v[212:215], v[148:151]\n"
    "buffer_load_dwordx4 v4, s[40:43], s57 offen offset:0 lds\n"
    "v_mfma_f32_16x16x32_bf16 v[148:151], a[184:187], v[228:231], v[148:151]\n"
    "v_permlane16_swap_b32 v77, v79\n"
    "v_mfma_f32_16x16x32_bf16 v[148:151], a[188:191], v[244:247], v[148:151]\n"
    "s_nop 0\n"
    "v_mov_b32_dpp v45, v77 row_ror:8 row_mask:0xf bank_mask:0xc\n"
    "v_mov_b32_dpp v47, v79 row_ror:8 row_mask:0xf bank_mask:0xc\n"
    "v_mfma_f32_16x16x32_bf16 v[144:147], a[188:191], v[240:243], v[144:147]\n"
    "s_add_u32 m0, s56, 0x8400\n"
    "s_xor_b32 s57, s54, 0x21100\n"
    "v_mov_b32_dpp v77, v61 row_ror:8 row_mask:0xf bank_mask:0x3\n"
    "v_mfma_f32_16x16x32_bf16 v[144:147], a[184:187], v[224:227], v[144:147]\n"
    "buffer_load_dwordx4 v4, s[40:43], s57 offen offset:0 lds\n"
    "v_mfma_f32_16x16x32_bf16 v[144:147], a[180:183], v[208:211], v[144:147]\n"
    "v_mov_b32_dpp v78, v62 row_ror:8 row_mask:0xf bank_mask:0x3\n"
    "v_mov_b32_dpp v79, v63 row_ror:8 row_mask:0xf bank_mask:0x3\n"
    "v_mfma_f32_16x16x32_bf16 v[144:147], a[176:179], v[192:195], v[144:147]\n"
    "s_waitcnt lgkmcnt(0)\n"
    "s_add_u32 m0, s56, 0x8800\n"
    "v_mfma_f32_16x16x32_bf16 v[96:99], a[160:163], a[192:195], v[96:99]\n"
    "s_xor_b32 s57, s54, 0x22100\n"
    "v_mfma_f32_16x16x32_bf16 v[96:99], a[164:167], a[208:211], v[96:99]\n"
    "ds_read_b64_tr_b16 a[176:177], v2 offset:49152\n"
    "v_mfma_f32_16x16x32_bf16 v[96:99], a[168:171], a[224:227], v[96:99]\n"
    "ds_read_b64_tr_b16 a[178:179], v2 offset:51200\n"
    "v_mfma_f32_16x16x32_bf16 v[96:99], a[172:175], a[240:243], v[96:99]\n"
    "ds_read_b64_tr_b16 a[180:181], v2 offset:53248\n"
    "v_mfma_f32_16x16x32_bf16 v[100:103], a[172:175], a[244:247], v[100:103]\n"
    "ds_read_b64_tr_b16 a[182:183], v2 offset:55296\n"
    "v_mfma_f32_16x16x32_bf16 v[100:103], a[168:171], a[228:231], v[100:103]\n"
    "ds_read_b64_tr_b16 a[184:185], v2 offset:57344\n"
    "v_mfma_f32_16x16x32_bf16 v[100:103], a[164:167], a[212:215], v[100:103]\n"
    "ds_read_b64_tr_b16 a[186:187], v2 offset:59392\n"
    "s_nop 0\n"
    "v_mul_f32 v96, v8, v96\n"
    "v_mfma_f32_16x16x32_bf16 v[100:103], a[160:163], a[196:199], v[100:103]\n"
    "ds_read_b64_tr_b16 a[188:189], v2 offset:61440\n"
    "v_mul_f32 v97, v8, v97\n"
    "v_mfma_f32_16x16x32_bf16 v[104:107], a[160:163], a[200:203], v[104:107]\n"
    "ds_read_b64_tr_b16 a[190:191], v2 offset:63488\n"
    "v_mul_f32 v98, v8, v98\n"
    "v_mfma_f32_16x16x32_bf16 v[104:107], a[164:167], a[216:219], v[104:107]\n"
    "buffer_load_dwordx4 v4, s[40:43], s57 offen offset:0 lds\n"
    "v_mfma_f32_16x16x32_bf16 v[104:107], a[168:171], a[232:235], v[104:107]\n"
    "v_mul_f32 v99, v8, v99\n"
    "v_cvt_pk_bf16_f32 v96, v96, v97\n"
    "v_mfma_f32_16x16x32_bf16 v[104:107], a[172:175], a[248:251], v[104:107]\n"
    "v_cvt_pk_bf16_f32 v97, v98, v99\n"
    "v_mul_f32 v100, v9, v100\n"
    "v_mfma_f32_16x16x32_bf16 v[108:111], a[172:175], a[252:255], v[108:111]\n"
    "s_add_u32 m0, s56, 0x8c00\n"
    "s_xor_b32 s57, s54, 0x23100\n"
    "v_mul_f32 v101, v9, v101\n"
    "v_mfma_f32_16x16x32_bf16 v[108:111], a[168:171], a[236:239], v[108:111]\n"
    "buffer_load_dwordx4 v4, s[40:43], s57 offen offset:0 lds\n"
    "v_mfma_f32_16x16x32_bf16 v[108:111], a[164:167], a[220:223], v[108:111]\n"
    "v_mul_f32 v102, v9, v102\n"
    "v_mul_f32 v103, v9, v103\n"
    "v_mfma_f32_16x16x32_bf16 v[108:111], a[160:163], a[204:207], v[108:111]\n"
    "s_waitcnt lgkmcnt(0)\n"
    "s_xor_b32 s57, s54, 0x30100\n"
    "v_cvt_pk_bf16_f32 v100, v100, v101\n"
    "v_mfma_f32_16x16x32_bf16 v[124:127], a[176:179], a[204:207], v[124:127]\n"
    "v_cvt_pk_bf16_f32 v101, v102, v103\n"
    "v_mul_f32 v104, v10, v104\n"
    "v_mfma_f32_16x16x32_bf16 v[124:127], a[180:183], a[220:223], v[124:127]\n"
    "ds_read_b64_tr_b16 a[160:161], v1 offset:49664\n"
    "v_mul_f32 v105, v10, v105\n"
    "v_mfma_f32_16x16x32_bf16 v[124:127], a[184:187], a[236:239], v[124:127]\n"
    "ds_read_b64_tr_b16 a[162:163], v1 offset:51712\n"
    "v_mul_f32 v106, v10, v106\n"
    "v_mfma_f32_16x16x32_bf16 v[124:127], a[188:191], a[252:255], v[124:127]\n"
    "ds_read_b64_tr_b16 a[164:165], v1 offset:53760\n"
    "v_mul_f32 v107, v10, v107\n"
    "v_mfma_f32_16x16x32_bf16 v[120:123], a[188:191], a[248:251], v[120:123]\n"
    "ds_read_b64_tr_b16 a[166:167], v1 offset:55808\n"
    "v_cvt_pk_bf16_f32 v104, v104, v105\n"
    "v_mfma_f32_16x16x32_bf16 v[120:123], a[184:187], a[232:235], v[120:123]\n"
    "ds_read_b64_tr_b16 a[168:169], v1 offset:57856\n"
    "v_cvt_pk_bf16_f32 v105, v106, v107\n"
    "v_mfma_f32_16x16x32_bf16 v[120:123], a[180:183], a[216:219], v[120:123]\n"
    "ds_read_b64_tr_b16 a[170:171], v1 offset:59904\n"
    "v_mul_f32 v108, v11, v108\n"
    "v_mfma_f32_16x16x32_bf16 v[120:123], a[176:179], a[200:203], v[120:123]\n"
    "ds_read_b64_tr_b16 a[172:173], v1 offset:61952\n"
    "v_mul_f32 v109, v11, v109\n"
    "v_mfma_f32_16x16x32_bf16 v[116:119], a[176:179], a[196:199], v[116:119]\n"
    "ds_read_b64_tr_b16 a[174:175], v1 offset:64000\n"
    "v_mul_f32 v110, v11, v110\n"
    "v_mfma_f32_16x16x32_bf16 v[116:119], a[180:183], a[212:215], v[116:119]\n"
    "buffer_store_dwordx4 v[44:47], v6, s[48:51], s70 offen sc1\n"
    "v_mfma_f32_16x16x32_bf16 v[116:119], a[184:187], a[228:231], v[116:119]\n"
    "s_xor_b32 s70, s70, 0x80\n"
    "v_mul_f32 v120, v10, v120\n"
    "v_mul_f32 v121, v10, v121\n"
    "v_mfma_f32_16x16x32_bf16 v[116:119], a[188:191], a[244:247], v[116:119]\n"
    "v_mul_f32 v122, v10, v122\n"
    "v_mul_f32 v123, v10, v123\n"
    "v_mfma_f32_16x16x32_bf16 v[112:115], a[188:191], a[240:243], v[112:115]\n"
    "v_cvt_pk_bf16_f32 v106, v120, v121\n"
    "v_cvt_pk_bf16_f32 v107, v122, v123\n"
    "v_mfma_f32_16x16x32_bf16 v[112:115], a[184:187], a[224:227], v[112:115]\n"
    "v_permlane16_swap_b32 v104, v106\n"
    "v_mfma_f32_16x16x32_bf16 v[112:115], a[180:183], a[208:211], v[112:115]\n"
    "v_mul_f32 v116, v9, v116\n"
    "v_mul_f32 v117, v9, v117\n"
    "v_mfma_f32_16x16x32_bf16 v[112:115], a[176:179], a[192:195], v[112:115]\n"
    "s_waitcnt lgkmcnt(0)\n"
    "s_add_u32 m0, s56, 0xc000\n"
    "v_mul_f32 v118, v9, v118\n"
    "v_mfma_f32_16x16x32_bf16 v[128:131], a[160:163], a[192:195], v[128:131]\n"
    "v_mul_f32 v119, v9, v119\n"
    "v_cvt_pk_bf16_f32 v102, v116, v117\n"
    "v_mfma_f32_16x16x32_bf16 v[128:131], a[164:167], a[208:211], v[128:131]\n"
    "ds_read_b64_tr_b16 a[176:177], v2 offset:49664\n"
    "v_cvt_pk_bf16_f32 v103, v118, v119\n"
    "v_mfma_f32_16x16x32_bf16 v[128:131], a[168:171], a[224:227], v[128:131]\n"
    "ds_read_b64_tr_b16 a[178:179], v2 offset:51712\n"
    "v_mul_f32 v112, v8, v112\n"
    "v_mfma_f32_16x16x32_bf16 v[128:131], a[172:175], a[240:243], v[128:131]\n"
    "ds_read_b64_tr_b16 a[180:181], v2 offset:53760\n"
    "v_mul_f32 v113, v8, v113\n"
    "v_mfma_f32_16x16x32_bf16 v[132:135], a[172:175], a[244:247], v[132:135]\n"
    "ds_read_b64_tr_b16 a[182:183], v2 offset:55808\n"
    "v_mul_f32 v114, v8, v114\n"
    "v_mfma_f32_16x16x32_bf16 v[132:135], a[168:171], a[228:231], v[132:135]\n"
    "ds_read_b64_tr_b16 a[184:185], v2 offset:57856\n"
    "v_mul_f32 v115, v8, v115\n"
    "v_mfma_f32_16x16x32_bf16 v[132:135], a[164:167], a[212:215], v[132:135]\n"
    "ds_read_b64_tr_b16 a[186:187], v2 offset:59904\n"
    "v_mul_f32 v128, v8, v128\n"
    "v_mfma_f32_16x16x32_bf16 v[132:135], a[160:163], a[196:199], v[132:135]\n"
    "ds_read_b64_tr_b16 a[188:189], v2 offset:61952\n"
    "v_mul_f32 v129, v8, v129\n"
    "v_mfma_f32_16x16x32_bf16 v[136:139], a[160:163], a[200:203], v[136:139]\n"
    "ds_read_b64_tr_b16 a[190:191], v2 offset:64000\n"
    "v_mul_f32 v130, v8, v130\n"
    "v_mfma_f32_16x16x32_bf16 v[136:139], a[164:167], a[216:219], v[136:139]\n"
    "buffer_store_dwordx4 v[76:79], v6, s[48:51], s71 offen sc1\n"
    "v_mfma_f32_16x16x32_bf16 v[136:139], a[168:171], a[232:235], v[136:139]\n"
    "s_xor_b32 s71, s71, 0x80\n"
    "v_mul_f32 v131, v8, v131\n"
    "v_cvt_pk_bf16_f32 v128, v128, v129\n"
    "v_mfma_f32_16x16x32_bf16 v[136:139], a[172:175], a[248:251], v[136:139]\n"
    "v_cvt_pk_bf16_f32 v129, v130, v131\n"
    "v_cvt_pk_bf16_f32 v98, v112, v113\n"
    "v_mfma_f32_16x16x32_bf16 v[140:143], a[172:175], a[252:255], v[140:143]\n"
    "v_cvt_pk_bf16_f32 v99, v114, v115\n"
    "v_mul_f32 v132, v9, v132\n"
    "v_mfma_f32_16x16x32_bf16 v[140:143], a[168:171], a[236:239], v[140:143]\n"
    "v_permlane16_swap_b32 v96, v98\n"
    "v_mfma_f32_16x16x32_bf16 v[140:143], a[164:167], a[220:223], v[140:143]\n"
    "v_permlane16_swap_b32 v97, v99\n"
    "v_mfma_f32_16x16x32_bf16 v[140:143], a[160:163], a[204:207], v[140:143]\n"
    "s_waitcnt vmcnt(14) lgkmcnt(0)\n"
    "v_mfma_f32_16x16x32_bf16 v[156:159], a[176:179], a[204:207], v[156:159]\n"
    "s_barrier\n"
    "v_mfma_f32_16x16x32_bf16 v[156:159], a[180:183], a[220:223], v[156:159]\n"
    "ds_read_b64_tr_b16 a[160:161], v1 offset:0\n"
    "v_mfma_f32_16x16x32_bf16 v[156:159], a[184:187], a[236:239], v[156:159]\n"
    "ds_read_b64_tr_b16 a[162:163], v1 offset:2048\n"
    "v_mov_b32 v112, v96\n"
    "v_mfma_f32_16x16x32_bf16 v[156:159], a[188:191], a[252:255], v[156:159]\n"
    "ds_read_b64_tr_b16 a[164:165], v1 offset:4096\n"
    "v_mov_b32 v113, v97\n"
    "v_mfma_f32_16x16x32_bf16 v[152:155], a[188:191], a[248:251], v[152:155]\n"
    "ds_read_b64_tr_b16 a[166:167], v1 offset:6144\n"
    "v_mov_b32 v114, v98\n"
    "v_mfma_f32_16x16x32_bf16 v[152:155], a[184:187], a[232:235], v[152:155]\n"
    "ds_read_b64_tr_b16 a[168:169], v1 offset:8192\n"
    "v_mov_b32 v115, v99\n"
    "v_mfma_f32_16x16x32_bf16 v[152:155], a[180:183], a[216:219], v[152:155]\n"
    "ds_read_b64_tr_b16 a[170:171], v1 offset:10240\n"
    "v_mul_f32 v133, v9, v133\n"
    "v_mfma_f32_16x16x32_bf16 v[152:155], a[176:179], a[200:203], v[152:155]\n"
    "ds_read_b64_tr_b16 a[172:173], v1 offset:12288\n"
    "v_mul_f32 v134, v9, v134\n"
    "v_mfma_f32_16x16x32_bf16 v[148:151], a[176:179], a[196:199], v[148:151]\n"
    "ds_read_b64_tr_b16 a[174:175], v1 offset:14336\n"
    "v_mul_f32 v135, v9, v135\n"
    "v_mfma_f32_16x16x32_bf16 v[148:151], a[180:183], a[212:215], v[148:151]\n"
    "buffer_load_dwordx4 v4, s[40:43], s57 offen offset:0 lds\n"
    "v_mfma_f32_16x16x32_bf16 v[148:151], a[184:187], a[228:231], v[148:151]\n"
    "v_cvt_pk_bf16_f32 v132, v132, v133\n"
    "v_mul_f32 v136, v10, v136\n"
    "v_mfma_f32_16x16x32_bf16 v[148:151], a[188:191], a[244:247], v[148:151]\n"
    "v_cvt_pk_bf16_f32 v133, v134, v135\n"
    "v_mul_f32 v137, v10, v137\n"
    "v_mfma_f32_16x16x32_bf16 v[144:147], a[188:191], a[240:243], v[144:147]\n"
    "s_add_u32 m0, s56, 0xc400\n"
    "s_xor_b32 s57, s54, 0x31100\n"
    "v_mul_f32 v138, v10, v138\n"
    "v_mfma_f32_16x16x32_bf16 v[144:147], a[184:187], a[224:227], v[144:147]\n"
    "buffer_load_dwordx4 v4, s[40:43], s57 offen offset:0 lds\n"
    "v_mfma_f32_16x16x32_bf16 v[144:147], a[180:183], a[208:211], v[144:147]\n"
    "v_mul_f32 v148, v9, v148\n"
    "v_mul_f32 v149, v9, v149\n"
    "v_mfma_f32_16x16x32_bf16 v[144:147], a[176:179], a[192:195], v[144:147]\n"
    "s_waitcnt lgkmcnt(0)\n"
    "s_add_u32 m0, s56, 0xc800\n"
    "v_mul_f32 v150, v9, v150\n"
    "v_mfma_f32_16x16x32_bf16 v[32:35], a[160:163], a[128:131], 0\n"
    "s_xor_b32 s57, s54, 0x32100\n"
    "v_mul_f32 v151, v9, v151\n"
    "v_cvt_pk_bf16_f32 v134, v148, v149\n"
    "v_mfma_f32_16x16x32_bf16 v[32:35], a[164:167], a[136:139], v[32:35]\n"
    "ds_read_b64_tr_b16 a[176:177], v2 offset:0\n"
    "v_cvt_pk_bf16_f32 v135, v150, v151\n"
    "v_mfma_f32_16x16x32_bf16 v[32:35], a[168:171], a[144:147], v[32:35]\n"
    "ds_read_b64_tr_b16 a[178:179], v2 offset:2048\n"
    "v_mul_f32 v144, v8, v144\n"
    "v_mfma_f32_16x16x32_bf16 v[32:35], a[172:175], a[152:155], v[32:35]\n"
    "ds_read_b64_tr_b16 a[180:181], v2 offset:4096\n"
    "v_mul_f32 v145, v8, v145\n"
    "v_mfma_f32_16x16x32_bf16 v[36:39], a[172:175], v[184:187], 0\n"
    "ds_read_b64_tr_b16 a[182:183], v2 offset:6144\n"
    "v_mul_f32 v146, v8, v146\n"
    "v_mfma_f32_16x16x32_bf16 v[36:39], a[168:171], v[176:179], v[36:39]\n"
    "ds_read_b64_tr_b16 a[184:185], v2 offset:8192\n"
    "v_mul_f32 v147, v8, v147\n"
    "v_mfma_f32_16x16x32_bf16 v[36:39], a[164:167], v[168:171], v[36:39]\n"
    "ds_read_b64_tr_b16 a[186:187], v2 offset:10240\n"
    "v_cvt_pk_bf16_f32 v130, v144, v145\n"
    "v_mfma_f32_16x16x32_bf16 v[36:39], a[160:163], v[160:163], v[36:39]\n"
    "ds_read_b64_tr_b16 a[188:189], v2 offset:12288\n"
    "v_cvt_pk_bf16_f32 v131, v146, v147\n"
    "v_mfma_f32_16x16x32_bf16 v[40:43], a[160:163], a[132:135], 0\n"
    "ds_read_b64_tr_b16 a[190:191], v2 offset:14336\n"
    "v_mul_f32 v139, v10, v139\n"
    "v_mfma_f32_16x16x32_bf16 v[40:43], a[164:167], a[140:143], v[40:43]\n"
    "buffer_load_dwordx4 v4, s[40:43], s57 offen offset:0 lds\n"
    "v_mfma_f32_16x16x32_bf16 v[40:43], a[168:171], a[148:151], v[40:43]\n"
    "v_permlane16_swap_b32 v128, v130\n"
    "v_mfma_f32_16x16x32_bf16 v[40:43], a[172:175], a[156:159], v[40:43]\n"
    "v_permlane16_swap_b32 v129, v131\n"
    "v_mfma_f32_16x16x32_bf16 v[44:47], a[172:175], v[188:191], 0\n"
    "s_add_u32 m0, s56, 0xcc00\n"
    "s_xor_b32 s57, s54, 0x33100\n"
    "v_mov_b32_dpp v96, v128 row_ror:8 row_mask:0xf bank_mask:0xc\n"
    "v_mfma_f32_16x16x32_bf16 v[44:47], a[168:171], v[180:183], v[44:47]\n"
    "buffer_load_dwordx4 v4, s[40:43], s57 offen offset:0 lds\n"
    "v_mfma_f32_16x16x32_bf16 v[44:47], a[164:167], v[172:175], v[44:47]\n"
    "v_mov_b32_dpp v97, v129 row_ror:8 row_mask:0xf bank_mask:0xc\n"
    "v_mov_b32_dpp v98, v130 row_ror:8 row_mask:0xf bank_mask:0xc\n"
    "v_mfma_f32_16x16x32_bf16 v[44:47], a[160:163], v[164:167], v[44:47]\n"
    "s_waitcnt lgkmcnt(0)\n"
    "s_xor_b32 s57, s54, 0x20180\n"
    "v_mov_b32_dpp v99, v131 row_ror:8 row_mask:0xf bank_mask:0xc\n"
    "v_mfma_f32_16x16x32_bf16 v[60:63], a[176:179], v[164:167], 0\n"
    "v_mov_b32_dpp v128, v112 row_ror:8 row_mask:0xf bank_mask:0x3\n"
    "v_mov_b32_dpp v129, v113 row_ror:8 row_mask:0xf bank_mask:0x3\n"
    "v_mfma_f32_16x16x32_bf16 v[60:63], a[180:183], v[172:175], v[60:63]\n"
    "ds_read_b64_tr_b16 a[160:161], v1 offset:512\n"
    "v_mov_b32_dpp v130, v114 row_ror:8 row_mask:0xf bank_mask:0x3\n"
    "v_mfma_f32_16x16x32_bf16 v[60:63], a[184:187], v[180:183], v[60:63]\n"
    "ds_read_b64_tr_b16 a[162:163], v1 offset:2560\n"
    "v_mov_b32_dpp v131, v115 row_ror:8 row_mask:0xf bank_mask:0x3\n"
    "v_mfma_f32_16x16x32_bf16 v[60:63], a[188:191], v[188:191], v[60:63]\n"
    "ds_read_b64_tr_b16 a[164:165], v1 offset:4608\n"
    "v_mul_f32 v152, v10, v152\n"
    "v_mfma_f32_16x16x32_bf16 v[56:59], a[188:191], a[156:159], 0\n"
    "ds_read_b64_tr_b16 a[166:167], v1 offset:6656\n"
    "v_mul_f32 v153, v10, v153\n"
    "v_mfma_f32_16x16x32_bf16 v[56:59], a[184:187], a[148:151], v[56:59]\n"
    "ds_read_b64_tr_b16 a[168:169], v1 offset:8704\n"
    "v_mul_f32 v154, v10, v154\n"
    "v_mfma_f32_16x16x32_bf16 v[56:59], a[180:183], a[140:143], v[56:59]\n"
    "ds_read_b64_tr_b16 a[170:171], v1 offset:10752\n"
    "v_mul_f32 v155, v10, v155\n"
    "v_mfma_f32_16x16x32_bf16 v[56:59], a[176:179], a[132:135], v[56:59]\n"
    "ds_read_b64_tr_b16 a[172:173], v1 offset:12800\n"
    "v_cvt_pk_bf16_f32 v136, v136, v137\n"
    "v_mfma_f32_16x16x32_bf16 v[52:55], a[176:179], v[160:163], 0\n"
    "ds_read_b64_tr_b16 a[174:175], v1 offset:14848\n"
    "v_cvt_pk_bf16_f32 v137, v138, v139\n"
    "v_mfma_f32_16x16x32_bf16 v[52:55], a[180:183], v[168:171], v[52:55]\n"
    "buffer_store_dwordx4 v[96:99], v6, s[48:51], s64 offen sc1\n"
    "v_mfma_f32_16x16x32_bf16 v[52:55], a[184:187], v[176:179], v[52:55]\n"
    "s_xor_b32 s64, s64, 0x180\n"
    "v_permlane16_swap_b32 v100, v102\n"
    "v_mfma_f32_16x16x32_bf16 v[52:55], a[188:191], v[184:187], v[52:55]\n"
    "v_permlane16_swap_b32 v132, v134\n"
    "v_mfma_f32_16x16x32_bf16 v[48:51], a[188:191], a[152:155], 0\n"
    "v_permlane16_swap_b32 v101, v103\n"
    "v_mfma_f32_16x16x32_bf16 v[48:51], a[184:187], a[144:147], v[48:51]\n"
    "v_permlane16_swap_b32 v133, v135\n"
    "v_mfma_f32_16x16x32_bf16 v[48:51], a[180:183], a[136:139], v[48:51]\n"
    "v_mov_b32 v116, v100\n"
    "v_mov_b32 v117, v101\n"
    "v_mfma_f32_16x16x32_bf16 v[48:51], a[176:179], a[128:131], v[48:51]\n"
    "s_waitcnt lgkmcnt(0)\n"
    "s_add_u32 m0, s56, 0x0\n"
    "v_mov_b32 v118, v102\n"
    "v_mfma_f32_16x16x32_bf16 v[64:67], a[160:163], a[128:131], 0\n"
    "v_mov_b32 v119, v103\n"
    "v_mov_b32_dpp v100, v132 row_ror:8 row_mask:0xf bank_mask:0xc\n"
    "v_mfma_f32_16x16x32_bf16 v[64:67], a[164:167], a[136:139], v[64:67]\n"
    "ds_read_b64_tr_b16 a[176:177], v2 offset:512\n"
    "v_mov_b32_dpp v101, v133 row_ror:8 row_mask:0xf bank_mask:0xc\n"
    "v_mfma_f32_16x16x32_bf16 v[64:67], a[168:171], a[144:147], v[64:67]\n"
    "ds_read_b64_tr_b16 a[178:179], v2 offset:2560\n"
    "v_mov_b32_dpp v102, v134 row_ror:8 row_mask:0xf bank_mask:0xc\n"
    "v_mfma_f32_16x16x32_bf16 v[64:67], a[172:175], a[152:155], v[64:67]\n"
    "ds_read_b64_tr_b16 a[180:181], v2 offset:4608\n"
    "v_mov_b32_dpp v103, v135 row_ror:8 row_mask:0xf bank_mask:0xc\n"
    "v_mfma_f32_16x16x32_bf16 v[68:71], a[172:175], v[184:187], 0\n"
    "ds_read_b64_tr_b16 a[182:183], v2 offset:6656\n"
    "v_mov_b32_dpp v132, v116 row_ror:8 row_mask:0xf bank_mask:0x3\n"
    "v_mfma_f32_16x16x32_bf16 v[68:71], a[168:171], v[176:179], v[68:71]\n"
    "ds_read_b64_tr_b16 a[184:185], v2 offset:8704\n"
    "v_mov_b32_dpp v133, v117 row_ror:8 row_mask:0xf bank_mask:0x3\n"
    "v_mfma_f32_16x16x32_bf16 v[68:71], a[164:167], v[168:171], v[68:71]\n"
    "ds_read_b64_tr_b16 a[186:187], v2 offset:10752\n"
    "v_mov_b32_dpp v134, v118 row_ror:8 row_mask:0xf bank_mask:0x3\n"
    "v_mfma_f32_16x16x32_bf16 v[68:71], a[160:163], v[160:163], v[68:71]\n"
    "ds_read_b64_tr_b16 a[188:189], v2 offset:12800\n"
    "v_mov_b32_dpp v135, v119 row_ror:8 row_mask:0xf bank_mask:0x3\n"
    "v_mfma_f32_16x16x32_bf16 v[72:75], a[160:163], a[132:135], 0\n"
    "ds_read_b64_tr_b16 a[190:191], v2 offset:14848\n"
    "v_cvt_pk_bf16_f32 v138, v152, v153\n"
    "v_mfma_f32_16x16x32_bf16 v[72:75], a[164:167], a[140:143], v[72:75]\n"
    "buffer_store_dwordx4 v[128:131], v6, s[48:51], s65 offen sc1\n"
    "v_mfma_f32_16x16x32_bf16 v[72:75], a[168:171], a[148:151], v[72:75]\n"
    "s_xor_b32 s65, s65, 0x180\n"
    "v_cvt_pk_bf16_f32 v139, v154, v155\n"
    "v_mov_b32 v120, v104\n"
    "v_mfma_f32_16x16x32_bf16 v[72:75], a[172:175], a[156:159], v[72:75]\n"
    "v_permlane16_swap_b32 v136, v138\n"
    "v_mfma_f32_16x16x32_bf16 v[76:79], a[172:175], v[188:191], 0\n"
    "v_permlane16_swap_b32 v105, v107\n"
    "v_mfma_f32_16x16x32_bf16 v[76:79], a[168:171], v[180:183], v[76:79]\n"
    "v_permlane16_swap_b32 v137, v139\n"
    "v_mfma_f32_16x16x32_bf16 v[76:79], a[164:167], v[172:175], v[76:79]\n"
    "v_mov_b32 v121, v105\n"
    "v_mov_b32 v122, v106\n"
    "v_mfma_f32_16x16x32_bf16 v[76:79], a[160:163], v[164:167], v[76:79]\n"
    "s_waitcnt vmcnt(14) lgkmcnt(0)\n"
    "v_mfma_f32_16x16x32_bf16 v[92:95], a[176:179], v[164:167], 0\n"
    "s_barrier\n"
    "v_mfma_f32_16x16x32_bf16 v[92:95], a[180:183], v[172:175], v[92:95]\n"
    "ds_read_b64_tr_b16 a[160:161], v1 offset:16384\n"
    "v_mfma_f32_16x16x32_bf16 v[92:95], a[184:187], v[180:183], v[92:95]\n"
    "ds_read_b64_tr_b16 a[162:163], v1 offset:18432\n"
    "v_mov_b32 v123, v107\n"
    "v_mfma_f32_16x16x32_bf16 v[92:95], a[188:191], v[188:191], v[92:95]\n"
    "ds_read_b64_tr_b16 a[164:165], v1 offset:20480\n"
    "v_mov_b32_dpp v104, v136 row_ror:8 row_mask:0xf bank_mask:0xc\n"
    "v_mfma_f32_16x16x32_bf16 v[88:91], a[188:191], a[156:159], 0\n"
    "ds_read_b64_tr_b16 a[166:167], v1 offset:22528\n"
    "v_mov_b32_dpp v105, v137 row_ror:8 row_mask:0xf bank_mask:0xc\n"
    "v_mfma_f32_16x16x32_bf16 v[88:91], a[184:187], a[148:151], v[88:91]\n"
    "ds_read_b64_tr_b16 a[168:169], v1 offset:24576\n"
    "v_mov_b32_dpp v106, v138 row_ror:8 row_mask:0xf bank_mask:0xc\n"
    "v_mfma_f32_16x16x32_bf16 v[88:91], a[180:183], a[140:143], v[88:91]\n"
    "ds_read_b64_tr_b16 a[170:171], v1 offset:26624\n"
    "v_mov_b32_dpp v107, v139 row_ror:8 row_mask:0xf bank_mask:0xc\n"
    "v_mfma_f32_16x16x32_bf16 v[88:91], a[176:179], a[132:135], v[88:91]\n"
    "ds_read_b64_tr_b16 a[172:173], v1 offset:28672\n"
    "v_mov_b32_dpp v136, v120 row_ror:8 row_mask:0xf bank_mask:0x3\n"
    "v_mfma_f32_16x16x32_bf16 v[84:87], a[176:179], v[160:163], 0\n"
    "ds_read_b64_tr_b16 a[174:175], v1 offset:30720\n"
    "v_mov_b32_dpp v137, v121 row_ror:8 row_mask:0xf bank_mask:0x3\n"
    "v_mfma_f32_16x16x32_bf16 v[84:87], a[180:183], v[168:171], v[84:87]\n"
    "buffer_load_dwordx4 v4, s[40:43], s57 offen offset:0 lds\n"
    "v_mfma_f32_16x16x32_bf16 v[84:87], a[184:187], v[176:179], v[84:87]\n"
    "v_mov_b32_dpp v138, v122 row_ror:8 row_mask:0xf bank_mask:0x3\n"
    "v_mov_b32_dpp v139, v123 row_ror:8 row_mask:0xf bank_mask:0x3\n"
    "v_mfma_f32_16x16x32_bf16 v[84:87], a[188:191], v[184:187], v[84:87]\n"
    "v_mul_f32 v111, v11, v111\n"
    "v_mul_f32 v124, v11, v124\n"
    "v_mfma_f32_16x16x32_bf16 v[80:83], a[188:191], a[152:155], 0\n"
    "s_add_u32 m0, s56, 0x400\n"
    "s_xor_b32 s57, s54, 0x21180\n"
    "v_mul_f32 v125, v11, v125\n"
    "v_mfma_f32_16x16x32_bf16 v[80:83], a[184:187], a[144:147], v[80:83]\n"
    "buffer_load_dwordx4 v4, s[40:43], s57 offen offset:0 lds\n"
    "v_mfma_f32_16x16x32_bf16 v[80:83], a[180:183], a[136:139], v[80:83]\n"
    "v_mul_f32 v126, v11, v126\n"
    "v_mul_f32 v127, v11, v127\n"
    "v_mfma_f32_16x16x32_bf16 v[80:83], a[176:179], a[128:131], v[80:83]\n"
    "s_waitcnt lgkmcnt(0)\n"
    "s_add_u32 m0, s56, 0x800\n"
    "v_mul_f32 v140, v11, v140\n"
    "v_mfma_f32_16x16x32_bf16 v[32:35], a[160:163], a[192:195], v[32:35]\n"
    "s_xor_b32 s57, s54, 0x22180\n"
    "v_mul_f32 v141, v11, v141\n"
    "v_mul_f32 v142, v11, v142\n"
    "v_mfma_f32_16x16x32_bf16 v[32:35], a[164:167], a[208:211], v[32:35]\n"
    "ds_read_b64_tr_b16 a[176:177], v2 offset:16384\n"
    "v_mul_f32 v143, v11, v143\n"
    "v_mfma_f32_16x16x32_bf16 v[32:35], a[168:171], a[224:227], v[32:35]\n"
    "ds_read_b64_tr_b16 a[178:179], v2 offset:18432\n"
    "v_mul_f32 v156, v11, v156\n"
    "v_mfma_f32_16x16x32_bf16 v[32:35], a[172:175], a[240:243], v[32:35]\n"
    "ds_read_b64_tr_b16 a[180:181], v2 offset:20480\n"
    "v_mul_f32 v157, v11, v157\n"
    "v_mfma_f32_16x16x32_bf16 v[36:39], a[172:175], a[244:247], v[36:39]\n"
    "ds_read_b64_tr_b16 a[182:183], v2 offset:22528\n"
    "v_mul_f32 v158, v11, v158\n"
    "v_mfma_f32_16x16x32_bf16 v[36:39], a[168:171], a[228:231], v[36:39]\n"
    "ds_read_b64_tr_b16 a[184:185], v2 offset:24576\n"
    "v_mul_f32 v159, v11, v159\n"
    "v_mfma_f32_16x16x32_bf16 v[36:39], a[164:167], a[212:215], v[36:39]\n"
    "ds_read_b64_tr_b16 a[186:187], v2 offset:26624\n"
    "v_cvt_pk_bf16_f32 v108, v108, v109\n"
    "v_mfma_f32_16x16x32_bf16 v[36:39], a[160:163], a[196:199], v[36:39]\n"
    "ds_read_b64_tr_b16 a[188:189], v2 offset:28672\n"
    "v_cvt_pk_bf16_f32 v140, v140, v141\n"
    "L_pad11_%=:\n"
    "s_cbranch_execz L_ret2_%=\n"
    "v_mfma_f32_16x16x32_bf16 v[40:43], a[160:163], a[200:203], v[40:43]\n"
    "ds_read_b64_tr_b16 a[190:191], v2 offset:30720\n"
    "v_cvt_pk_bf16_f32 v109, v110, v111\n"
    "v_mfma_f32_16x16x32_bf16 v[40:43], a[164:167], a[216:219], v[40:43]\n"
    "buffer_load_dwordx4 v4, s[40:43], s57 offen offset:0 lds\n"
    "v_mfma_f32_16x16x32_bf16 v[40:43], a[168:171], a[232:235], v[40:43]\n"
    "v_cvt_pk_bf16_f32 v141, v142, v143\n"
    "v_cvt_pk_bf16_f32 v110, v124, v125\n"
    "v_mfma_f32_16x16x32_bf16 v[40:43], a[172:175], a[248:251], v[40:43]\n"
    "v_cvt_pk_bf16_f32 v111, v126, v127\n"
    "v_cvt_pk_bf16_f32 v142, v156, v157\n"
    "v_mfma_f32_16x16x32_bf16 v[44:47], a[172:175], a[252:255], v[44:47]\n"
    "s_add_u32 m0, s56, 0xc00\n"
    "s_xor_b32 s57, s54, 0x23180\n"
    "v_cvt_pk_bf16_f32 v143, v158, v159\n"
    "v_mfma_f32_16x16x32_bf16 v[44:47], a[168:171], a[236:239], v[44:47]\n"
    "buffer_load_dwordx4 v4, s[40:43], s57 offen offset:0 lds\n"
    "v_mfma_f32_16x16x32_bf16 v[44:47], a[164:167], a[220:223], v[44:47]\n"
    "v_permlane16_swap_b32 v108, v110\n"
    "v_mfma_f32_16x16x32_bf16 v[44:47], a[160:163], a[204:207], v[44:47]\n"
    "s_waitcnt lgkmcnt(0)\n"
    "s_xor_b32 s57, s54, 0x10180\n"
    "v_mov_b32 v124, v108\n"
    "v_mfma_f32_16x16x32_bf16 v[60:63], a[176:179], a[204:207], v[60:63]\n"
    "v_permlane16_swap_b32 v140, v142\n"
    "v_mfma_f32_16x16x32_bf16 v[60:63], a[180:183], a[220:223], v[60:63]\n"
    "ds_read_b64_tr_b16 a[160:161], v1 offset:16896\n"
    "v_mov_b32 v126, v110\n"
    "v_mfma_f32_16x16x32_bf16 v[60:63], a[184:187], a[236:239], v[60:63]\n"
    "ds_read_b64_tr_b16 a[162:163], v1 offset:18944\n"
    "v_mov_b32_dpp v108, v140 row_ror:8 row_mask:0xf bank_mask:0xc\n"
    "v_mfma_f32_16x16x32_bf16 v[60:63], a[188:191], a[252:255], v[60:63]\n"
    "ds_read_b64_tr_b16 a[164:165], v1 offset:20992\n"
    "v_mov_b32_dpp v110, v142 row_ror:8 row_mask:0xf bank_mask:0xc\n"
    "v_mfma_f32_16x16x32_bf16 v[56:59], a[188:191], a[248:251], v[56:59]\n"
    "ds_read_b64_tr_b16 a[166:167], v1 offset:23040\n"
    "v_mov_b32_dpp v140, v124 row_ror:8 row_mask:0xf bank_mask:0x3\n"
    "v_mfma_f32_16x16x32_bf16 v[56:59], a[184:187], a[232:235], v[56:59]\n"
    "ds_read_b64_tr_b16 a[168:169], v1 offset:25088\n"
    "v_mov_b32_dpp v142, v126 row_ror:8 row_mask:0xf bank_mask:0x3\n"
    "v_mfma_f32_16x16x32_bf16 v[56:59], a[180:183], a[216:219], v[56:59]\n"
    "ds_read_b64_tr_b16 a[170:171], v1 offset:27136\n"
    "v_mfma_f32_16x16x32_bf16 v[56:59], a[176:179], a[200:203], v[56:59]\n"
    "ds_read_b64_tr_b16 a[172:173], v1 offset:29184\n"
    "v_mfma_f32_16x16x32_bf16 v[52:55], a[176:179], a[196:199], v[52:55]\n"
    "ds_read_b64_tr_b16 a[174:175], v1 offset:31232\n"
    "v_mfma_f32_16x16x32_bf16 v[52:55], a[180:183], a[212:215], v[52:55]\n"
    "buffer_store_dwordx4 v[100:103], v6, s[48:51], s66 offen sc1\n"
    "v_mfma_f32_16x16x32_bf16 v[52:55], a[184:187], a[228:231], v[52:55]\n"
    "s_xor_b32 s66, s66, 0x180\n"
    "v_permlane16_swap_b32 v109, v111\n"
    "v_mfma_f32_16x16x32_bf16 v[52:55], a[188:191], a[244:247], v[52:55]\n"
    "v_permlane16_swap_b32 v141, v143\n"
    "v_mfma_f32_16x16x32_bf16 v[48:51], a[188:191], a[240:243], v[48:51]\n"
    "v_mov_b32 v125, v109\n"
    "v_mov_b32 v127, v111\n"
    "v_mfma_f32_16x16x32_bf16 v[48:51], a[184:187], a[224:227], v[48:51]\n"
    "v_mov_b32_dpp v109, v141 row_ror:8 row_mask:0xf bank_mask:0xc\n"
    "v_mov_b32_dpp v111, v143 row_ror:8 row_mask:0xf bank_mask:0xc\n"
    "v_mfma_f32_16x16x32_bf16 v[48:51], a[180:183], a[208:211], v[48:51]\n"
    "v_mov_b32_dpp v141, v125 row_ror:8 row_mask:0xf bank_mask:0x3\n"
    "v_mov_b32_dpp v143, v127 row_ror:8 row_mask:0xf bank_mask:0x3\n"
    "v_mfma_f32_16x16x32_bf16 v[48:51], a[176:179], a[192:195], v[48:51]\n"
    "s_waitcnt lgkmcnt(0)\n"
    "s_add_u32 m0, s56, 0x4000\n"
    "v_mfma_f32_16x16x32_bf16 v[64:67], a[160:163], a[192:195], v[64:67]\n"
    "v_mfma_f32_16x16x32_bf16 v[64:67], a[164:167], a[208:211], v[64:67]\n"
    "ds_read_b64_tr_b16 a[176:177], v2 offset:16896\n"
    "v_mfma_f32_16x16x32_bf16 v[64:67], a[168:171], a[224:227], v[64:67]\n"
    "ds_read_b64_tr_b16 a[178:179], v2 offset:18944\n"
    "v_add_u32 v31, s93, v0\n"
    "v_mfma_f32_16x16x32_bf16 v[64:67], a[172:175], a[240:243], v[64:67]\n"
    "ds_read_b64_tr_b16 a[180:181], v2 offset:20992\n"
    "v_add_u32 v31, 0x20000, v31\n"
    "v_mfma_f32_16x16x32_bf16 v[68:71], a[172:175], a[244:247], v[68:71]\n"
    "ds_read_b64_tr_b16 a[182:183], v2 offset:23040\n"
    "v_mfma_f32_16x16x32_bf16 v[68:71], a[168:171], a[228:231], v[68:71]\n"
    "ds_read_b64_tr_b16 a[184:185], v2 offset:25088\n"
    "v_mfma_f32_16x16x32_bf16 v[68:71], a[164:167], a[212:215], v[68:71]\n"
    "ds_read_b64_tr_b16 a[186:187], v2 offset:27136\n"
    "v_mfma_f32_16x16x32_bf16 v[68:71], a[160:163], a[196:199], v[68:71]\n"
    "ds_read_b64_tr_b16 a[188:189], v2 offset:29184\n"
    "v_mfma_f32_16x16x32_bf16 v[72:75], a[160:163], a[200:203], v[72:75]\n"
    "ds_read_b64_tr_b16 a[190:191], v2 offset:31232\n"
    "v_mfma_f32_16x16x32_bf16 v[72:75], a[164:167], a[216:219], v[72:75]\n"
    "buffer_store_dwordx4 v[132:135], v6, s[48:51], s67 offen sc1\n"
    "v_mfma_f32_16x16x32_bf16 v[72:75], a[168:171], a[232:235], v[72:75]\n"
    "ds_read_b128 a[64:67], v31 offset:0\n"
    "v_mfma_f32_16x16x32_bf16 v[72:75], a[172:175], a[248:251], v[72:75]\n"
    "ds_read_b128 a[68:71], v31 offset:512\n"
    "v_mfma_f32_16x16x32_bf16 v[76:79], a[172:175], a[252:255], v[76:79]\n"
    "s_xor_b32 s67, s67, 0x180\n"
    "v_mfma_f32_16x16x32_bf16 v[76:79], a[168:171], a[236:239], v[76:79]\n"
    "v_mfma_f32_16x16x32_bf16 v[76:79], a[164:167], a[220:223], v[76:79]\n"
    "v_mfma_f32_16x16x32_bf16 v[76:79], a[160:163], a[204:207], v[76:79]\n"
    "s_waitcnt vmcnt(14) lgkmcnt(2)\n"
    "v_mfma_f32_16x16x32_bf16 v[92:95], a[176:179], a[204:207], v[92:95]\n"
    "s_barrier\n"
    "v_mfma_f32_16x16x32_bf16 v[92:95], a[180:183], a[220:223], v[92:95]\n"
    "ds_read_b64_tr_b16 a[160:161], v1 offset:32768\n"
    "v_mfma_f32_16x16x32_bf16 v[92:95], a[184:187], a[236:239], v[92:95]\n"
    "ds_read_b64_tr_b16 a[162:163], v1 offset:34816\n"
    "v_mfma_f32_16x16x32_bf16 v[92:95], a[188:191], a[252:255], v[92:95]\n"
    "ds_read_b64_tr_b16 a[164:165], v1 offset:36864\n"
    "v_mfma_f32_16x16x32_bf16 v[88:91], a[188:191], a[248:251], v[88:91]\n"
    "ds_read_b64_tr_b16 a[166:167], v1 offset:38912\n"
    "v_mfma_f32_16x16x32_bf16 v[88:91], a[184:187], a[232:235], v[88:91]\n"
    "ds_read_b64_tr_b16 a[168:169], v1 offset:40960\n"
    "v_mfma_f32_16x16x32_bf16 v[88:91], a[180:183], a[216:219], v[88:91]\n"
    "ds_read_b64_tr_b16 a[170:171], v1 offset:43008\n"
    "v_mfma_f32_16x16x32_bf16 v[88:91], a[176:179], a[200:203], v[88:91]\n"
    "ds_read_b64_tr_b16 a[172:173], v1 offset:45056\n"
    "v_mfma_f32_16x16x32_bf16 v[84:87], a[176:179], a[196:199], v[84:87]\n"
    "ds_read_b64_tr_b16 a[174:175], v1 offset:47104\n"
    "v_mfma_f32_16x16x32_bf16 v[84:87], a[180:183], a[212:215], v[84:87]\n"
    "buffer_load_dwordx4 v4, s[40:43], s57 offen offset:0 lds\n"
    "v_mfma_f32_16x16x32_bf16 v[84:87], a[184:187], a[228:231], v[84:87]\n"
    "ds_read_b128 a[72:75], v31 offset:1024\n"
    "v_mfma_f32_16x16x32_bf16 v[84:87], a[188:191], a[244:247], v[84:87]\n"
    "ds_read_b128 a[76:79], v31 offset:1536\n"
    "v_mfma_f32_16x16x32_bf16 v[80:83], a[188:191], a[240:243], v[80:83]\n"
    "s_add_u32 m0, s56, 0x4400\n"
    "s_xor_b32 s57, s54, 0x11180\n"
    "v_mfma_f32_16x16x32_bf16 v[80:83], a[184:187], a[224:227], v[80:83]\n"
    "buffer_load_dwordx4 v4, s[40:43], s57 offen offset:0 lds\n"
    "v_mfma_f32_16x16x32_bf16 v[80:83], a[180:183], a[208:211], v[80:83]\n"
    "s_mov_b32 s72, s73\n"
    "v_mfma_f32_16x16x32_bf16 v[80:83], a[176:179], a[192:195], v[80:83]\n"
    "s_waitcnt lgkmcnt(2)\n"
    "s_add_u32 m0, s56, 0x4800\n"
    "v_mfma_f32_16x16x32_bf16 v[32:35], a[160:163], a[0:3], v[32:35]\n"
    "s_xor_b32 s57, s54, 0x12180\n"
    "v_mfma_f32_16x16x32_bf16 v[32:35], a[164:167], a[16:19], v[32:35]\n"
    "ds_read_b64_tr_b16 a[176:177], v2 offset:32768\n"
    "v_mfma_f32_16x16x32_bf16 v[32:35], a[168:171], a[32:35], v[32:35]\n"
    "ds_read_b64_tr_b16 a[178:179], v2 offset:34816\n"
    "v_mfma_f32_16x16x32_bf16 v[32:35], a[172:175], a[48:51], v[32:35]\n"
    "ds_read_b64_tr_b16 a[180:181], v2 offset:36864\n"
    "v_mfma_f32_16x16x32_bf16 v[36:39], a[172:175], a[52:55], v[36:39]\n"
    "ds_read_b64_tr_b16 a[182:183], v2 offset:38912\n"
    "v_mfma_f32_16x16x32_bf16 v[36:39], a[168:171], a[36:39], v[36:39]\n"
    "ds_read_b64_tr_b16 a[184:185], v2 offset:40960\n"
    "v_mfma_f32_16x16x32_bf16 v[36:39], a[164:167], a[20:23], v[36:39]\n"
    "ds_read_b64_tr_b16 a[186:187], v2 offset:43008\n"
    "v_mfma_f32_16x16x32_bf16 v[36:39], a[160:163], a[4:7], v[36:39]\n"
    "ds_read_b64_tr_b16 a[188:189], v2 offset:45056\n"
    "v_mfma_f32_16x16x32_bf16 v[40:43], a[160:163], a[8:11], v[40:43]\n"
    "ds_read_b64_tr_b16 a[190:191], v2 offset:47104\n"
    "v_mfma_f32_16x16x32_bf16 v[40:43], a[164:167], a[24:27], v[40:43]\n"
    "buffer_load_dwordx4 v4, s[40:43], s57 offen offset:0 lds\n"
    "v_mfma_f32_16x16x32_bf16 v[40:43], a[168:171], a[40:43], v[40:43]\n"
    "ds_read_b128 a[80:83], v31 offset:2048\n"
    "v_mfma_f32_16x16x32_bf16 v[40:43], a[172:175], a[56:59], v[40:43]\n"
    "ds_read_b128 a[84:87], v31 offset:2560\n"
    "v_mfma_f32_16x16x32_bf16 v[44:47], a[172:175], a[60:63], v[44:47]\n"
    "s_add_u32 m0, s56, 0x4c00\n"
    "s_xor_b32 s57, s54, 0x13180\n"
    "v_mfma_f32_16x16x32_bf16 v[44:47], a[168:171], a[44:47], v[44:47]\n"
    "buffer_load_dwordx4 v4, s[40:43], s57 offen offset:0 lds\n"
    "v_mfma_f32_16x16x32_bf16 v[44:47], a[164:167], a[28:31], v[44:47]\n"
    "s_mov_b32 s73, s74\n"
    "v_mfma_f32_16x16x32_bf16 v[44:47], a[160:163], a[12:15], v[44:47]\n"
    "s_waitcnt lgkmcnt(2)\n"
    "s_xor_b32 s57, s54, 0x180\n"
    "v_mbcnt_lo_u32_b32 v30, -1, 0\n"
    "v_mbcnt_hi_u32_b32 v30, -1, v30\n"
    "v_mfma_f32_16x16x32_bf16 v[60:63], a[176:179], a[12:15], v[60:63]\n"
    "s_mov_b32 s74, s75\n"
    "v_mfma_f32_16x16x32_bf16 v[60:63], a[180:183], a[28:31], v[60:63]\n"
    "ds_read_b64_tr_b16 a[160:161], v1 offset:33280\n"
    "v_mfma_f32_16x16x32_bf16 v[60:63], a[184:187], a[44:47], v[60:63]\n"
    "ds_read_b64_tr_b16 a[162:163], v1 offset:35328\n"
    "v_mfma_f32_16x16x32_bf16 v[60:63], a[188:191], a[60:63], v[60:63]\n"
    "ds_read_b64_tr_b16 a[164:165], v1 offset:37376\n"
    "v_mfma_f32_16x16x32_bf16 v[56:59], a[188:191], a[56:59], v[56:59]\n"
    "ds_read_b64_tr_b16 a[166:167], v1 offset:39424\n"
    "v_mfma_f32_16x16x32_bf16 v[56:59], a[184:187], a[40:43], v[56:59]\n"
    "ds_read_b64_tr_b16 a[168:169], v1 offset:41472\n"
    "v_mfma_f32_16x16x32_bf16 v[56:59], a[180:183], a[24:27], v[56:59]\n"
    "ds_read_b64_tr_b16 a[170:171], v1 offset:43520\n"
    "v_mfma_f32_16x16x32_bf16 v[56:59], a[176:179], a[8:11], v[56:59]\n"
    "ds_read_b64_tr_b16 a[172:173], v1 offset:45568\n"
    "v_mfma_f32_16x16x32_bf16 v[52:55], a[176:179], a[4:7], v[52:55]\n"
    "ds_read_b64_tr_b16 a[174:175], v1 offset:47616\n"
    "v_mfma_f32_16x16x32_bf16 v[52:55], a[180:183], a[20:23], v[52:55]\n"
    "buffer_store_dwordx4 v[104:107], v6, s[48:51], s68 offen sc1\n"
    "v_mfma_f32_16x16x32_bf16 v[52:55], a[184:187], a[36:39], v[52:55]\n"
    "ds_read_b128 a[88:91], v31 offset:3072\n"
    "v_mfma_f32_16x16x32_bf16 v[52:55], a[188:191], a[52:55], v[52:55]\n"
    "ds_read_b128 a[92:95], v31 offset:3584\n"
    "v_mfma_f32_16x16x32_bf16 v[48:51], a[188:191], a[48:51], v[48:51]\n"
    "s_xor_b32 s68, s68, 0x180\n"
    "v_mfma_f32_16x16x32_bf16 v[48:51], a[184:187], a[32:35], v[48:51]\n"
    "s_mov_b32 s76, s77\n"
    "v_mfma_f32_16x16x32_bf16 v[48:51], a[180:183], a[16:19], v[48:51]\n"
    "s_mov_b32 s77, s78\n"
    "v_mfma_f32_16x16x32_bf16 v[48:51], a[176:179], a[0:3], v[48:51]\n"
    "s_waitcnt lgkmcnt(2)\n"
    "s_add_u32 m0, s56, 0x8000\n"
    "v_lshlrev_b32 v30, 7, v30\n"
    "v_mfma_f32_16x16x32_bf16 v[64:67], a[160:163], a[0:3], v[64:67]\n"
    "s_mov_b32 s78, s79\n"
    "v_mfma_f32_16x16x32_bf16 v[64:67], a[164:167], a[16:19], v[64:67]\n"
    "ds_read_b64_tr_b16 a[176:177], v2 offset:33280\n"
    "v_mfma_f32_16x16x32_bf16 v[64:67], a[168:171], a[32:35], v[64:67]\n"
    "ds_read_b64_tr_b16 a[178:179], v2 offset:35328\n"
    "v_mfma_f32_16x16x32_bf16 v[64:67], a[172:175], a[48:51], v[64:67]\n"
    "ds_read_b64_tr_b16 a[180:181], v2 offset:37376\n"
    "v_mfma_f32_16x16x32_bf16 v[68:71], a[172:175], a[52:55], v[68:71]\n"
    "ds_read_b64_tr_b16 a[182:183], v2 offset:39424\n"
    "v_mfma_f32_16x16x32_bf16 v[68:71], a[168:171], a[36:39], v[68:71]\n"
    "ds_read_b64_tr_b16 a[184:185], v2 offset:41472\n"
    "v_mfma_f32_16x16x32_bf16 v[68:71], a[164:167], a[20:23], v[68:71]\n"
    "ds_read_b64_tr_b16 a[186:187], v2 offset:43520\n"
    "v_mfma_f32_16x16x32_bf16 v[68:71], a[160:163], a[4:7], v[68:71]\n"
    "ds_read_b64_tr_b16 a[188:189], v2 offset:45568\n"
    "v_mfma_f32_16x16x32_bf16 v[72:75], a[160:163], a[8:11], v[72:75]\n"
    "ds_read_b64_tr_b16 a[190:191], v2 offset:47616\n"
    "v_mfma_f32_16x16x32_bf16 v[72:75], a[164:167], a[24:27], v[72:75]\n"
    "buffer_store_dwordx4 v[136:139], v6, s[48:51], s69 offen sc1\n"
    "v_mfma_f32_16x16x32_bf16 v[72:75], a[168:171], a[40:43], v[72:75]\n"
    "buffer_load_dword v29, v30, s[96:99], 0 offen\n"
    "v_mfma_f32_16x16x32_bf16 v[72:75], a[172:175], a[56:59], v[72:75]\n"
    "s_xor_b32 s69, s69, 0x180\n"
    "v_mfma_f32_16x16x32_bf16 v[76:79], a[172:175], a[60:63], v[76:79]\n"
    "buffer_load_dword v28, v30, s[96:99], s35 offen\n"
    "v_mfma_f32_16x16x32_bf16 v[76:79], a[168:171], a[44:47], v[76:79]\n"
    "s_mov_b32 s80, s81\n"
    "v_mfma_f32_16x16x32_bf16 v[76:79], a[164:167], a[28:31], v[76:79]\n"
    "s_mov_b32 s81, s82\n"
    "v_mfma_f32_16x16x32_bf16 v[76:79], a[160:163], a[12:15], v[76:79]\n"
    "s_waitcnt vmcnt(16) lgkmcnt(0)\n"
    "v_mfma_f32_16x16x32_bf16 v[92:95], a[176:179], a[12:15], v[92:95]\n"
    "s_barrier\n"
    "v_mfma_f32_16x16x32_bf16 v[92:95], a[180:183], a[28:31], v[92:95]\n"
    "ds_read_b64_tr_b16 a[160:161], v1 offset:49152\n"
    "v_mfma_f32_16x16x32_bf16 v[92:95], a[184:187], a[44:47], v[92:95]\n"
    "ds_read_b64_tr_b16 a[162:163], v1 offset:51200\n"
    "v_subrev_u32 v24, s86, v8\n"
    "v_mfma_f32_16x16x32_bf16 v[92:95], a[188:191], a[60:63], v[92:95]\n"
    "ds_read_b64_tr_b16 a[164:165], v1 offset:53248\n"
    "v_subrev_u32 v25, s86, v9\n"
    "v_mfma_f32_16x16x32_bf16 v[88:91], a[188:191], a[56:59], v[88:91]\n"
    "ds_read_b64_tr_b16 a[166:167], v1 offset:55296\n"
    "v_subrev_u32 v26, s86, v10\n"
    "v_mfma_f32_16x16x32_bf16 v[88:91], a[184:187], a[40:43], v[88:91]\n"
    "ds_read_b64_tr_b16 a[168:169], v1 offset:57344\n"
    "v_subrev_u32 v27, s86, v11\n"
    "v_mfma_f32_16x16x32_bf16 v[88:91], a[180:183], a[24:27], v[88:91]\n"
    "ds_read_b64_tr_b16 a[170:171], v1 offset:59392\n"
    "v_max3_u32 v24, v24, v25, v26\n"
    "v_mfma_f32_16x16x32_bf16 v[88:91], a[176:179], a[8:11], v[88:91]\n"
    "ds_read_b64_tr_b16 a[172:173], v1 offset:61440\n"
    "v_max_u32 v24, v24, v27\n"
    "v_mfma_f32_16x16x32_bf16 v[84:87], a[176:179], a[4:7], v[84:87]\n"
    "ds_read_b64_tr_b16 a[174:175], v1 offset:63488\n"
    "v_cmp_lt_u32 vcc, s87, v24\n"
    "v_mfma_f32_16x16x32_bf16 v[84:87], a[180:183], a[20:23], v[84:87]\n"
    "buffer_load_dwordx4 v4, s[40:43], s57 offen offset:0 lds\n"
    "v_mfma_f32_16x16x32_bf16 v[84:87], a[184:187], a[36:39], v[84:87]\n"
    "s_mov_b32 s82, s83\n"
    "v_mfma_f32_16x16x32_bf16 v[84:87], a[188:191], a[52:55], v[84:87]\n"
    "v_mfma_f32_16x16x32_bf16 v[80:83], a[188:191], a[48:51], v[80:83]\n"
    "s_add_u32 m0, s56, 0x8400\n"
    "s_xor_b32 s57, s54, 0x1180\n"
    "v_mfma_f32_16x16x32_bf16 v[80:83], a[184:187], a[32:35], v[80:83]\n"
    "buffer_load_dwordx4 v4, s[40:43], s57 offen offset:0 lds\n"
    "v_mfma_f32_16x16x32_bf16 v[80:83], a[180:183], a[16:19], v[80:83]\n"
    "v_mfma_f32_16x16x32_bf16 v[80:83], a[176:179], a[0:3], v[80:83]\n"
    "s_waitcnt lgkmcnt(0)\n"
    "s_add_u32 m0, s56, 0x8800\n"
    "v_mfma_f32_16x16x32_bf16 v[32:35], a[160:163], v[192:195], v[32:35]\n"
    "s_xor_b32 s57, s54, 0x2180\n"
    "v_mfma_f32_16x16x32_bf16 v[32:35], a[164:167], v[208:211], v[32:35]\n"
    "ds_read_b64_tr_b16 a[176:177], v2 offset:49152\n"
    "v_mfma_f32_16x16x32_bf16 v[32:35], a[168:171], v[224:227], v[32:35]\n"
    "ds_read_b64_tr_b16 a[178:179], v2 offset:51200\n"
    "v_mfma_f32_16x16x32_bf16 v[32:35], a[172:175], v[240:243], v[32:35]\n"
    "ds_read_b64_tr_b16 a[180:181], v2 offset:53248\n"
    "v_mfma_f32_16x16x32_bf16 v[36:39], a[172:175], v[244:247], v[36:39]\n"
    "ds_read_b64_tr_b16 a[182:183], v2 offset:55296\n"
    "v_mfma_f32_16x16x32_bf16 v[36:39], a[168:171], v[228:231], v[36:39]\n"
    "ds_read_b64_tr_b16 a[184:185], v2 offset:57344\n"
    "v_mfma_f32_16x16x32_bf16 v[36:39], a[164:167], v[212:215], v[36:39]\n"
    "ds_read_b64_tr_b16 a[186:187], v2 offset:59392\n"
    "s_nop 0\n"
    "v_mul_f32 v32, v8, v32\n"
    "v_mfma_f32_16x16x32_bf16 v[36:39], a[160:163], v[196:199], v[36:39]\n"
    "ds_read_b64_tr_b16 a[188:189], v2 offset:61440\n"
    "v_mul_f32 v33, v8, v33\n"
    "v_mfma_f32_16x16x32_bf16 v[40:43], a[160:163], v[200:203], v[40:43]\n"
    "ds_read_b64_tr_b16 a[190:191], v2 offset:63488\n"
    "v_mul_f32 v34, v8, v34\n"
    "v_mfma_f32_16x16x32_bf16 v[40:43], a[164:167], v[216:219], v[40:43]\n"
    "buffer_load_dwordx4 v4, s[40:43], s57 offen offset:0 lds\n"
    "v_mfma_f32_16x16x32_bf16 v[40:43], a[168:171], v[232:235], v[40:43]\n"
    "s_cmp_lg_u64 vcc, 0\n"
    "s_cselect_b32 s85, s95, 0\n"
    "v_mul_f32 v35, v8, v35\n"
    "v_cvt_pk_bf16_f32 v32, v32, v33\n"
    "v_mfma_f32_16x16x32_bf16 v[40:43], a[172:175], v[248:251], v[40:43]\n"
    "s_or_b32 s59, s59, s85\n"
    "v_cvt_pk_bf16_f32 v33, v34, v35\n"
    "v_mul_f32 v36, v9, v36\n"
    "v_mfma_f32_16x16x32_bf16 v[44:47], a[172:175], v[252:255], v[44:47]\n"
    "s_add_u32 m0, s56, 0x8c00\n"
    "s_xor_b32 s57, s54, 0x3180\n"
    "v_mul_f32 v37, v9, v37\n"
    "v_mfma_f32_16x16x32_bf16 v[44:47], a[168:171], v[236:239], v[44:47]\n"
    "buffer_load_dwordx4 v4, s[40:43], s57 offen offset:0 lds\n"
    "v_mfma_f32_16x16x32_bf16 v[44:47], a[164:167], v[220:223], v[44:47]\n"
    "s_lshl_b32 s95, s95, 1\n"
    "v_mul_f32 v38, v9, v38\n"
    "v_mul_f32 v39, v9, v39\n"
    "v_mfma_f32_16x16x32_bf16 v[44:47], a[160:163], v[204:207], v[44:47]\n"
    "s_waitcnt lgkmcnt(0)\n"
    "s_xor_b32 s57, s54, 0x30180\n"
    "v_cvt_pk_bf16_f32 v36, v36, v37\n"
    "v_mfma_f32_16x16x32_bf16 v[60:63], a[176:179], v[204:207], v[60:63]\n"
    "v_cvt_pk_bf16_f32 v37, v38, v39\n"
    "v_mul_f32 v40, v10, v40\n"
    "v_mfma_f32_16x16x32_bf16 v[60:63], a[180:183], v[220:223], v[60:63]\n"
    "ds_read_b64_tr_b16 a[160:161], v1 offset:49664\n"
    "v_mul_f32 v41, v10, v41\n"
    "v_mfma_f32_16x16x32_bf16 v[60:63], a[184:187], v[236:239], v[60:63]\n"
    "ds_read_b64_tr_b16 a[162:163], v1 offset:51712\n"
    "v_mul_f32 v42, v10, v42\n"
    "v_mfma_f32_16x16x32_bf16 v[60:63], a[188:191], v[252:255], v[60:63]\n"
    "ds_read_b64_tr_b16 a[164:165], v1 offset:53760\n"
    "v_mul_f32 v43, v10, v43\n"
    "v_mfma_f32_16x16x32_bf16 v[56:59], a[188:191], v[248:251], v[56:59]\n"
    "ds_read_b64_tr_b16 a[166:167], v1 offset:55808\n"
    "v_cvt_pk_bf16_f32 v40, v40, v41\n"
    "v_mfma_f32_16x16x32_bf16 v[56:59], a[184:187], v[232:235], v[56:59]\n"
    "ds_read_b64_tr_b16 a[168:169], v1 offset:57856\n"
    "v_cvt_pk_bf16_f32 v41, v42, v43\n"
    "v_mfma_f32_16x16x32_bf16 v[56:59], a[180:183], v[216:219], v[56:59]\n"
    "ds_read_b64_tr_b16 a[170:171], v1 offset:59904\n"
    "v_mul_f32 v44, v11, v44\n"
    "v_mfma_f32_16x16x32_bf16 v[56:59], a[176:179], v[200:203], v[56:59]\n"
    "ds_read_b64_tr_b16 a[172:173], v1 offset:61952\n"
    "v_mul_f32 v45, v11, v45\n"
    "v_mfma_f32_16x16x32_bf16 v[52:55], a[176:179], v[196:199], v[52:55]\n"
    "ds_read_b64_tr_b16 a[174:175], v1 offset:64000\n"
    "v_mul_f32 v46, v11, v46\n"
    "v_mfma_f32_16x16x32_bf16 v[52:55], a[180:183], v[212:215], v[52:55]\n"
    "buffer_store_dwordx4 v[108:111], v6, s[48:51], s70 offen sc1\n"
    "v_mfma_f32_16x16x32_bf16 v[52:55], a[184:187], v[228:231], v[52:55]\n"
    "s_xor_b32 s70, s70, 0x180\n"
    "v_mul_f32 v56, v10, v56\n"
    "v_mul_f32 v57, v10, v57\n"
    "v_mfma_f32_16x16x32_bf16 v[52:55], a[188:191], v[244:247], v[52:55]\n"
    "v_mul_f32 v58, v10, v58\n"
    "v_mul_f32 v59, v10, v59\n"
    "v_mfma_f32_16x16x32_bf16 v[48:51], a[188:191], v[240:243], v[48:51]\n"
    "v_cvt_pk_bf16_f32 v42, v56, v57\n"
    "v_cvt_pk_bf16_f32 v43, v58, v59\n"
    "v_mfma_f32_16x16x32_bf16 v[48:51], a[184:187], v[224:227], v[48:51]\n"
    "v_permlane16_swap_b32 v40, v42\n"
    "v_mfma_f32_16x16x32_bf16 v[48:51], a[180:183], v[208:211], v[48:51]\n"
    "v_mul_f32 v52, v9, v52\n"
    "v_mul_f32 v53, v9, v53\n"
    "v_mfma_f32_16x16x32_bf16 v[48:51], a[176:179], v[192:195], v[48:51]\n"
    "s_waitcnt lgkmcnt(0)\n"
    "s_add_u32 m0, s56, 0xc000\n"
    "v_mul_f32 v54, v9, v54\n"
    "v_mfma_f32_16x16x32_bf16 v[64:67], a[160:163], v[192:195], v[64:67]\n"
    "v_mul_f32 v55, v9, v55\n"
    "v_cvt_pk_bf16_f32 v38, v52, v53\n"
    "v_mfma_f32_16x16x32_bf16 v[64:67], a[164:167], v[208:211], v[64:67]\n"
    "ds_read_b64_tr_b16 a[176:177], v2 offset:49664\n"
    "v_cvt_pk_bf16_f32 v39, v54, v55\n"
    "v_mfma_f32_16x16x32_bf16 v[64:67], a[168:171], v[224:227], v[64:67]\n"
    "ds_read_b64_tr_b16 a[178:179], v2 offset:51712\n"
    "v_mul_f32 v48, v8, v48\n"
    "v_mfma_f32_16x16x32_bf16 v[64:67], a[172:175], v[240:243], v[64:67]\n"
    "ds_read_b64_tr_b16 a[180:181], v2 offset:53760\n"
    "v_mul_f32 v49, v8, v49\n"
    "v_mfma_f32_16x16x32_bf16 v[68:71], a[172:175], v[244:247], v[68:71]\n"
    "ds_read_b64_tr_b16 a[182:183], v2 offset:55808\n"
    "v_mul_f32 v50, v8, v50\n"
    "v_mfma_f32_16x16x32_bf16 v[68:71], a[168:171], v[228:231], v[68:71]\n"
    "ds_read_b64_tr_b16 a[184:185], v2 offset:57856\n"
    "v_mul_f32 v51, v8, v51\n"
    "v_mfma_f32_16x16x32_bf16 v[68:71], a[164:167], v[212:215], v[68:71]\n"
    "ds_read_b64_tr_b16 a[186:187], v2 offset:59904\n"
    "v_mul_f32 v64, v8, v64\n"
    "v_mfma_f32_16x16x32_bf16 v[68:71], a[160:163], v[196:199], v[68:71]\n"
    "ds_read_b64_tr_b16 a[188:189], v2 offset:61952\n"
    "v_mul_f32 v65, v8, v65\n"
    "v_mfma_f32_16x16x32_bf16 v[72:75], a[160:163], v[200:203], v[72:75]\n"
    "ds_read_b64_tr_b16 a[190:191], v2 offset:64000\n"
    "v_mul_f32 v66, v8, v66\n"
    "v_mfma_f32_16x16x32_bf16 v[72:75], a[164:167], v[216:219], v[72:75]\n"
    "buffer_store_dwordx4 v[140:143], v6, s[48:51], s71 offen sc1\n"
    "v_mfma_f32_16x16x32_bf16 v[72:75], a[168:171], v[232:235], v[72:75]\n"
    "s_xor_b32 s71, s71, 0x180\n"
    "v_mul_f32 v67, v8, v67\n"
    "v_cvt_pk_bf16_f32 v64, v64, v65\n"
    "v_mfma_f32_16x16x32_bf16 v[72:75], a[172:175], v[248:251], v[72:75]\n"
    "v_cvt_pk_bf16_f32 v65, v66, v67\n"
    "v_cvt_pk_bf16_f32 v34, v48, v49\n"
    "v_mfma_f32_16x16x32_bf16 v[76:79], a[172:175], v[252:255], v[76:79]\n"
    "v_cvt_pk_bf16_f32 v35, v50, v51\n"
    "v_mul_f32 v68, v9, v68\n"
    "v_mfma_f32_16x16x32_bf16 v[76:79], a[168:171], v[236:239], v[76:79]\n"
    "v_permlane16_swap_b32 v32, v34\n"
    "v_mfma_f32_16x16x32_bf16 v[76:79], a[164:167], v[220:223], v[76:79]\n"
    "v_permlane16_swap_b32 v33, v35\n"
    "v_mfma_f32_16x16x32_bf16 v[76:79], a[160:163], v[204:207], v[76:79]\n"
    "s_waitcnt vmcnt(16) lgkmcnt(0)\n"
    "v_mfma_f32_16x16x32_bf16 v[92:95], a[176:179], v[204:207], v[92:95]\n"
    "s_barrier\n"
    "v_mfma_f32_16x16x32_bf16 v[92:95], a[180:183], v[220:223], v[92:95]\n"
    "ds_read_b64_tr_b16 a[160:161], v1 offset:0\n"
    "v_mfma_f32_16x16x32_bf16 v[92:95], a[184:187], v[236:239], v[92:95]\n"
    "ds_read_b64_tr_b16 a[162:163], v1 offset:2048\n"
    "v_mov_b32 v48, v32\n"
    "v_mfma_f32_16x16x32_bf16 v[92:95], a[188:191], v[252:255], v[92:95]\n"
    "ds_read_b64_tr_b16 a[164:165], v1 offset:4096\n"
    "v_mov_b32 v49, v33\n"
    "v_mfma_f32_16x16x32_bf16 v[88:91], a[188:191], v[248:251], v[88:91]\n"
    "ds_read_b64_tr_b16 a[166:167], v1 offset:6144\n"
    "v_mov_b32 v50, v34\n"
    "v_mfma_f32_16x16x32_bf16 v[88:91], a[184:187], v[232:235], v[88:91]\n"
    "ds_read_b64_tr_b16 a[168:169], v1 offset:8192\n"
    "v_mov_b32 v51, v35\n"
    "v_mfma_f32_16x16x32_bf16 v[88:91], a[180:183], v[216:219], v[88:91]\n"
    "ds_read_b64_tr_b16 a[170:171], v1 offset:10240\n"
    "v_mul_f32 v69, v9, v69\n"
    "v_mfma_f32_16x16x32_bf16 v[88:91], a[176:179], v[200:203], v[88:91]\n"
    "ds_read_b64_tr_b16 a[172:173], v1 offset:12288\n"
    "v_mul_f32 v70, v9, v70\n"
    "v_mfma_f32_16x16x32_bf16 v[84:87], a[176:179], v[196:199], v[84:87]\n"
    "ds_read_b64_tr_b16 a[174:175], v1 offset:14336\n"
    "v_mul_f32 v71, v9, v71\n"
    "v_mfma_f32_16x16x32_bf16 v[84:87], a[180:183], v[212:215], v[84:87]\n"
    "buffer_load_dwordx4 v4, s[40:43], s57 offen offset:0 lds\n"
    "v_mfma_f32_16x16x32_bf16 v[84:87], a[184:187], v[228:231], v[84:87]\n"
    "v_cvt_pk_bf16_f32 v68, v68, v69\n"
    "v_mul_f32 v72, v10, v72\n"
    "v_mfma_f32_16x16x32_bf16 v[84:87], a[188:191], v[244:247], v[84:87]\n"
    "v_cvt_pk_bf16_f32 v69, v70, v71\n"
    "v_mul_f32 v73, v10, v73\n"
    "v_mfma_f32_16x16x32_bf16 v[80:83], a[188:191], v[240:243], v[80:83]\n"
    "s_add_u32 m0, s56, 0xc400\n"
    "s_xor_b32 s57, s54, 0x31180\n"
    "v_mul_f32 v74, v10, v74\n"
    "v_mfma_f32_16x16x32_bf16 v[80:83], a[184:187], v[224:227], v[80:83]\n"
    "buffer_load_dwordx4 v4, s[40:43], s57 offen offset:0 lds\n"
    "v_mfma_f32_16x16x32_bf16 v[80:83], a[180:183], v[208:211], v[80:83]\n"
    "v_mul_f32 v84, v9, v84\n"
    "v_mul_f32 v85, v9, v85\n"
    "v_mfma_f32_16x16x32_bf16 v[80:83], a[176:179], v[192:195], v[80:83]\n"
    "s_waitcnt lgkmcnt(0)\n"
    "s_add_u32 m0, s56, 0xc800\n"
    "v_mul_f32 v86, v9, v86\n"
    "v_mfma_f32_16x16x32_bf16 v[96:99], a[160:163], a[0:3], 0\n"
    "s_xor_b32 s57, s54, 0x32180\n"
    "v_mul_f32 v87, v9, v87\n"
    "v_cvt_pk_bf16_f32 v70, v84, v85\n"
    "v_mfma_f32_16x16x32_bf16 v[96:99], a[164:167], a[16:19], v[96:99]\n"
    "ds_read_b64_tr_b16 a[176:177], v2 offset:0\n"
    "v_cvt_pk_bf16_f32 v71, v86, v87\n"
    "v_mfma_f32_16x16x32_bf16 v[96:99], a[168:171], a[32:35], v[96:99]\n"
    "ds_read_b64_tr_b16 a[178:179], v2 offset:2048\n"
    "v_mul_f32 v80, v8, v80\n"
    "v_mfma_f32_16x16x32_bf16 v[96:99], a[172:175], a[48:51], v[96:99]\n"
    "ds_read_b64_tr_b16 a[180:181], v2 offset:4096\n"
    "v_mul_f32 v81, v8, v81\n"
    "v_mfma_f32_16x16x32_bf16 v[100:103], a[172:175], a[52:55], 0\n"
    "ds_read_b64_tr_b16 a[182:183], v2 offset:6144\n"
    "v_mul_f32 v82, v8, v82\n"
    "v_mfma_f32_16x16x32_bf16 v[100:103], a[168:171], a[36:39], v[100:103]\n"
    "ds_read_b64_tr_b16 a[184:185], v2 offset:8192\n"
    "v_mul_f32 v83, v8, v83\n"
    "v_mfma_f32_16x16x32_bf16 v[100:103], a[164:167], a[20:23], v[100:103]\n"
    "ds_read_b64_tr_b16 a[186:187], v2 offset:10240\n"
    "v_cvt_pk_bf16_f32 v66, v80, v81\n"
    "v_mfma_f32_16x16x32_bf16 v[100:103], a[160:163], a[4:7], v[100:103]\n"
    "ds_read_b64_tr_b16 a[188:189], v2 offset:12288\n"
    "v_cvt_pk_bf16_f32 v67, v82, v83\n"
    "v_mfma_f32_16x16x32_bf16 v[104:107], a[160:163], a[8:11], 0\n"
    "ds_read_b64_tr_b16 a[190:191], v2 offset:14336\n"
    "v_mul_f32 v75, v10, v75\n"
    "v_mfma_f32_16x16x32_bf16 v[104:107], a[164:167], a[24:27], v[104:107]\n"
    "buffer_load_dwordx4 v4, s[40:43], s57 offen offset:0 lds\n"
    "v_mfma_f32_16x16x32_bf16 v[104:107], a[168:171], a[40:43], v[104:107]\n"
    "v_permlane16_swap_b32 v64, v66\n"
    "v_mfma_f32_16x16x32_bf16 v[104:107], a[172:175], a[56:59], v[104:107]\n"
    "v_permlane16_swap_b32 v65, v67\n"
    "v_mfma_f32_16x16x32_bf16 v[108:111], a[172:175], a[60:63], 0\n"
    "s_add_u32 m0, s56, 0xcc00\n"
    "s_xor_b32 s57, s54, 0x33180\n"
    "v_mov_b32_dpp v32, v64 row_ror:8 row_mask:0xf bank_mask:0xc\n"
    "v_mfma_f32_16x16x32_bf16 v[108:111], a[168:171], a[44:47], v[108:111]\n"
    "buffer_load_dwordx4 v4, s[40:43], s57 offen offset:0 lds\n"
    "v_mfma_f32_16x16x32_bf16 v[108:111], a[164:167], a[28:31], v[108:111]\n"
    "v_mov_b32_dpp v33, v65 row_ror:8 row_mask:0xf bank_mask:0xc\n"
    "v_mov_b32_dpp v34, v66 row_ror:8 row_mask:0xf bank_mask:0xc\n"
    "v_mfma_f32_16x16x32_bf16 v[108:111], a[160:163], a[12:15], v[108:111]\n"
    "s_waitcnt lgkmcnt(0)\n"
    "s_add_u32 s57, s53, 0x0\n"
    "v_mov_b32_dpp v35, v67 row_ror:8 row_mask:0xf bank_mask:0xc\n"
    "v_mfma_f32_16x16x32_bf16 v[124:127], a[176:179], a[12:15], 0\n"
    "v_mov_b32_dpp v64, v48 row_ror:8 row_mask:0xf bank_mask:0x3\n"
    "v_mov_b32_dpp v65, v49 row_ror:8 row_mask:0xf bank_mask:0x3\n"
    "v_mfma_f32_16x16x32_bf16 v[124:127], a[180:183], a[28:31], v[124:127]\n"
    "ds_read_b64_tr_b16 a[160:161], v1 offset:512\n"
    "v_mov_b32_dpp v66, v50 row_ror:8 row_mask:0xf bank_mask:0x3\n"
    "v_mfma_f32_16x16x32_bf16 v[124:127], a[184:187], a[44:47], v[124:127]\n"
    "ds_read_b64_tr_b16 a[162:163], v1 offset:2560\n"
    "v_mov_b32_dpp v67, v51 row_ror:8 row_mask:0xf bank_mask:0x3\n"
    "v_mfma_f32_16x16x32_bf16 v[124:127], a[188:191], a[60:63], v[124:127]\n"
    "ds_read_b64_tr_b16 a[164:165], v1 offset:4608\n"
    "v_mul_f32 v88, v10, v88\n"
    "v_mfma_f32_16x16x32_bf16 v[120:123], a[188:191], a[56:59], 0\n"
    "ds_read_b64_tr_b16 a[166:167], v1 offset:6656\n"
    "v_mul_f32 v89, v10, v89\n"
    "v_mfma_f32_16x16x32_bf16 v[120:123], a[184:187], a[40:43], v[120:123]\n"
    "ds_read_b64_tr_b16 a[168:169], v1 offset:8704\n"
    "v_mul_f32 v90, v10, v90\n"
    "v_mfma_f32_16x16x32_bf16 v[120:123], a[180:183], a[24:27], v[120:123]\n"
    "ds_read_b64_tr_b16 a[170:171], v1 offset:10752\n"
    "v_mul_f32 v91, v10, v91\n"
    "v_mfma_f32_16x16x32_bf16 v[120:123], a[176:179], a[8:11], v[120:123]\n"
    "ds_read_b64_tr_b16 a[172:173], v1 offset:12800\n"
    "v_cvt_pk_bf16_f32 v72, v72, v73\n"
    "v_mfma_f32_16x16x32_bf16 v[116:119], a[176:179], a[4:7], 0\n"
    "ds_read_b64_tr_b16 a[174:175], v1 offset:14848\n"
    "v_cvt_pk_bf16_f32 v73, v74, v75\n"
    "v_mfma_f32_16x16x32_bf16 v[116:119], a[180:183], a[20:23], v[116:119]\n"
    "buffer_store_dwordx4 v[32:35], v6, s[48:51], s64 offen sc1\n"
    "v_mfma_f32_16x16x32_bf16 v[116:119], a[184:187], a[36:39], v[116:119]\n"
    "s_xor_b32 s64, s64, 0x80\n"
    "v_permlane16_swap_b32 v36, v38\n"
    "v_mfma_f32_16x16x32_bf16 v[116:119], a[188:191], a[52:55], v[116:119]\n"
    "v_permlane16_swap_b32 v68, v70\n"
    "v_mfma_f32_16x16x32_bf16 v[112:115], a[188:191], a[48:51], 0\n"
    "v_permlane16_swap_b32 v37, v39\n"
    "v_mfma_f32_16x16x32_bf16 v[112:115], a[184:187], a[32:35], v[112:115]\n"
    "v_permlane16_swap_b32 v69, v71\n"
    "v_mfma_f32_16x16x32_bf16 v[112:115], a[180:183], a[16:19], v[112:115]\n"
    "v_mov_b32 v52, v36\n"
    "v_mov_b32 v53, v37\n"
    "v_mfma_f32_16x16x32_bf16 v[112:115], a[176:179], a[0:3], v[112:115]\n"
    "s_waitcnt lgkmcnt(0)\n"
    "s_add_u32 m0, s56, 0x0\n"
    "v_mov_b32 v54, v38\n"
    "v_mfma_f32_16x16x32_bf16 v[128:131], a[160:163], a[0:3], 0\n"
    "v_mov_b32 v55, v39\n"
    "v_mov_b32_dpp v36, v68 row_ror:8 row_mask:0xf bank_mask:0xc\n"
    "v_mfma_f32_16x16x32_bf16 v[128:131], a[164:167], a[16:19], v[128:131]\n"
    "ds_read_b64_tr_b16 a[176:177], v2 offset:512\n"
    "v_mov_b32_dpp v37, v69 row_ror:8 row_mask:0xf bank_mask:0xc\n"
    "v_mfma_f32_16x16x32_bf16 v[128:131], a[168:171], a[32:35], v[128:131]\n"
    "ds_read_b64_tr_b16 a[178:179], v2 offset:2560\n"
    "v_mov_b32_dpp v38, v70 row_ror:8 row_mask:0xf bank_mask:0xc\n"
    "v_mfma_f32_16x16x32_bf16 v[128:131], a[172:175], a[48:51], v[128:131]\n"
    "ds_read_b64_tr_b16 a[180:181], v2 offset:4608\n"
    "v_mov_b32_dpp v39, v71 row_ror:8 row_mask:0xf bank_mask:0xc\n"
    "v_mfma_f32_16x16x32_bf16 v[132:135], a[172:175], a[52:55], 0\n"
    "ds_read_b64_tr_b16 a[182:183], v2 offset:6656\n"
    "v_mov_b32_dpp v68, v52 row_ror:8 row_mask:0xf bank_mask:0x3\n"
    "v_mfma_f32_16x16x32_bf16 v[132:135], a[168:171], a[36:39], v[132:135]\n"
    "ds_read_b64_tr_b16 a[184:185], v2 offset:8704\n"
    "v_mov_b32_dpp v69, v53 row_ror:8 row_mask:0xf bank_mask:0x3\n"
    "v_mfma_f32_16x16x32_bf16 v[132:135], a[164:167], a[20:23], v[132:135]\n"
    "ds_read_b64_tr_b16 a[186:187], v2 offset:10752\n"
    "v_mov_b32_dpp v70, v54 row_ror:8 row_mask:0xf bank_mask:0x3\n"
    "L_pad12_%=:\n"
    "s_cbranch_execz L_ret3_%=\n"
    "v_mfma_f32_16x16x32_bf16 v[132:135], a[160:163], a[4:7], v[132:135]\n"
    "ds_read_b64_tr_b16 a[188:189], v2 offset:12800\n"
    "v_mov_b32_dpp v71, v55 row_ror:8 row_mask:0xf bank_mask:0x3\n"
    "v_mfma_f32_16x16x32_bf16 v[136:139], a[160:163], a[8:11], 0\n"
    "ds_read_b64_tr_b16 a[190:191], v2 offset:14848\n"
    "v_cvt_pk_bf16_f32 v74, v88, v89\n"
    "v_mfma_f32_16x16x32_bf16 v[136:139], a[164:167], a[24:27], v[136:139]\n"
    "buffer_store_dwordx4 v[64:67], v6, s[48:51], s65 offen sc1\n"
    "v_mfma_f32_16x16x32_bf16 v[136:139], a[168:171], a[40:43], v[136:139]\n"
    "s_xor_b32 s65, s65, 0x80\n"
    "v_cvt_pk_bf16_f32 v75, v90, v91\n"
    "v_mov_b32 v56, v40\n"
    "v_mfma_f32_16x16x32_bf16 v[136:139], a[172:175], a[56:59], v[136:139]\n"
    "v_permlane16_swap_b32 v72, v74\n"
    "v_mfma_f32_16x16x32_bf16 v[140:143], a[172:175], a[60:63], 0\n"
    "v_permlane16_swap_b32 v41, v43\n"
    "v_mfma_f32_16x16x32_bf16 v[140:143], a[168:171], a[44:47], v[140:143]\n"
    "v_permlane16_swap_b32 v73, v75\n"
    "v_mfma_f32_16x16x32_bf16 v[140:143], a[164:167], a[28:31], v[140:143]\n"
    "v_mov_b32 v57, v41\n"
    "v_mov_b32 v58, v42\n"
    "v_mfma_f32_16x16x32_bf16 v[140:143], a[160:163], a[12:15], v[140:143]\n"
    "s_waitcnt vmcnt(16) lgkmcnt(0)\n"
    "v_mfma_f32_16x16x32_bf16 v[156:159], a[176:179], a[12:15], 0\n"
    "s_barrier\n"
    "v_mfma_f32_16x16x32_bf16 v[156:159], a[180:183], a[28:31], v[156:159]\n"
    "ds_read_b64_tr_b16 a[160:161], v1 offset:16384\n"
    "v_mfma_f32_16x16x32_bf16 v[156:159], a[184:187], a[44:47], v[156:159]\n"
    "ds_read_b64_tr_b16 a[162:163], v1 offset:18432\n"
    "v_mov_b32 v59, v43\n"
    "v_mfma_f32_16x16x32_bf16 v[156:159], a[188:191], a[60:63], v[156:159]\n"
    "ds_read_b64_tr_b16 a[164:165], v1 offset:20480\n"
    "v_mov_b32_dpp v40, v72 row_ror:8 row_mask:0xf bank_mask:0xc\n"
    "v_mfma_f32_16x16x32_bf16 v[152:155], a[188:191], a[56:59], 0\n"
    "ds_read_b64_tr_b16 a[166:167], v1 offset:22528\n"
    "v_mov_b32_dpp v41, v73 row_ror:8 row_mask:0xf bank_mask:0xc\n"
    "v_mfma_f32_16x16x32_bf16 v[152:155], a[184:187], a[40:43], v[152:155]\n"
    "ds_read_b64_tr_b16 a[168:169], v1 offset:24576\n"
    "v_mov_b32_dpp v42, v74 row_ror:8 row_mask:0xf bank_mask:0xc\n"
    "v_mfma_f32_16x16x32_bf16 v[152:155], a[180:183], a[24:27], v[152:155]\n"
    "ds_read_b64_tr_b16 a[170:171], v1 offset:26624\n"
    "v_mov_b32_dpp v43, v75 row_ror:8 row_mask:0xf bank_mask:0xc\n"
    "v_mfma_f32_16x16x32_bf16 v[152:155], a[176:179], a[8:11], v[152:155]\n"
    "ds_read_b64_tr_b16 a[172:173], v1 offset:28672\n"
    "v_mov_b32_dpp v72, v56 row_ror:8 row_mask:0xf bank_mask:0x3\n"
    "v_mfma_f32_16x16x32_bf16 v[148:151], a[176:179], a[4:7], 0\n"
    "ds_read_b64_tr_b16 a[174:175], v1 offset:30720\n"
    "v_mov_b32_dpp v73, v57 row_ror:8 row_mask:0xf bank_mask:0x3\n"
    "v_mfma_f32_16x16x32_bf16 v[148:151], a[180:183], a[20:23], v[148:151]\n"
    "buffer_load_dwordx4 v3, s[36:39], s57 offen offset:0 lds\n"
    "v_mfma_f32_16x16x32_bf16 v[148:151], a[184:187], a[36:39], v[148:151]\n"
    "v_mov_b32_dpp v74, v58 row_ror:8 row_mask:0xf bank_mask:0x3\n"
    "v_mov_b32_dpp v75, v59 row_ror:8 row_mask:0xf bank_mask:0x3\n"
    "v_mfma_f32_16x16x32_bf16 v[148:151], a[188:191], a[52:55], v[148:151]\n"
    "v_mul_f32 v47, v11, v47\n"
    "v_mul_f32 v60, v11, v60\n"
    "v_mfma_f32_16x16x32_bf16 v[144:147], a[188:191], a[48:51], 0\n"
    "s_add_u32 m0, s56, 0x380\n"
    "v_mul_f32 v61, v11, v61\n"
    "v_mul_f32 v62, v11, v62\n"
    "v_mfma_f32_16x16x32_bf16 v[144:147], a[184:187], a[32:35], v[144:147]\n"
    "buffer_load_dwordx4 v3, s[36:39], s57 offen offset:128 lds\n"
    "v_mfma_f32_16x16x32_bf16 v[144:147], a[180:183], a[16:19], v[144:147]\n"
    "v_mul_f32 v63, v11, v63\n"
    "v_mul_f32 v76, v11, v76\n"
    "v_mfma_f32_16x16x32_bf16 v[144:147], a[176:179], a[0:3], v[144:147]\n"
    "s_waitcnt lgkmcnt(0)\n"
    "s_add_u32 m0, s56, 0x700\n"
    "v_mul_f32 v77, v11, v77\n"
    "s_cmp_eq_u32 s58, 1\n"
    "s_cbranch_scc1 L_tail_%=\n"
    "v_mfma_f32_16x16x32_bf16 v[96:99], a[160:163], a[192:195], v[96:99]\n"
    "v_mul_f32 v78, v11, v78\n"
    "v_mul_f32 v79, v11, v79\n"
    "v_mfma_f32_16x16x32_bf16 v[96:99], a[164:167], a[208:211], v[96:99]\n"
    "ds_read_b64_tr_b16 a[176:177], v2 offset:16384\n"
    "v_mul_f32 v92, v11, v92\n"
    "v_mfma_f32_16x16x32_bf16 v[96:99], a[168:171], a[224:227], v[96:99]\n"
    "ds_read_b64_tr_b16 a[178:179], v2 offset:18432\n"
    "v_add_u32 v31, s93, v0\n"
    "v_mfma_f32_16x16x32_bf16 v[96:99], a[172:175], a[240:243], v[96:99]\n"
    "ds_read_b64_tr_b16 a[180:181], v2 offset:20480\n"
    "v_add_u32 v31, 0x10000, v31\n"
    "v_mfma_f32_16x16x32_bf16 v[100:103], a[172:175], a[244:247], v[100:103]\n"
    "ds_read_b64_tr_b16 a[182:183], v2 offset:22528\n"
    "v_mul_f32 v93, v11, v93\n"
    "v_mfma_f32_16x16x32_bf16 v[100:103], a[168:171], a[228:231], v[100:103]\n"
    "ds_read_b64_tr_b16 a[184:185], v2 offset:24576\n"
    "v_mul_f32 v94, v11, v94\n"
    "v_mfma_f32_16x16x32_bf16 v[100:103], a[164:167], a[212:215], v[100:103]\n"
    "ds_read_b64_tr_b16 a[186:187], v2 offset:26624\n"
    "v_mul_f32 v95, v11, v95\n"
    "v_mfma_f32_16x16x32_bf16 v[100:103], a[160:163], a[196:199], v[100:103]\n"
    "ds_read_b64_tr_b16 a[188:189], v2 offset:28672\n"
    "v_cvt_pk_bf16_f32 v44, v44, v45\n"
    "v_mfma_f32_16x16x32_bf16 v[104:107], a[160:163], a[200:203], v[104:107]\n"
    "ds_read_b64_tr_b16 a[190:191], v2 offset:30720\n"
    "v_cvt_pk_bf16_f32 v76, v76, v77\n"
    "v_mfma_f32_16x16x32_bf16 v[104:107], a[164:167], a[216:219], v[104:107]\n"
    "buffer_load_dwordx4 v3, s[36:39], s57 offen offset:256 lds\n"
    "v_mfma_f32_16x16x32_bf16 v[104:107], a[168:171], a[232:235], v[104:107]\n"
    "ds_read_b128 a[0:3], v31 offset:0\n"
    "v_cvt_pk_bf16_f32 v45, v46, v47\n"
    "v_mfma_f32_16x16x32_bf16 v[104:107], a[172:175], a[248:251], v[104:107]\n"
    "ds_read_b128 a[4:7], v31 offset:512\n"
    "v_cvt_pk_bf16_f32 v77, v78, v79\n"
    "v_mfma_f32_16x16x32_bf16 v[108:111], a[172:175], a[252:255], v[108:111]\n"
    "s_add_u32 m0, s56, 0xa80\n"
    "v_cvt_pk_bf16_f32 v46, v60, v61\n"
    "v_cvt_pk_bf16_f32 v47, v62, v63\n"
    "v_mfma_f32_16x16x32_bf16 v[108:111], a[168:171], a[236:239], v[108:111]\n"
    "buffer_load_dwordx4 v3, s[36:39], s57 offen offset:384 lds\n"
    "v_mfma_f32_16x16x32_bf16 v[108:111], a[164:167], a[220:223], v[108:111]\n"
    "ds_read_b128 a[8:11], v31 offset:1024\n"
    "v_cvt_pk_bf16_f32 v78, v92, v93\n"
    "v_mfma_f32_16x16x32_bf16 v[108:111], a[160:163], a[204:207], v[108:111]\n"
    "s_waitcnt lgkmcnt(3)\n"
    "s_add_u32 s57, s53, 0x4000\n"
    "v_cvt_pk_bf16_f32 v79, v94, v95\n"
    "v_mfma_f32_16x16x32_bf16 v[124:127], a[176:179], a[204:207], v[124:127]\n"
    "ds_read_b128 a[12:15], v31 offset:1536\n"
    "v_mfma_f32_16x16x32_bf16 v[124:127], a[180:183], a[220:223], v[124:127]\n"
    "ds_read_b64_tr_b16 a[160:161], v1 offset:16896\n"
    "v_mfma_f32_16x16x32_bf16 v[124:127], a[184:187], a[236:239], v[124:127]\n"
    "ds_read_b64_tr_b16 a[162:163], v1 offset:18944\n"
    "v_mfma_f32_16x16x32_bf16 v[124:127], a[188:191], a[252:255], v[124:127]\n"
    "ds_read_b64_tr_b16 a[164:165], v1 offset:20992\n"
    "v_mfma_f32_16x16x32_bf16 v[120:123], a[188:191], a[248:251], v[120:123]\n"
    "ds_read_b64_tr_b16 a[166:167], v1 offset:23040\n"
    "v_mfma_f32_16x16x32_bf16 v[120:123], a[184:187], a[232:235], v[120:123]\n"
    "ds_read_b64_tr_b16 a[168:169], v1 offset:25088\n"
    "v_mfma_f32_16x16x32_bf16 v[120:123], a[180:183], a[216:219], v[120:123]\n"
    "ds_read_b64_tr_b16 a[170:171], v1 offset:27136\n"
    "v_mfma_f32_16x16x32_bf16 v[120:123], a[176:179], a[200:203], v[120:123]\n"
    "ds_read_b64_tr_b16 a[172:173], v1 offset:29184\n"
    "v_mfma_f32_16x16x32_bf16 v[116:119], a[176:179], a[196:199], v[116:119]\n"
    "ds_read_b64_tr_b16 a[174:175], v1 offset:31232\n"
    "v_mfma_f32_16x16x32_bf16 v[116:119], a[180:183], a[212:215], v[116:119]\n"
    "buffer_store_dwordx4 v[36:39], v6, s[48:51], s66 offen sc1\n"
    "v_mfma_f32_16x16x32_bf16 v[116:119], a[184:187], a[228:231], v[116:119]\n"
    "ds_read_b128 a[16:19], v31 offset:2048\n"
    "v_mfma_f32_16x16x32_bf16 v[116:119], a[188:191], a[244:247], v[116:119]\n"
    "ds_read_b128 a[20:23], v31 offset:2560\n"
    "v_mfma_f32_16x16x32_bf16 v[112:115], a[188:191], a[240:243], v[112:115]\n"
    "ds_read_b128 a[24:27], v31 offset:3072\n"
    "v_mfma_f32_16x16x32_bf16 v[112:115], a[184:187], a[224:227], v[112:115]\n"
    "ds_read_b128 a[28:31], v31 offset:3584\n"
    "v_mfma_f32_16x16x32_bf16 v[112:115], a[180:183], a[208:211], v[112:115]\n"
    "ds_read_b128 a[32:35], v31 offset:32768\n"
    "v_mfma_f32_16x16x32_bf16 v[112:115], a[176:179], a[192:195], v[112:115]\n"
    "s_waitcnt lgkmcnt(5)\n"
    "s_add_u32 m0, s56, 0x4000\n"
    "v_mfma_f32_16x16x32_bf16 v[128:131], a[160:163], a[192:195], v[128:131]\n"
    "ds_read_b128 a[36:39], v31 offset:33280\n"
    "v_mfma_f32_16x16x32_bf16 v[128:131], a[164:167], a[208:211], v[128:131]\n"
    "ds_read_b64_tr_b16 a[176:177], v2 offset:16896\n"
    "v_mfma_f32_16x16x32_bf16 v[128:131], a[168:171], a[224:227], v[128:131]\n"
    "ds_read_b64_tr_b16 a[178:179], v2 offset:18944\n"
    "v_mfma_f32_16x16x32_bf16 v[128:131], a[172:175], a[240:243], v[128:131]\n"
    "ds_read_b64_tr_b16 a[180:181], v2 offset:20992\n"
    "v_mfma_f32_16x16x32_bf16 v[132:135], a[172:175], a[244:247], v[132:135]\n"
    "ds_read_b64_tr_b16 a[182:183], v2 offset:23040\n"
    "v_mfma_f32_16x16x32_bf16 v[132:135], a[168:171], a[228:231], v[132:135]\n"
    "ds_read_b64_tr_b16 a[184:185], v2 offset:25088\n"
    "v_mfma_f32_16x16x32_bf16 v[132:135], a[164:167], a[212:215], v[132:135]\n"
    "ds_read_b64_tr_b16 a[186:187], v2 offset:27136\n"
    "v_mfma_f32_16x16x32_bf16 v[132:135], a[160:163], a[196:199], v[132:135]\n"
    "ds_read_b64_tr_b16 a[188:189], v2 offset:29184\n"
    "v_mfma_f32_16x16x32_bf16 v[136:139], a[160:163], a[200:203], v[136:139]\n"
    "ds_read_b64_tr_b16 a[190:191], v2 offset:31232\n"
    "v_mfma_f32_16x16x32_bf16 v[136:139], a[164:167], a[216:219], v[136:139]\n"
    "buffer_store_dwordx4 v[68:71], v6, s[48:51], s67 offen sc1\n"
    "v_mfma_f32_16x16x32_bf16 v[136:139], a[168:171], a[232:235], v[136:139]\n"
    "ds_read_b128 a[40:43], v31 offset:33792\n"
    "v_mfma_f32_16x16x32_bf16 v[136:139], a[172:175], a[248:251], v[136:139]\n"
    "ds_read_b128 a[44:47], v31 offset:34304\n"
    "v_mfma_f32_16x16x32_bf16 v[140:143], a[172:175], a[252:255], v[140:143]\n"
    "ds_read_b128 a[48:51], v31 offset:34816\n"
    "v_mfma_f32_16x16x32_bf16 v[140:143], a[168:171], a[236:239], v[140:143]\n"
    "ds_read_b128 a[52:55], v31 offset:35328\n"
    "v_mfma_f32_16x16x32_bf16 v[140:143], a[164:167], a[220:223], v[140:143]\n"
    "ds_read_b128 a[56:59], v31 offset:35840\n"
    "v_mfma_f32_16x16x32_bf16 v[140:143], a[160:163], a[204:207], v[140:143]\n"
    "s_waitcnt vmcnt(14) lgkmcnt(5)\n"
    "v_mfma_f32_16x16x32_bf16 v[156:159], a[176:179], a[204:207], v[156:159]\n"
    "s_barrier\n"
    "v_mfma_f32_16x16x32_bf16 v[156:159], a[180:183], a[220:223], v[156:159]\n"
    "ds_read_b64_tr_b16 a[160:161], v1 offset:32768\n"
    "v_mfma_f32_16x16x32_bf16 v[156:159], a[184:187], a[236:239], v[156:159]\n"
    "ds_read_b64_tr_b16 a[162:163], v1 offset:34816\n"
    "v_mfma_f32_16x16x32_bf16 v[156:159], a[188:191], a[252:255], v[156:159]\n"
    "ds_read_b64_tr_b16 a[164:165], v1 offset:36864\n"
    "v_mfma_f32_16x16x32_bf16 v[152:155], a[188:191], a[248:251], v[152:155]\n"
    "ds_read_b64_tr_b16 a[166:167], v1 offset:38912\n"
    "v_mfma_f32_16x16x32_bf16 v[152:155], a[184:187], a[232:235], v[152:155]\n"
    "ds_read_b64_tr_b16 a[168:169], v1 offset:40960\n"
    "v_mfma_f32_16x16x32_bf16 v[152:155], a[180:183], a[216:219], v[152:155]\n"
    "ds_read_b64_tr_b16 a[170:171], v1 offset:43008\n"
    "v_mfma_f32_16x16x32_bf16 v[152:155], a[176:179], a[200:203], v[152:155]\n"
    "ds_read_b64_tr_b16 a[172:173], v1 offset:45056\n"
    "v_mfma_f32_16x16x32_bf16 v[148:151], a[176:179], a[196:199], v[148:151]\n"
    "ds_read_b64_tr_b16 a[174:175], v1 offset:47104\n"
    "v_mfma_f32_16x16x32_bf16 v[148:151], a[180:183], a[212:215], v[148:151]\n"
    "buffer_load_dwordx4 v3, s[36:39], s57 offen offset:0 lds\n"
    "v_mfma_f32_16x16x32_bf16 v[148:151], a[184:187], a[228:231], v[148:151]\n"
    "ds_read_b128 a[60:63], v31 offset:36352\n"
    "v_mfma_f32_16x16x32_bf16 v[148:151], a[188:191], a[244:247], v[148:151]\n"
    "s_xor_b32 s66, s66, 0x80\n"
    "s_xor_b32 s67, s67, 0x80\n"
    "v_mfma_f32_16x16x32_bf16 v[144:147], a[188:191], a[240:243], v[144:147]\n"
    "s_add_u32 m0, s56, 0x4380\n"
    "v_permlane16_swap_b32 v44, v46\n"
    "v_mfma_f32_16x16x32_bf16 v[144:147], a[184:187], a[224:227], v[144:147]\n"
    "buffer_load_dwordx4 v3, s[36:39], s57 offen offset:128 lds\n"
    "v_mfma_f32_16x16x32_bf16 v[144:147], a[180:183], a[208:211], v[144:147]\n"
    "s_add_u32 m0, s56, 0x4700\n"
    "v_permlane16_swap_b32 v76, v78\n"
    "v_mfma_f32_16x16x32_bf16 v[144:147], a[176:179], a[192:195], v[144:147]\n"
    "s_waitcnt vmcnt(10) lgkmcnt(1)\n"
    "v_mfma_f32_16x16x32_bf16 v[96:99], a[160:163], a[128:131], v[96:99]\n"
    "s_barrier\n"
    "v_mfma_f32_16x16x32_bf16 v[96:99], a[164:167], a[136:139], v[96:99]\n"
    "ds_read_b64_tr_b16 a[176:177], v1 offset:49152\n"
    "v_mfma_f32_16x16x32_bf16 v[96:99], a[168:171], a[144:147], v[96:99]\n"
    "ds_read_b64_tr_b16 a[178:179], v1 offset:51200\n"
    "v_mov_b32 v60, v44\n"
    "v_mfma_f32_16x16x32_bf16 v[96:99], a[172:175], a[152:155], v[96:99]\n"
    "ds_read_b64_tr_b16 a[180:181], v1 offset:53248\n"
    "v_mov_b32 v62, v46\n"
    "v_mfma_f32_16x16x32_bf16 v[100:103], a[172:175], v[184:187], v[100:103]\n"
    "ds_read_b64_tr_b16 a[182:183], v1 offset:55296\n"
    "v_mov_b32_dpp v44, v76 row_ror:8 row_mask:0xf bank_mask:0xc\n"
    "v_mfma_f32_16x16x32_bf16 v[100:103], a[168:171], v[176:179], v[100:103]\n"
    "ds_read_b64_tr_b16 a[184:185], v1 offset:57344\n"
    "v_mov_b32_dpp v46, v78 row_ror:8 row_mask:0xf bank_mask:0xc\n"
    "v_mfma_f32_16x16x32_bf16 v[100:103], a[164:167], v[168:171], v[100:103]\n"
    "ds_read_b64_tr_b16 a[186:187], v1 offset:59392\n"
    "v_mov_b32_dpp v76, v60 row_ror:8 row_mask:0xf bank_mask:0x3\n"
    "v_mfma_f32_16x16x32_bf16 v[100:103], a[160:163], v[160:163], v[100:103]\n"
    "ds_read_b64_tr_b16 a[188:189], v1 offset:61440\n"
    "v_mov_b32_dpp v78, v62 row_ror:8 row_mask:0xf bank_mask:0x3\n"
    "v_mfma_f32_16x16x32_bf16 v[104:107], a[160:163], a[132:135], v[104:107]\n"
    "ds_read_b64_tr_b16 a[190:191], v1 offset:63488\n"
    "v_mfma_f32_16x16x32_bf16 v[104:107], a[164:167], a[140:143], v[104:107]\n"
    "buffer_load_dwordx4 v3, s[36:39], s57 offen offset:256 lds\n"
    "v_mfma_f32_16x16x32_bf16 v[104:107], a[168:171], a[148:151], v[104:107]\n"
    "ds_read_b64_tr_b16 a[208:209], v1 offset:33280\n"
    "v_mfma_f32_16x16x32_bf16 v[104:107], a[172:175], a[156:159], v[104:107]\n"
    "ds_read_b64_tr_b16 a[210:211], v1 offset:35328\n"
    "v_mfma_f32_16x16x32_bf16 v[108:111], a[172:175], v[188:191], v[108:111]\n"
    "s_add_u32 m0, s56, 0x4a80\n"
    "v_permlane16_swap_b32 v45, v47\n"
    "v_mfma_f32_16x16x32_bf16 v[108:111], a[168:171], v[180:183], v[108:111]\n"
    "buffer_load_dwordx4 v3, s[36:39], s57 offen offset:384 lds\n"
    "v_mfma_f32_16x16x32_bf16 v[108:111], a[164:167], v[172:175], v[108:111]\n"
    "ds_read_b64_tr_b16 a[212:213], v1 offset:37376\n"
    "v_mov_b32 v61, v45\n"
    "v_mfma_f32_16x16x32_bf16 v[108:111], a[160:163], v[164:167], v[108:111]\n"
    "s_waitcnt lgkmcnt(3)\n"
    "v_permlane16_swap_b32 v77, v79\n"
    "v_mfma_f32_16x16x32_bf16 v[96:99], a[176:179], v[192:195], v[96:99]\n"
    "ds_read_b64_tr_b16 a[214:215], v1 offset:39424\n"
    "v_mov_b32 v63, v47\n"
    "v_mfma_f32_16x16x32_bf16 v[96:99], a[180:183], v[208:211], v[96:99]\n"
    "ds_read_b64_tr_b16 a[160:161], v2 offset:32768\n"
    "v_mov_b32_dpp v45, v77 row_ror:8 row_mask:0xf bank_mask:0xc\n"
    "v_mfma_f32_16x16x32_bf16 v[96:99], a[184:187], v[224:227], v[96:99]\n"
    "ds_read_b64_tr_b16 a[162:163], v2 offset:34816\n"
    "v_mov_b32_dpp v47, v79 row_ror:8 row_mask:0xf bank_mask:0xc\n"
    "v_mfma_f32_16x16x32_bf16 v[96:99], a[188:191], v[240:243], v[96:99]\n"
    "ds_read_b64_tr_b16 a[164:165], v2 offset:36864\n"
    "v_mov_b32_dpp v77, v61 row_ror:8 row_mask:0xf bank_mask:0x3\n"
    "v_mfma_f32_16x16x32_bf16 v[100:103], a[188:191], v[244:247], v[100:103]\n"
    "ds_read_b64_tr_b16 a[166:167], v2 offset:38912\n"
    "v_mov_b32_dpp v79, v63 row_ror:8 row_mask:0xf bank_mask:0x3\n"
    "v_mfma_f32_16x16x32_bf16 v[100:103], a[184:187], v[228:231], v[100:103]\n"
    "ds_read_b64_tr_b16 a[168:169], v2 offset:40960\n"
    "v_mfma_f32_16x16x32_bf16 v[100:103], a[180:183], v[212:215], v[100:103]\n"
    "ds_read_b64_tr_b16 a[170:171], v2 offset:43008\n"
    "v_mul_f32 v96, v8, v96\n"
    "v_mfma_f32_16x16x32_bf16 v[100:103], a[176:179], v[196:199], v[100:103]\n"
    "ds_read_b64_tr_b16 a[172:173], v2 offset:45056\n"
    "v_mul_f32 v97, v8, v97\n"
    "v_mfma_f32_16x16x32_bf16 v[104:107], a[176:179], v[200:203], v[104:107]\n"
    "ds_read_b64_tr_b16 a[174:175], v2 offset:47104\n"
    "v_mul_f32 v98, v8, v98\n"
    "v_mfma_f32_16x16x32_bf16 v[104:107], a[180:183], v[216:219], v[104:107]\n"
    "buffer_store_dwordx4 v[40:43], v6, s[48:51], s68 offen sc1\n"
    "v_mfma_f32_16x16x32_bf16 v[104:107], a[184:187], v[232:235], v[104:107]\n"
    "ds_read_b64_tr_b16 a[216:217], v1 offset:41472\n"
    "v_mul_f32 v99, v8, v99\n"
    "v_mfma_f32_16x16x32_bf16 v[104:107], a[188:191], v[248:251], v[104:107]\n"
    "ds_read_b64_tr_b16 a[218:219], v1 offset:43520\n"
    "v_cvt_pk_bf16_f32 v96, v96, v97\n"
    "v_mfma_f32_16x16x32_bf16 v[108:111], a[188:191], v[252:255], v[108:111]\n"
    "ds_read_b64_tr_b16 a[220:221], v1 offset:45568\n"
    "v_cvt_pk_bf16_f32 v97, v98, v99\n"
    "v_mfma_f32_16x16x32_bf16 v[108:111], a[184:187], v[236:239], v[108:111]\n"
    "ds_read_b64_tr_b16 a[222:223], v1 offset:47616\n"
    "v_mul_f32 v100, v9, v100\n"
    "v_mfma_f32_16x16x32_bf16 v[108:111], a[180:183], v[220:223], v[108:111]\n"
    "ds_read_b64_tr_b16 a[224:225], v1 offset:49664\n"
    "v_mul_f32 v101, v9, v101\n"
    "v_mfma_f32_16x16x32_bf16 v[108:111], a[176:179], v[204:207], v[108:111]\n"
    "s_waitcnt lgkmcnt(5)\n"
    "v_mul_f32 v102, v9, v102\n"
    "v_mul_f32 v103, v9, v103\n"
    "v_mfma_f32_16x16x32_bf16 v[112:115], a[160:163], a[128:131], v[112:115]\n"
    "ds_read_b64_tr_b16 a[226:227], v1 offset:51712\n"
    "v_cvt_pk_bf16_f32 v100, v100, v101\n"
    "v_mfma_f32_16x16x32_bf16 v[112:115], a[164:167], a[136:139], v[112:115]\n"
    "ds_read_b64_tr_b16 a[176:177], v2 offset:49152\n"
    "v_cvt_pk_bf16_f32 v101, v102, v103\n"
    "v_mfma_f32_16x16x32_bf16 v[112:115], a[168:171], a[144:147], v[112:115]\n"
    "ds_read_b64_tr_b16 a[178:179], v2 offset:51200\n"
    "v_mul_f32 v104, v10, v104\n"
    "v_mfma_f32_16x16x32_bf16 v[112:115], a[172:175], a[152:155], v[112:115]\n"
    "ds_read_b64_tr_b16 a[180:181], v2 offset:53248\n"
    "v_mul_f32 v105, v10, v105\n"
    "v_mfma_f32_16x16x32_bf16 v[116:119], a[172:175], v[184:187], v[116:119]\n"
    "ds_read_b64_tr_b16 a[182:183], v2 offset:55296\n"
    "v_mul_f32 v106, v10, v106\n"
    "v_mfma_f32_16x16x32_bf16 v[116:119], a[168:171], v[176:179], v[116:119]\n"
    "ds_read_b64_tr_b16 a[184:185], v2 offset:57344\n"
    "v_mul_f32 v107, v10, v107\n"
    "v_mfma_f32_16x16x32_bf16 v[116:119], a[164:167], v[168:171], v[116:119]\n"
    "ds_read_b64_tr_b16 a[186:187], v2 offset:59392\n"
    "v_cvt_pk_bf16_f32 v104, v104, v105\n"
    "v_mfma_f32_16x16x32_bf16 v[116:119], a[160:163], v[160:163], v[116:119]\n"
    "ds_read_b64_tr_b16 a[188:189], v2 offset:61440\n"
    "v_cvt_pk_bf16_f32 v105, v106, v107\n"
    "v_mfma_f32_16x16x32_bf16 v[120:123], a[160:163], a[132:135], v[120:123]\n"
    "ds_read_b64_tr_b16 a[190:191], v2 offset:63488\n"
    "v_mul_f32 v108, v11, v108\n"
    "v_mfma_f32_16x16x32_bf16 v[120:123], a[164:167], a[140:143], v[120:123]\n"
    "buffer_store_dwordx4 v[72:75], v6, s[48:51], s69 offen sc1\n"
    "v_mfma_f32_16x16x32_bf16 v[120:123], a[168:171], a[148:151], v[120:123]\n"
    "ds_read_b64_tr_b16 a[228:229], v1 offset:53760\n"
    "v_mul_f32 v109, v11, v109\n"
    "v_mfma_f32_16x16x32_bf16 v[120:123], a[172:175], a[156:159], v[120:123]\n"
    "ds_read_b64_tr_b16 a[230:231], v1 offset:55808\n"
    "v_mul_f32 v110, v11, v110\n"
    "v_mfma_f32_16x16x32_bf16 v[124:127], a[172:175], v[188:191], v[124:127]\n"
    "ds_read_b64_tr_b16 a[232:233], v1 offset:57856\n"
    "v_mul_f32 v111, v11, v111\n"
    "v_mfma_f32_16x16x32_bf16 v[124:127], a[168:171], v[180:183], v[124:127]\n"
    "ds_read_b64_tr_b16 a[234:235], v1 offset:59904\n"
    "v_cvt_pk_bf16_f32 v108, v108, v109\n"
    "v_mfma_f32_16x16x32_bf16 v[124:127], a[164:167], v[172:175], v[124:127]\n"
    "ds_read_b64_tr_b16 a[236:237], v1 offset:61952\n"
    "v_cvt_pk_bf16_f32 v109, v110, v111\n"
    "v_mfma_f32_16x16x32_bf16 v[124:127], a[160:163], v[164:167], v[124:127]\n"
    "s_waitcnt lgkmcnt(5)\n"
    "v_mfma_f32_16x16x32_bf16 v[112:115], a[176:179], v[192:195], v[112:115]\n"
    "ds_read_b64_tr_b16 a[238:239], v1 offset:64000\n"
    "v_mfma_f32_16x16x32_bf16 v[112:115], a[180:183], v[208:211], v[112:115]\n"
    "ds_read_b64_tr_b16 a[240:241], v2 offset:33280\n"
    "v_mfma_f32_16x16x32_bf16 v[112:115], a[184:187], v[224:227], v[112:115]\n"
    "ds_read_b64_tr_b16 a[242:243], v2 offset:35328\n"
    "v_mfma_f32_16x16x32_bf16 v[112:115], a[188:191], v[240:243], v[112:115]\n"
    "ds_read_b64_tr_b16 a[244:245], v2 offset:37376\n"
    "v_mfma_f32_16x16x32_bf16 v[116:119], a[176:179], v[196:199], v[116:119]\n"
    "ds_read_b64_tr_b16 a[246:247], v2 offset:39424\n"
    "v_mfma_f32_16x16x32_bf16 v[116:119], a[180:183], v[212:215], v[116:119]\n"
    "ds_read_b64_tr_b16 a[248:249], v2 offset:41472\n"
    "v_mfma_f32_16x16x32_bf16 v[116:119], a[184:187], v[228:231], v[116:119]\n"
    "ds_read_b64_tr_b16 a[250:251], v2 offset:43520\n"
    "s_nop 0\n"
    "v_mul_f32 v112, v8, v112\n"
    "v_mfma_f32_16x16x32_bf16 v[116:119], a[188:191], v[244:247], v[116:119]\n"
    "ds_read_b64_tr_b16 a[252:253], v2 offset:45568\n"
    "v_mul_f32 v113, v8, v113\n"
    "v_mfma_f32_16x16x32_bf16 v[120:123], a[176:179], v[200:203], v[120:123]\n"
    "ds_read_b64_tr_b16 a[254:255], v2 offset:47616\n"
    "v_mul_f32 v114, v8, v114\n"
    "v_mfma_f32_16x16x32_bf16 v[120:123], a[180:183], v[216:219], v[120:123]\n"
    "s_xor_b32 s68, s68, 0x80\n"
    "v_mul_f32 v115, v8, v115\n"
    "v_cvt_pk_bf16_f32 v98, v112, v113\n"
    "v_mfma_f32_16x16x32_bf16 v[120:123], a[184:187], v[232:235], v[120:123]\n"
    "s_xor_b32 s69, s69, 0x80\n"
    "v_cvt_pk_bf16_f32 v99, v114, v115\n"
    "v_mul_f32 v116, v9, v116\n"
    "v_mfma_f32_16x16x32_bf16 v[120:123], a[188:191], v[248:251], v[120:123]\n"
    "v_permlane16_swap_b32 v96, v98\n"
    "v_mfma_f32_16x16x32_bf16 v[124:127], a[176:179], v[204:207], v[124:127]\n"
    "v_permlane16_swap_b32 v97, v99\n"
    "v_mfma_f32_16x16x32_bf16 v[124:127], a[180:183], v[220:223], v[124:127]\n"
    "ds_read_b64_tr_b16 a[176:177], v2 offset:49664\n"
    "v_mov_b32 v112, v96\n"
    "v_mfma_f32_16x16x32_bf16 v[124:127], a[184:187], v[236:239], v[124:127]\n"
    "ds_read_b64_tr_b16 a[178:179], v2 offset:51712\n"
    "v_mov_b32 v113, v97\n"
    "v_mfma_f32_16x16x32_bf16 v[124:127], a[188:191], v[252:255], v[124:127]\n"
    "s_waitcnt lgkmcnt(15)\n"
    "v_mov_b32 v114, v98\n"
    "v_mov_b32 v115, v99\n"
    "v_mfma_f32_16x16x32_bf16 v[128:131], a[208:211], a[128:131], v[128:131]\n"
    "ds_read_b64_tr_b16 a[180:181], v2 offset:53760\n"
    "v_mul_f32 v117, v9, v117\n"
    "v_mfma_f32_16x16x32_bf16 v[128:131], a[212:215], a[136:139], v[128:131]\n"
    "ds_read_b64_tr_b16 a[182:183], v2 offset:55808\n"
    "v_mul_f32 v118, v9, v118\n"
    "v_mfma_f32_16x16x32_bf16 v[128:131], a[216:219], a[144:147], v[128:131]\n"
    "ds_read_b64_tr_b16 a[184:185], v2 offset:57856\n"
    "v_mul_f32 v119, v9, v119\n"
    "v_mfma_f32_16x16x32_bf16 v[128:131], a[220:223], a[152:155], v[128:131]\n"
    "ds_read_b64_tr_b16 a[186:187], v2 offset:59904\n"
    "s_waitcnt lgkmcnt(14)\n"
    "v_mfma_f32_16x16x32_bf16 v[128:131], a[224:227], v[192:195], v[128:131]\n"
    "ds_read_b64_tr_b16 a[188:189], v2 offset:61952\n"
    "v_cvt_pk_bf16_f32 v102, v116, v117\n"
    "v_mfma_f32_16x16x32_bf16 v[128:131], a[228:231], v[208:211], v[128:131]\n"
    "ds_read_b64_tr_b16 a[190:191], v2 offset:64000\n"
    "v_cvt_pk_bf16_f32 v103, v118, v119\n"
    "v_mfma_f32_16x16x32_bf16 v[128:131], a[232:235], v[224:227], v[128:131]\n"
    "v_permlane16_swap_b32 v100, v102\n"
    "v_mfma_f32_16x16x32_bf16 v[128:131], a[236:239], v[240:243], v[128:131]\n"
    "s_waitcnt lgkmcnt(8)\n"
    "v_permlane16_swap_b32 v101, v103\n"
    "v_mfma_f32_16x16x32_bf16 v[144:147], a[240:243], a[128:131], v[144:147]\n"
    "v_mov_b32 v116, v100\n"
    "v_mov_b32 v117, v101\n"
    "v_mfma_f32_16x16x32_bf16 v[144:147], a[244:247], a[136:139], v[144:147]\n"
    "v_mov_b32 v118, v102\n"
    "v_mov_b32 v119, v103\n"
    "v_mfma_f32_16x16x32_bf16 v[144:147], a[248:251], a[144:147], v[144:147]\n"
    "v_mul_f32 v128, v8, v128\n"
    "v_mul_f32 v129, v8, v129\n"
    "v_mfma_f32_16x16x32_bf16 v[144:147], a[252:255], a[152:155], v[144:147]\n"
    "s_waitcnt lgkmcnt(0)\n"
    "v_mul_f32 v130, v8, v130\n"
    "v_mul_f32 v131, v8, v131\n"
    "v_mfma_f32_16x16x32_bf16 v[144:147], a[176:179], v[192:195], v[144:147]\n"
    "buffer_store_dwordx4 v[44:47], v6, s[48:51], s70 offen sc1\n"
    "v_mfma_f32_16x16x32_bf16 v[144:147], a[180:183], v[208:211], v[144:147]\n"
    "s_xor_b32 s70, s70, 0x80\n"
    "v_cvt_pk_bf16_f32 v128, v128, v129\n"
    "v_mul_f32 v120, v10, v120\n"
    "v_mfma_f32_16x16x32_bf16 v[144:147], a[184:187], v[224:227], v[144:147]\n"
    "s_add_u32 s57, s53, 0x8000\n"
    "v_cvt_pk_bf16_f32 v129, v130, v131\n"
    "v_mul_f32 v121, v10, v121\n"
    "v_mfma_f32_16x16x32_bf16 v[144:147], a[188:191], v[240:243], v[144:147]\n"
    "s_waitcnt vmcnt(9) lgkmcnt(0)\n"
    "v_mfma_f32_16x16x32_bf16 v[132:135], a[208:211], v[160:163], v[132:135]\n"
    "s_barrier\n"
    "v_mfma_f32_16x16x32_bf16 v[132:135], a[212:215], v[168:171], v[132:135]\n"
    "s_add_u32 m0, s56, 0x8000\n"
    "v_mfma_f32_16x16x32_bf16 v[132:135], a[216:219], v[176:179], v[132:135]\n"
    "buffer_load_dwordx4 v3, s[36:39], s57 offen offset:0 lds\n"
    "v_mfma_f32_16x16x32_bf16 v[132:135], a[220:223], v[184:187], v[132:135]\n"
    "v_mul_f32 v144, v8, v144\n"
    "v_mul_f32 v145, v8, v145\n"
    "v_mfma_f32_16x16x32_bf16 v[132:135], a[224:227], v[196:199], v[132:135]\n"
    "v_mul_f32 v146, v8, v146\n"
    "v_mul_f32 v147, v8, v147\n"
    "v_mfma_f32_16x16x32_bf16 v[132:135], a[228:231], v[212:215], v[132:135]\n"
    "v_cvt_pk_bf16_f32 v130, v144, v145\n"
    "v_cvt_pk_bf16_f32 v131, v146, v147\n"
    "v_mfma_f32_16x16x32_bf16 v[132:135], a[232:235], v[228:231], v[132:135]\n"
    "v_permlane16_swap_b32 v128, v130\n"
    "v_mfma_f32_16x16x32_bf16 v[132:135], a[236:239], v[244:247], v[132:135]\n"
    "s_add_u32 m0, s56, 0x8380\n"
    "v_permlane16_swap_b32 v129, v131\n"
    "v_mfma_f32_16x16x32_bf16 v[148:151], a[240:243], v[160:163], v[148:151]\n"
    "buffer_load_dwordx4 v3, s[36:39], s57 offen offset:128 lds\n"
    "v_mfma_f32_16x16x32_bf16 v[148:151], a[244:247], v[168:171], v[148:151]\n"
    "v_mov_b32_dpp v96, v128 row_ror:8 row_mask:0xf bank_mask:0xc\n"
    "v_mov_b32_dpp v97, v129 row_ror:8 row_mask:0xf bank_mask:0xc\n"
    "v_mfma_f32_16x16x32_bf16 v[148:151], a[248:251], v[176:179], v[148:151]\n"
    "v_mov_b32_dpp v98, v130 row_ror:8 row_mask:0xf bank_mask:0xc\n"
    "v_mov_b32_dpp v99, v131 row_ror:8 row_mask:0xf bank_mask:0xc\n"
    "v_mfma_f32_16x16x32_bf16 v[148:151], a[252:255], v[184:187], v[148:151]\n"
    "v_mov_b32_dpp v128, v112 row_ror:8 row_mask:0xf bank_mask:0x3\n"
    "v_mov_b32_dpp v129, v113 row_ror:8 row_mask:0xf bank_mask:0x3\n"
    "v_mfma_f32_16x16x32_bf16 v[148:151], a[176:179], v[196:199], v[148:151]\n"
    "v_mov_b32_dpp v130, v114 row_ror:8 row_mask:0xf bank_mask:0x3\n"
    "v_mov_b32_dpp v131, v115 row_ror:8 row_mask:0xf bank_mask:0x3\n"
    "v_mfma_f32_16x16x32_bf16 v[148:151], a[180:183], v[212:215], v[148:151]\n"
    "s_add_u32 m0, s56, 0x8700\n"
    "v_mul_f32 v132, v9, v132\n"
    "v_mul_f32 v133, v9, v133\n"
    "v_mfma_f32_16x16x32_bf16 v[148:151], a[184:187], v[228:231], v[148:151]\n"
    "buffer_load_dwordx4 v3, s[36:39], s57 offen offset:256 lds\n"
    "v_mfma_f32_16x16x32_bf16 v[148:151], a[188:191], v[244:247], v[148:151]\n"
    "v_mul_f32 v134, v9, v134\n"
    "v_mul_f32 v135, v9, v135\n"
    "v_mfma_f32_16x16x32_bf16 v[136:139], a[208:211], a[132:135], v[136:139]\n"
    "v_cvt_pk_bf16_f32 v132, v132, v133\n"
    "v_mul_f32 v122, v10, v122\n"
    "v_mfma_f32_16x16x32_bf16 v[136:139], a[212:215], a[140:143], v[136:139]\n"
    "ds_read_b128 a[192:195], v0 offset:0\n"
    "v_cvt_pk_bf16_f32 v133, v134, v135\n"
    "v_mfma_f32_16x16x32_bf16 v[136:139], a[216:219], a[148:151], v[136:139]\n"
    "v_mul_f32 v148, v9, v148\n"
    "v_mul_f32 v149, v9, v149\n"
    "v_mfma_f32_16x16x32_bf16 v[136:139], a[220:223], a[156:159], v[136:139]\n"
    "s_add_u32 m0, s56, 0x8a80\n"
    "v_mul_f32 v150, v9, v150\n"
    "v_mul_f32 v151, v9, v151\n"
    "v_mfma_f32_16x16x32_bf16 v[136:139], a[224:227], v[200:203], v[136:139]\n"
    "ds_read_b128 a[196:199], v0 offset:512\n"
    "v_cvt_pk_bf16_f32 v134, v148, v149\n"
    "v_mfma_f32_16x16x32_bf16 v[136:139], a[228:231], v[216:219], v[136:139]\n"
    "buffer_load_dwordx4 v3, s[36:39], s57 offen offset:384 lds\n"
    "v_mfma_f32_16x16x32_bf16 v[136:139], a[232:235], v[232:235], v[136:139]\n"
    "v_cvt_pk_bf16_f32 v135, v150, v151\n"
    "v_mul_f32 v123, v10, v123\n"
    "v_mfma_f32_16x16x32_bf16 v[136:139], a[236:239], v[248:251], v[136:139]\n"
    "ds_read_b128 a[200:203], v0 offset:1024\n"
    "v_cvt_pk_bf16_f32 v106, v120, v121\n"
    "v_mfma_f32_16x16x32_bf16 v[152:155], a[240:243], a[132:135], v[152:155]\n"
    "s_add_u32 s57, s53, 0xc000\n"
    "v_permlane16_swap_b32 v132, v134\n"
    "v_mfma_f32_16x16x32_bf16 v[152:155], a[244:247], a[140:143], v[152:155]\n"
    "s_add_u32 m0, s56, 0xc000\n"
    "v_permlane16_swap_b32 v133, v135\n"
    "v_mfma_f32_16x16x32_bf16 v[152:155], a[248:251], a[148:151], v[152:155]\n"
    "ds_read_b128 a[204:207], v0 offset:1536\n"
    "v_mov_b32_dpp v100, v132 row_ror:8 row_mask:0xf bank_mask:0xc\n"
    "v_mfma_f32_16x16x32_bf16 v[152:155], a[252:255], a[156:159], v[152:155]\n"
    "buffer_load_dwordx4 v3, s[36:39], s57 offen offset:0 lds\n"
    "v_mfma_f32_16x16x32_bf16 v[152:155], a[176:179], v[200:203], v[152:155]\n"
    "v_mov_b32_dpp v101, v133 row_ror:8 row_mask:0xf bank_mask:0xc\n"
    "v_mov_b32_dpp v102, v134 row_ror:8 row_mask:0xf bank_mask:0xc\n"
    "v_mfma_f32_16x16x32_bf16 v[152:155], a[180:183], v[216:219], v[152:155]\n"
    "ds_read_b128 a[160:163], v0 offset:2048\n"
    "v_mov_b32_dpp v103, v135 row_ror:8 row_mask:0xf bank_mask:0xc\n"
    "v_mfma_f32_16x16x32_bf16 v[152:155], a[184:187], v[232:235], v[152:155]\n"
    "v_mov_b32_dpp v132, v116 row_ror:8 row_mask:0xf bank_mask:0x3\n"
    "v_mov_b32_dpp v133, v117 row_ror:8 row_mask:0xf bank_mask:0x3\n"
    "v_mfma_f32_16x16x32_bf16 v[152:155], a[188:191], v[248:251], v[152:155]\n"
    "s_add_u32 m0, s56, 0xc380\n"
    "v_mov_b32_dpp v134, v118 row_ror:8 row_mask:0xf bank_mask:0x3\n"
    "v_mov_b32_dpp v135, v119 row_ror:8 row_mask:0xf bank_mask:0x3\n"
    "v_mfma_f32_16x16x32_bf16 v[140:143], a[208:211], v[164:167], v[140:143]\n"
    "ds_read_b128 a[164:167], v0 offset:2560\n"
    "v_mul_f32 v136, v10, v136\n"
    "v_mfma_f32_16x16x32_bf16 v[140:143], a[212:215], v[172:175], v[140:143]\n"
    "buffer_load_dwordx4 v3, s[36:39], s57 offen offset:128 lds\n"
    "v_mfma_f32_16x16x32_bf16 v[140:143], a[216:219], v[180:183], v[140:143]\n"
    "v_mul_f32 v137, v10, v137\n"
    "v_mul_f32 v138, v10, v138\n"
    "v_mfma_f32_16x16x32_bf16 v[140:143], a[220:223], v[188:191], v[140:143]\n"
    "ds_read_b128 a[168:171], v0 offset:3072\n"
    "v_mul_f32 v139, v10, v139\n"
    "v_mfma_f32_16x16x32_bf16 v[140:143], a[224:227], v[204:207], v[140:143]\n"
    "v_mul_f32 v152, v10, v152\n"
    "v_mul_f32 v153, v10, v153\n"
    "v_mfma_f32_16x16x32_bf16 v[140:143], a[228:231], v[220:223], v[140:143]\n"
    "buffer_store_dwordx4 v[76:79], v6, s[48:51], s71 offen sc1\n"
    "v_mfma_f32_16x16x32_bf16 v[140:143], a[232:235], v[236:239], v[140:143]\n"
    "ds_read_b128 a[172:175], v0 offset:3584\n"
    "v_mul_f32 v154, v10, v154\n"
    "v_mfma_f32_16x16x32_bf16 v[140:143], a[236:239], v[252:255], v[140:143]\n"
    "s_xor_b32 s71, s71, 0x80\n"
    "v_mul_f32 v155, v10, v155\n"
    "v_cvt_pk_bf16_f32 v136, v136, v137\n"
    "v_mfma_f32_16x16x32_bf16 v[156:159], a[240:243], v[164:167], v[156:159]\n"
    "v_cvt_pk_bf16_f32 v137, v138, v139\n"
    "v_cvt_pk_bf16_f32 v107, v122, v123\n"
    "v_mfma_f32_16x16x32_bf16 v[156:159], a[244:247], v[172:175], v[156:159]\n"
    "v_cvt_pk_bf16_f32 v138, v152, v153\n"
    "v_cvt_pk_bf16_f32 v139, v154, v155\n"
    "v_mfma_f32_16x16x32_bf16 v[156:159], a[248:251], v[180:183], v[156:159]\n"
    "L_pad13_%=:\n"
    "s_cbranch_execz L_ret3_%=\n"
    "v_permlane16_swap_b32 v104, v106\n"
    "v_mfma_f32_16x16x32_bf16 v[156:159], a[252:255], v[188:191], v[156:159]\n"
    "v_permlane16_swap_b32 v136, v138\n"
    "v_mfma_f32_16x16x32_bf16 v[156:159], a[176:179], v[204:207], v[156:159]\n"
    "v_permlane16_swap_b32 v105, v107\n"
    "v_mfma_f32_16x16x32_bf16 v[156:159], a[180:183], v[220:223], v[156:159]\n"
    "v_permlane16_swap_b32 v137, v139\n"
    "v_mfma_f32_16x16x32_bf16 v[156:159], a[184:187], v[236:239], v[156:159]\n"
    "v_mov_b32 v120, v104\n"
    "v_mov_b32 v121, v105\n"
    "v_mfma_f32_16x16x32_bf16 v[156:159], a[188:191], v[252:255], v[156:159]\n"
    "s_waitcnt vmcnt(63) lgkmcnt(0)\n"
    "v_mov_b32 v122, v106\n"
    "v_mov_b32 v123, v107\n"
    "s_sub_u32 s58, s58, 1\n"
    "s_branch L_blk_%=\n"
    "L_tail_%=:\n"
    "v_mfma_f32_16x16x32_bf16 v[96:99], a[160:163], a[192:195], v[96:99]\n"
    "L_pad14_%=:\n"
    "s_cbranch_execz L_ret3_%=\n"
    "s_waitcnt vmcnt(4)\n"
    "s_barrier\n"
    "v_mul_f32 v78, v11, v78\n"
    "v_mfma_f32_16x16x32_bf16 v[96:99], a[164:167], a[208:211], v[96:99]\n"
    "ds_read_b64_tr_b16 a[176:177], v2 offset:16384\n"
    "ds_read_b64_tr_b16 a[0:1], v1 offset:32768\n"
    "v_mfma_f32_16x16x32_bf16 v[96:99], a[168:171], a[224:227], v[96:99]\n"
    "ds_read_b64_tr_b16 a[178:179], v2 offset:18432\n"
    "ds_read_b64_tr_b16 a[2:3], v1 offset:34816\n"
    "v_mfma_f32_16x16x32_bf16 v[96:99], a[172:175], a[240:243], v[96:99]\n"
    "ds_read_b64_tr_b16 a[180:181], v2 offset:20480\n"
    "ds_read_b64_tr_b16 a[4:5], v1 offset:36864\n"
    "v_mfma_f32_16x16x32_bf16 v[100:103], a[172:175], a[244:247], v[100:103]\n"
    "ds_read_b64_tr_b16 a[182:183], v2 offset:22528\n"
    "ds_read_b64_tr_b16 a[6:7], v1 offset:38912\n"
    "v_mfma_f32_16x16x32_bf16 v[100:103], a[168:171], a[228:231], v[100:103]\n"
    "ds_read_b64_tr_b16 a[184:185], v2 offset:24576\n"
    "ds_read_b64_tr_b16 a[8:9], v1 offset:40960\n"
    "v_mfma_f32_16x16x32_bf16 v[100:103], a[164:167], a[212:215], v[100:103]\n"
    "ds_read_b64_tr_b16 a[186:187], v2 offset:26624\n"
    "ds_read_b64_tr_b16 a[10:11], v1 offset:43008\n"
    "v_mfma_f32_16x16x32_bf16 v[100:103], a[160:163], a[196:199], v[100:103]\n"
    "ds_read_b64_tr_b16 a[188:189], v2 offset:28672\n"
    "ds_read_b64_tr_b16 a[12:13], v1 offset:45056\n"
    "v_mfma_f32_16x16x32_bf16 v[104:107], a[160:163], a[200:203], v[104:107]\n"
    "ds_read_b64_tr_b16 a[190:191], v2 offset:30720\n"
    "ds_read_b64_tr_b16 a[14:15], v1 offset:47104\n"
    "v_mfma_f32_16x16x32_bf16 v[104:107], a[164:167], a[216:219], v[104:107]\n"
    "ds_read_b64_tr_b16 a[16:17], v2 offset:32768\n"
    "v_mul_f32 v79, v11, v79\n"
    "v_mfma_f32_16x16x32_bf16 v[104:107], a[168:171], a[232:235], v[104:107]\n"
    "buffer_store_dwordx4 v[36:39], v6, s[48:51], s66 offen nt sc1\n"
    "s_xor_b32 s66, s66, 0x80\n"
    "v_mfma_f32_16x16x32_bf16 v[104:107], a[172:175], a[248:251], v[104:107]\n"
    "ds_read_b64_tr_b16 a[18:19], v2 offset:34816\n"
    "v_mul_f32 v92, v11, v92\n"
    "v_mfma_f32_16x16x32_bf16 v[108:111], a[172:175], a[252:255], v[108:111]\n"
    "v_mul_f32 v93, v11, v93\n"
    "v_mul_f32 v94, v11, v94\n"
    "v_mfma_f32_16x16x32_bf16 v[108:111], a[168:171], a[236:239], v[108:111]\n"
    "ds_read_b64_tr_b16 a[20:21], v2 offset:36864\n"
    "v_mul_f32 v95, v11, v95\n"
    "v_mfma_f32_16x16x32_bf16 v[108:111], a[164:167], a[220:223], v[108:111]\n"
    "v_cvt_pk_bf16_f32 v44, v44, v45\n"
    "v_cvt_pk_bf16_f32 v76, v76, v77\n"
    "v_mfma_f32_16x16x32_bf16 v[108:111], a[160:163], a[204:207], v[108:111]\n"
    "s_waitcnt lgkmcnt(4)\n"
    "v_cvt_pk_bf16_f32 v45, v46, v47\n"
    "v_cvt_pk_bf16_f32 v77, v78, v79\n"
    "v_mfma_f32_16x16x32_bf16 v[124:127], a[176:179], a[204:207], v[124:127]\n"
    "v_cvt_pk_bf16_f32 v46, v60, v61\n"
    "v_cvt_pk_bf16_f32 v47, v62, v63\n"
    "v_mfma_f32_16x16x32_bf16 v[124:127], a[180:183], a[220:223], v[124:127]\n"
    "ds_read_b64_tr_b16 a[160:161], v1 offset:16896\n"
    "ds_read_b64_tr_b16 a[22:23], v2 offset:38912\n"
    "v_mfma_f32_16x16x32_bf16 v[124:127], a[184:187], a[236:239], v[124:127]\n"
    "ds_read_b64_tr_b16 a[162:163], v1 offset:18944\n"
    "ds_read_b64_tr_b16 a[24:25], v2 offset:40960\n"
    "v_mfma_f32_16x16x32_bf16 v[124:127], a[188:191], a[252:255], v[124:127]\n"
    "ds_read_b64_tr_b16 a[164:165], v1 offset:20992\n"
    "ds_read_b64_tr_b16 a[26:27], v2 offset:43008\n"
    "v_mfma_f32_16x16x32_bf16 v[120:123], a[188:191], a[248:251], v[120:123]\n"
    "ds_read_b64_tr_b16 a[166:167], v1 offset:23040\n"
    "ds_read_b64_tr_b16 a[28:29], v2 offset:45056\n"
    "v_mfma_f32_16x16x32_bf16 v[120:123], a[184:187], a[232:235], v[120:123]\n"
    "ds_read_b64_tr_b16 a[168:169], v1 offset:25088\n"
    "ds_read_b64_tr_b16 a[30:31], v2 offset:47104\n"
    "v_mfma_f32_16x16x32_bf16 v[120:123], a[180:183], a[216:219], v[120:123]\n"
    "ds_read_b64_tr_b16 a[170:171], v1 offset:27136\n"
    "ds_read_b64_tr_b16 a[32:33], v1 offset:33280\n"
    "v_mfma_f32_16x16x32_bf16 v[120:123], a[176:179], a[200:203], v[120:123]\n"
    "ds_read_b64_tr_b16 a[172:173], v1 offset:29184\n"
    "ds_read_b64_tr_b16 a[34:35], v1 offset:35328\n"
    "v_mfma_f32_16x16x32_bf16 v[116:119], a[176:179], a[196:199], v[116:119]\n"
    "ds_read_b64_tr_b16 a[174:175], v1 offset:31232\n"
    "ds_read_b64_tr_b16 a[36:37], v1 offset:37376\n"
    "v_mfma_f32_16x16x32_bf16 v[116:119], a[180:183], a[212:215], v[116:119]\n"
    "ds_read_b64_tr_b16 a[38:39], v1 offset:39424\n"
    "v_cvt_pk_bf16_f32 v78, v92, v93\n"
    "v_mfma_f32_16x16x32_bf16 v[116:119], a[184:187], a[228:231], v[116:119]\n"
    "buffer_store_dwordx4 v[68:71], v6, s[48:51], s67 offen nt sc1\n"
    "s_xor_b32 s67, s67, 0x80\n"
    "v_mfma_f32_16x16x32_bf16 v[116:119], a[188:191], a[244:247], v[116:119]\n"
    "ds_read_b64_tr_b16 a[40:41], v1 offset:41472\n"
    "v_cvt_pk_bf16_f32 v79, v94, v95\n"
    "v_mfma_f32_16x16x32_bf16 v[112:115], a[188:191], a[240:243], v[112:115]\n"
    "v_permlane16_swap_b32 v44, v46\n"
    "v_mfma_f32_16x16x32_bf16 v[112:115], a[184:187], a[224:227], v[112:115]\n"
    "ds_read_b64_tr_b16 a[42:43], v1 offset:43520\n"
    "v_mov_b32 v60, v44\n"
    "v_mfma_f32_16x16x32_bf16 v[112:115], a[180:183], a[208:211], v[112:115]\n"
    "v_permlane16_swap_b32 v76, v78\n"
    "v_mfma_f32_16x16x32_bf16 v[112:115], a[176:179], a[192:195], v[112:115]\n"
    "s_waitcnt lgkmcnt(4)\n"
    "v_permlane16_swap_b32 v45, v47\n"
    "v_mfma_f32_16x16x32_bf16 v[128:131], a[160:163], a[192:195], v[128:131]\n"
    "v_permlane16_swap_b32 v77, v79\n"
    "v_mfma_f32_16x16x32_bf16 v[128:131], a[164:167], a[208:211], v[128:131]\n"
    "ds_read_b64_tr_b16 a[176:177], v2 offset:16896\n"
    "ds_read_b64_tr_b16 a[44:45], v1 offset:45568\n"
    "v_mfma_f32_16x16x32_bf16 v[128:131], a[168:171], a[224:227], v[128:131]\n"
    "ds_read_b64_tr_b16 a[178:179], v2 offset:18944\n"
    "ds_read_b64_tr_b16 a[46:47], v1 offset:47616\n"
    "v_mfma_f32_16x16x32_bf16 v[128:131], a[172:175], a[240:243], v[128:131]\n"
    "ds_read_b64_tr_b16 a[180:181], v2 offset:20992\n"
    "ds_read_b64_tr_b16 a[48:49], v2 offset:33280\n"
    "v_mfma_f32_16x16x32_bf16 v[132:135], a[172:175], a[244:247], v[132:135]\n"
    "ds_read_b64_tr_b16 a[182:183], v2 offset:23040\n"
    "ds_read_b64_tr_b16 a[50:51], v2 offset:35328\n"
    "v_mfma_f32_16x16x32_bf16 v[132:135], a[168:171], a[228:231], v[132:135]\n"
    "ds_read_b64_tr_b16 a[184:185], v2 offset:25088\n"
    "ds_read_b64_tr_b16 a[52:53], v2 offset:37376\n"
    "v_mfma_f32_16x16x32_bf16 v[132:135], a[164:167], a[212:215], v[132:135]\n"
    "ds_read_b64_tr_b16 a[186:187], v2 offset:27136\n"
    "ds_read_b64_tr_b16 a[54:55], v2 offset:39424\n"
    "v_mfma_f32_16x16x32_bf16 v[132:135], a[160:163], a[196:199], v[132:135]\n"
    "ds_read_b64_tr_b16 a[188:189], v2 offset:29184\n"
    "ds_read_b64_tr_b16 a[56:57], v2 offset:41472\n"
    "v_mfma_f32_16x16x32_bf16 v[136:139], a[160:163], a[200:203], v[136:139]\n"
    "ds_read_b64_tr_b16 a[190:191], v2 offset:31232\n"
    "ds_read_b64_tr_b16 a[58:59], v2 offset:43520\n"
    "v_mfma_f32_16x16x32_bf16 v[136:139], a[164:167], a[216:219], v[136:139]\n"
    "ds_read_b64_tr_b16 a[60:61], v2 offset:45568\n"
    "v_mov_b32 v62, v46\n"
    "v_mfma_f32_16x16x32_bf16 v[136:139], a[168:171], a[232:235], v[136:139]\n"
    "buffer_store_dwordx4 v[40:43], v6, s[48:51], s68 offen nt sc1\n"
    "s_xor_b32 s68, s68, 0x80\n"
    "v_mfma_f32_16x16x32_bf16 v[136:139], a[172:175], a[248:251], v[136:139]\n"
    "ds_read_b64_tr_b16 a[62:63], v2 offset:47616\n"
    "v_mov_b32 v61, v45\n"
    "v_mfma_f32_16x16x32_bf16 v[140:143], a[172:175], a[252:255], v[140:143]\n"
    "v_mov_b32 v63, v47\n"
    "v_mov_b32_dpp v44, v76 row_ror:8 row_mask:0xf bank_mask:0xc\n"
    "v_mfma_f32_16x16x32_bf16 v[140:143], a[168:171], a[236:239], v[140:143]\n"
    "ds_read_b64_tr_b16 a[64:65], v1 offset:49152\n"
    "v_mov_b32_dpp v46, v78 row_ror:8 row_mask:0xf bank_mask:0xc\n"
    "v_mfma_f32_16x16x32_bf16 v[140:143], a[164:167], a[220:223], v[140:143]\n"
    "v_mov_b32_dpp v45, v77 row_ror:8 row_mask:0xf bank_mask:0xc\n"
    "v_mov_b32_dpp v47, v79 row_ror:8 row_mask:0xf bank_mask:0xc\n"
    "v_mfma_f32_16x16x32_bf16 v[140:143], a[160:163], a[204:207], v[140:143]\n"
    "s_waitcnt lgkmcnt(4)\n"
    "v_mov_b32_dpp v76, v60 row_ror:8 row_mask:0xf bank_mask:0x3\n"
    "v_mov_b32_dpp v78, v62 row_ror:8 row_mask:0xf bank_mask:0x3\n"
    "v_mfma_f32_16x16x32_bf16 v[156:159], a[176:179], a[204:207], v[156:159]\n"
    "ds_read_b64_tr_b16 a[66:67], v1 offset:51200\n"
    "v_mov_b32_dpp v77, v61 row_ror:8 row_mask:0xf bank_mask:0x3\n"
    "v_mfma_f32_16x16x32_bf16 v[156:159], a[180:183], a[220:223], v[156:159]\n"
    "ds_read_b64_tr_b16 a[68:69], v1 offset:53248\n"
    "v_mov_b32_dpp v79, v63 row_ror:8 row_mask:0xf bank_mask:0x3\n"
    "v_mfma_f32_16x16x32_bf16 v[156:159], a[184:187], a[236:239], v[156:159]\n"
    "ds_read_b64_tr_b16 a[70:71], v1 offset:55296\n"
    "v_mfma_f32_16x16x32_bf16 v[156:159], a[188:191], a[252:255], v[156:159]\n"
    "ds_read_b64_tr_b16 a[72:73], v1 offset:57344\n"
    "v_mfma_f32_16x16x32_bf16 v[152:155], a[188:191], a[248:251], v[152:155]\n"
    "ds_read_b64_tr_b16 a[74:75], v1 offset:59392\n"
    "v_mfma_f32_16x16x32_bf16 v[152:155], a[184:187], a[232:235], v[152:155]\n"
    "ds_read_b64_tr_b16 a[76:77], v1 offset:61440\n"
    "v_mfma_f32_16x16x32_bf16 v[152:155], a[180:183], a[216:219], v[152:155]\n"
    "buffer_store_dwordx4 v[72:75], v6, s[48:51], s69 offen nt sc1\n"
    "s_xor_b32 s69, s69, 0x80\n"
    "v_mfma_f32_16x16x32_bf16 v[152:155], a[176:179], a[200:203], v[152:155]\n"
    "ds_read_b64_tr_b16 a[78:79], v1 offset:63488\n"
    "v_mfma_f32_16x16x32_bf16 v[148:151], a[176:179], a[196:199], v[148:151]\n"
    "ds_read_b64_tr_b16 a[80:81], v2 offset:49152\n"
    "v_mfma_f32_16x16x32_bf16 v[148:151], a[180:183], a[212:215], v[148:151]\n"
    "ds_read_b64_tr_b16 a[82:83], v2 offset:51200\n"
    "v_mfma_f32_16x16x32_bf16 v[148:151], a[184:187], a[228:231], v[148:151]\n"
    "ds_read_b64_tr_b16 a[84:85], v2 offset:53248\n"
    "v_mfma_f32_16x16x32_bf16 v[148:151], a[188:191], a[244:247], v[148:151]\n"
    "ds_read_b64_tr_b16 a[86:87], v2 offset:55296\n"
    "v_mfma_f32_16x16x32_bf16 v[144:147], a[188:191], a[240:243], v[144:147]\n"
    "ds_read_b64_tr_b16 a[88:89], v2 offset:57344\n"
    "v_mfma_f32_16x16x32_bf16 v[144:147], a[184:187], a[224:227], v[144:147]\n"
    "ds_read_b64_tr_b16 a[90:91], v2 offset:59392\n"
    "v_mfma_f32_16x16x32_bf16 v[144:147], a[180:183], a[208:211], v[144:147]\n"
    "ds_read_b64_tr_b16 a[92:93], v2 offset:61440\n"
    "v_mfma_f32_16x16x32_bf16 v[144:147], a[176:179], a[192:195], v[144:147]\n"
    "s_waitcnt lgkmcnt(15)\n"
    "v_mfma_f32_16x16x32_bf16 v[96:99], a[0:3], a[128:131], v[96:99]\n"
    "ds_read_b64_tr_b16 a[94:95], v2 offset:63488\n"
    "v_mfma_f32_16x16x32_bf16 v[96:99], a[4:7], a[136:139], v[96:99]\n"
    "ds_read_b64_tr_b16 a[96:97], v1 offset:49664\n"
    "v_mfma_f32_16x16x32_bf16 v[96:99], a[8:11], a[144:147], v[96:99]\n"
    "ds_read_b64_tr_b16 a[98:99], v1 offset:51712\n"
    "v_mfma_f32_16x16x32_bf16 v[96:99], a[12:15], a[152:155], v[96:99]\n"
    "ds_read_b64_tr_b16 a[100:101], v1 offset:53760\n"
    "v_mfma_f32_16x16x32_bf16 v[112:115], a[16:19], a[128:131], v[112:115]\n"
    "buffer_store_dwordx4 v[44:47], v6, s[48:51], s70 offen nt sc1\n"
    "s_xor_b32 s70, s70, 0x80\n"
    "v_mfma_f32_16x16x32_bf16 v[112:115], a[20:23], a[136:139], v[112:115]\n"
    "ds_read_b64_tr_b16 a[102:103], v1 offset:55808\n"
    "v_mfma_f32_16x16x32_bf16 v[112:115], a[24:27], a[144:147], v[112:115]\n"
    "ds_read_b64_tr_b16 a[104:105], v1 offset:57856\n"
    "v_mfma_f32_16x16x32_bf16 v[112:115], a[28:31], a[152:155], v[112:115]\n"
    "ds_read_b64_tr_b16 a[106:107], v1 offset:59904\n"
    "v_mfma_f32_16x16x32_bf16 v[128:131], a[32:35], a[128:131], v[128:131]\n"
    "ds_read_b64_tr_b16 a[108:109], v1 offset:61952\n"
    "v_mfma_f32_16x16x32_bf16 v[128:131], a[36:39], a[136:139], v[128:131]\n"
    "ds_read_b64_tr_b16 a[110:111], v1 offset:64000\n"
    "v_mfma_f32_16x16x32_bf16 v[128:131], a[40:43], a[144:147], v[128:131]\n"
    "ds_read_b64_tr_b16 a[112:113], v2 offset:49664\n"
    "v_mfma_f32_16x16x32_bf16 v[128:131], a[44:47], a[152:155], v[128:131]\n"
    "ds_read_b64_tr_b16 a[114:115], v2 offset:51712\n"
    "v_mfma_f32_16x16x32_bf16 v[144:147], a[48:51], a[128:131], v[144:147]\n"
    "ds_read_b64_tr_b16 a[116:117], v2 offset:53760\n"
    "v_mfma_f32_16x16x32_bf16 v[144:147], a[52:55], a[136:139], v[144:147]\n"
    "ds_read_b64_tr_b16 a[118:119], v2 offset:55808\n"
    "v_mfma_f32_16x16x32_bf16 v[144:147], a[56:59], a[144:147], v[144:147]\n"
    "ds_read_b64_tr_b16 a[120:121], v2 offset:57856\n"
    "v_mfma_f32_16x16x32_bf16 v[144:147], a[60:63], a[152:155], v[144:147]\n"
    "s_waitcnt lgkmcnt(15)\n"
    "v_mfma_f32_16x16x32_bf16 v[96:99], a[64:67], v[192:195], v[96:99]\n"
    "ds_read_b64_tr_b16 a[122:123], v2 offset:59904\n"
    "v_mfma_f32_16x16x32_bf16 v[96:99], a[68:71], v[208:211], v[96:99]\n"
    "ds_read_b64_tr_b16 a[124:125], v2 offset:61952\n"
    "v_mfma_f32_16x16x32_bf16 v[96:99], a[72:75], v[224:227], v[96:99]\n"
    "ds_read_b64_tr_b16 a[126:127], v2 offset:64000\n"
    "v_mfma_f32_16x16x32_bf16 v[96:99], a[76:79], v[240:243], v[96:99]\n"
    "s_waitcnt lgkmcnt(15)\n"
    "v_mfma_f32_16x16x32_bf16 v[112:115], a[80:83], v[192:195], v[112:115]\n"
    "buffer_store_dwordx4 v[76:79], v6, s[48:51], s71 offen nt sc1\n"
    "s_xor_b32 s71, s71, 0x80\n"
    "v_mfma_f32_16x16x32_bf16 v[112:115], a[84:87], v[208:211], v[112:115]\n"
    "v_mfma_f32_16x16x32_bf16 v[112:115], a[88:91], v[224:227], v[112:115]\n"
    "v_mfma_f32_16x16x32_bf16 v[112:115], a[92:95], v[240:243], v[112:115]\n"
    "s_waitcnt lgkmcnt(8)\n"
    "v_mfma_f32_16x16x32_bf16 v[128:131], a[96:99], v[192:195], v[128:131]\n"
    "v_mfma_f32_16x16x32_bf16 v[128:131], a[100:103], v[208:211], v[128:131]\n"
    "v_mul_f32 v96, v8, v96\n"
    "v_mul_f32 v97, v8, v97\n"
    "v_mfma_f32_16x16x32_bf16 v[128:131], a[104:107], v[224:227], v[128:131]\n"
    "v_mul_f32 v98, v8, v98\n"
    "v_mul_f32 v99, v8, v99\n"
    "v_mfma_f32_16x16x32_bf16 v[128:131], a[108:111], v[240:243], v[128:131]\n"
    "s_waitcnt lgkmcnt(0)\n"
    "v_cvt_pk_bf16_f32 v96, v96, v97\n"
    "v_mfma_f32_16x16x32_bf16 v[144:147], a[112:115], v[192:195], v[144:147]\n"
    "v_cvt_pk_bf16_f32 v97, v98, v99\n"
    "v_mfma_f32_16x16x32_bf16 v[144:147], a[116:119], v[208:211], v[144:147]\n"
    "v_mul_f32 v112, v8, v112\n"
    "v_mul_f32 v113, v8, v113\n"
    "v_mfma_f32_16x16x32_bf16 v[144:147], a[120:123], v[224:227], v[144:147]\n"
    "v_mul_f32 v114, v8, v114\n"
    "v_mul_f32 v115, v8, v115\n"
    "v_mfma_f32_16x16x32_bf16 v[144:147], a[124:127], v[240:243], v[144:147]\n"
    "v_cvt_pk_bf16_f32 v98, v112, v113\n"
    "v_cvt_pk_bf16_f32 v99, v114, v115\n"
    "v_mfma_f32_16x16x32_bf16 v[100:103], a[0:3], v[160:163], v[100:103]\n"
    "v_permlane16_swap_b32 v96, v98\n"
    "v_mfma_f32_16x16x32_bf16 v[100:103], a[4:7], v[168:171], v[100:103]\n"
    "v_mul_f32 v128, v8, v128\n"
    "v_mul_f32 v129, v8, v129\n"
    "v_mfma_f32_16x16x32_bf16 v[100:103], a[8:11], v[176:179], v[100:103]\n"
    "v_mul_f32 v130, v8, v130\n"
    "v_mul_f32 v131, v8, v131\n"
    "v_mfma_f32_16x16x32_bf16 v[100:103], a[12:15], v[184:187], v[100:103]\n"
    "v_cvt_pk_bf16_f32 v128, v128, v129\n"
    "v_mov_b32 v112, v96\n"
    "v_mfma_f32_16x16x32_bf16 v[100:103], a[64:67], v[196:199], v[100:103]\n"
    "v_cvt_pk_bf16_f32 v129, v130, v131\n"
    "v_mov_b32 v114, v98\n"
    "v_mfma_f32_16x16x32_bf16 v[100:103], a[68:71], v[212:215], v[100:103]\n"
    "v_mul_f32 v144, v8, v144\n"
    "v_mul_f32 v145, v8, v145\n"
    "v_mfma_f32_16x16x32_bf16 v[100:103], a[72:75], v[228:231], v[100:103]\n"
    "v_mul_f32 v146, v8, v146\n"
    "v_mul_f32 v147, v8, v147\n"
    "v_mfma_f32_16x16x32_bf16 v[100:103], a[76:79], v[244:247], v[100:103]\n"
    "v_cvt_pk_bf16_f32 v130, v144, v145\n"
    "v_cvt_pk_bf16_f32 v131, v146, v147\n"
    "v_mfma_f32_16x16x32_bf16 v[116:119], a[16:19], v[160:163], v[116:119]\n"
    "v_permlane16_swap_b32 v128, v130\n"
    "v_mfma_f32_16x16x32_bf16 v[116:119], a[20:23], v[168:171], v[116:119]\n"
    "v_permlane16_swap_b32 v97, v99\n"
    "v_mfma_f32_16x16x32_bf16 v[116:119], a[24:27], v[176:179], v[116:119]\n"
    "v_permlane16_swap_b32 v129, v131\n"
    "v_mfma_f32_16x16x32_bf16 v[116:119], a[28:31], v[184:187], v[116:119]\n"
    "v_mov_b32 v113, v97\n"
    "v_mov_b32 v115, v99\n"
    "v_mfma_f32_16x16x32_bf16 v[116:119], a[80:83], v[196:199], v[116:119]\n"
    "v_mov_b32_dpp v96, v128 row_ror:8 row_mask:0xf bank_mask:0xc\n"
    "v_mov_b32_dpp v97, v129 row_ror:8 row_mask:0xf bank_mask:0xc\n"
    "v_mfma_f32_16x16x32_bf16 v[116:119], a[84:87], v[212:215], v[116:119]\n"
    "v_mov_b32_dpp v98, v130 row_ror:8 row_mask:0xf bank_mask:0xc\n"
    "v_mov_b32_dpp v99, v131 row_ror:8 row_mask:0xf bank_mask:0xc\n"
    "v_mfma_f32_16x16x32_bf16 v[116:119], a[88:91], v[228:231], v[116:119]\n"
    "buffer_store_dwordx4 v[96:99], v6, s[48:51], s64 offen nt sc1\n"
    "v_mfma_f32_16x16x32_bf16 v[116:119], a[92:95], v[244:247], v[116:119]\n"
    "v_mov_b32_dpp v128, v112 row_ror:8 row_mask:0xf bank_mask:0x3\n"
    "v_mov_b32_dpp v129, v113 row_ror:8 row_mask:0xf bank_mask:0x3\n"
    "v_mfma_f32_16x16x32_bf16 v[132:135], a[32:35], v[160:163], v[132:135]\n"
    "v_mov_b32_dpp v130, v114 row_ror:8 row_mask:0xf bank_mask:0x3\n"
    "v_mov_b32_dpp v131, v115 row_ror:8 row_mask:0xf bank_mask:0x3\n"
    "v_mfma_f32_16x16x32_bf16 v[132:135], a[36:39], v[168:171], v[132:135]\n"
    "v_mul_f32 v100, v9, v100\n"
    "v_mul_f32 v101, v9, v101\n"
    "v_mfma_f32_16x16x32_bf16 v[132:135], a[40:43], v[176:179], v[132:135]\n"
    "v_mul_f32 v102, v9, v102\n"
    "v_mul_f32 v103, v9, v103\n"
    "v_mfma_f32_16x16x32_bf16 v[132:135], a[44:47], v[184:187], v[132:135]\n"
    "v_cvt_pk_bf16_f32 v100, v100, v101\n"
    "v_mfma_f32_16x16x32_bf16 v[132:135], a[96:99], v[196:199], v[132:135]\n"
    "v_cvt_pk_bf16_f32 v101, v102, v103\n"
    "v_mfma_f32_16x16x32_bf16 v[132:135], a[100:103], v[212:215], v[132:135]\n"
    "v_mul_f32 v116, v9, v116\n"
    "v_mul_f32 v117, v9, v117\n"
    "v_mfma_f32_16x16x32_bf16 v[132:135], a[104:107], v[228:231], v[132:135]\n"
    "v_mul_f32 v118, v9, v118\n"
    "v_mul_f32 v119, v9, v119\n"
    "v_mfma_f32_16x16x32_bf16 v[132:135], a[108:111], v[244:247], v[132:135]\n"
    "v_cvt_pk_bf16_f32 v102, v116, v117\n"
    "v_cvt_pk_bf16_f32 v103, v118, v119\n"
    "v_mfma_f32_16x16x32_bf16 v[148:151], a[48:51], v[160:163], v[148:151]\n"
    "buffer_store_dwordx4 v[128:131], v6, s[48:51], s65 offen nt sc1\n"
    "v_mfma_f32_16x16x32_bf16 v[148:151], a[52:55], v[168:171], v[148:151]\n"
    "v_permlane16_swap_b32 v100, v102\n"
    "v_mfma_f32_16x16x32_bf16 v[148:151], a[56:59], v[176:179], v[148:151]\n"
    "v_permlane16_swap_b32 v101, v103\n"
    "v_mfma_f32_16x16x32_bf16 v[148:151], a[60:63], v[184:187], v[148:151]\n"
    "v_mov_b32 v116, v100\n"
    "v_mov_b32 v117, v101\n"
    "v_mfma_f32_16x16x32_bf16 v[148:151], a[112:115], v[196:199], v[148:151]\n"
    "v_mov_b32 v118, v102\n"
    "v_mov_b32 v119, v103\n"
    "v_mfma_f32_16x16x32_bf16 v[148:151], a[116:119], v[212:215], v[148:151]\n"
    "v_mul_f32 v132, v9, v132\n"
    "v_mul_f32 v133, v9, v133\n"
    "v_mfma_f32_16x16x32_bf16 v[148:151], a[120:123], v[228:231], v[148:151]\n"
    "v_mul_f32 v134, v9, v134\n"
    "v_mul_f32 v135, v9, v135\n"
    "v_mfma_f32_16x16x32_bf16 v[148:151], a[124:127], v[244:247], v[148:151]\n"
    "v_cvt_pk_bf16_f32 v132, v132, v133\n"
    "v_mfma_f32_16x16x32_bf16 v[104:107], a[0:3], a[132:135], v[104:107]\n"
    "v_cvt_pk_bf16_f32 v133, v134, v135\n"
    "v_mfma_f32_16x16x32_bf16 v[104:107], a[4:7], a[140:143], v[104:107]\n"
    "v_mfma_f32_16x16x32_bf16 v[104:107], a[8:11], a[148:151], v[104:107]\n"
    "v_mfma_f32_16x16x32_bf16 v[104:107], a[12:15], a[156:159], v[104:107]\n"
    "v_mfma_f32_16x16x32_bf16 v[104:107], a[64:67], v[200:203], v[104:107]\n"
    "v_mfma_f32_16x16x32_bf16 v[104:107], a[68:71], v[216:219], v[104:107]\n"
    "v_mul_f32 v148, v9, v148\n"
    "v_mul_f32 v149, v9, v149\n"
    "v_mfma_f32_16x16x32_bf16 v[104:107], a[72:75], v[232:235], v[104:107]\n"
    "v_mul_f32 v150, v9, v150\n"
    "v_mul_f32 v151, v9, v151\n"
    "v_mfma_f32_16x16x32_bf16 v[104:107], a[76:79], v[248:251], v[104:107]\n"
    "v_cvt_pk_bf16_f32 v134, v148, v149\n"
    "v_cvt_pk_bf16_f32 v135, v150, v151\n"
    "v_mfma_f32_16x16x32_bf16 v[120:123], a[16:19], a[132:135], v[120:123]\n"
    "v_permlane16_swap_b32 v132, v134\n"
    "v_mfma_f32_16x16x32_bf16 v[120:123], a[20:23], a[140:143], v[120:123]\n"
    "v_permlane16_swap_b32 v133, v135\n"
    "v_mfma_f32_16x16x32_bf16 v[120:123], a[24:27], a[148:151], v[120:123]\n"
    "v_mov_b32_dpp v100, v132 row_ror:8 row_mask:0xf bank_mask:0xc\n"
    "v_mov_b32_dpp v101, v133 row_ror:8 row_mask:0xf bank_mask:0xc\n"
    "v_mfma_f32_16x16x32_bf16 v[120:123], a[28:31], a[156:159], v[120:123]\n"
    "v_mov_b32_dpp v102, v134 row_ror:8 row_mask:0xf bank_mask:0xc\n"
    "v_mov_b32_dpp v103, v135 row_ror:8 row_mask:0xf bank_mask:0xc\n"
    "v_mfma_f32_16x16x32_bf16 v[120:123], a[80:83], v[200:203], v[120:123]\n"
    "buffer_store_dwordx4 v[100:103], v6, s[48:51], s66 offen nt sc1\n"
    "v_mfma_f32_16x16x32_bf16 v[120:123], a[84:87], v[216:219], v[120:123]\n"
    "v_mov_b32_dpp v132, v116 row_ror:8 row_mask:0xf bank_mask:0x3\n"
    "v_mov_b32_dpp v133, v117 row_ror:8 row_mask:0xf bank_mask:0x3\n"
    "v_mfma_f32_16x16x32_bf16 v[120:123], a[88:91], v[232:235], v[120:123]\n"
    "v_mov_b32_dpp v134, v118 row_ror:8 row_mask:0xf bank_mask:0x3\n"
    "v_mov_b32_dpp v135, v119 row_ror:8 row_mask:0xf bank_mask:0x3\n"
    "v_mfma_f32_16x16x32_bf16 v[120:123], a[92:95], v[248:251], v[120:123]\n"
    "v_mul_f32 v104, v10, v104\n"
    "v_mul_f32 v105, v10, v105\n"
    "v_mfma_f32_16x16x32_bf16 v[136:139], a[32:35], a[132:135], v[136:139]\n"
    "v_mul_f32 v106, v10, v106\n"
    "v_mul_f32 v107, v10, v107\n"
    "v_mfma_f32_16x16x32_bf16 v[136:139], a[36:39], a[140:143], v[136:139]\n"
    "v_cvt_pk_bf16_f32 v104, v104, v105\n"
    "v_mfma_f32_16x16x32_bf16 v[136:139], a[40:43], a[148:151], v[136:139]\n"
    "v_cvt_pk_bf16_f32 v105, v106, v107\n"
    "v_mfma_f32_16x16x32_bf16 v[136:139], a[44:47], a[156:159], v[136:139]\n"
    "v_mfma_f32_16x16x32_bf16 v[136:139], a[96:99], v[200:203], v[136:139]\n"
    "v_mfma_f32_16x16x32_bf16 v[136:139], a[100:103], v[216:219], v[136:139]\n"
    "v_mul_f32 v120, v10, v120\n"
    "v_mul_f32 v121, v10, v121\n"
    "v_mfma_f32_16x16x32_bf16 v[136:139], a[104:107], v[232:235], v[136:139]\n"
    "buffer_store_dwordx4 v[132:135], v6, s[48:51], s67 offen nt sc1\n"
    "v_mfma_f32_16x16x32_bf16 v[136:139], a[108:111], v[248:251], v[136:139]\n"
    "v_mul_f32 v122, v10, v122\n"
    "v_mul_f32 v123, v10, v123\n"
    "v_mfma_f32_16x16x32_bf16 v[152:155], a[48:51], a[132:135], v[152:155]\n"
    "v_cvt_pk_bf16_f32 v106, v120, v121\n"
    "v_cvt_pk_bf16_f32 v107, v122, v123\n"
    "v_mfma_f32_16x16x32_bf16 v[152:155], a[52:55], a[140:143], v[152:155]\n"
    "v_permlane16_swap_b32 v104, v106\n"
    "v_mfma_f32_16x16x32_bf16 v[152:155], a[56:59], a[148:151], v[152:155]\n"
    "v_permlane16_swap_b32 v105, v107\n"
    "v_mfma_f32_16x16x32_bf16 v[152:155], a[60:63], a[156:159], v[152:155]\n"
    "v_mov_b32 v120, v104\n"
    "v_mov_b32 v121, v105\n"
    "v_mfma_f32_16x16x32_bf16 v[152:155], a[112:115], v[200:203], v[152:155]\n"
    "v_mov_b32 v122, v106\n"
    "v_mov_b32 v123, v107\n"
    "v_mfma_f32_16x16x32_bf16 v[152:155], a[116:119], v[216:219], v[152:155]\n"
    "v_mul_f32 v136, v10, v136\n"
    "v_mul_f32 v137, v10, v137\n"
    "v_mfma_f32_16x16x32_bf16 v[152:155], a[120:123], v[232:235], v[152:155]\n"
    "v_mul_f32 v138, v10, v138\n"
    "v_mul_f32 v139, v10, v139\n"
    "v_mfma_f32_16x16x32_bf16 v[152:155], a[124:127], v[248:251], v[152:155]\n"
    "v_cvt_pk_bf16_f32 v136, v136, v137\n"
    "v_mfma_f32_16x16x32_bf16 v[108:111], a[0:3], v[164:167], v[108:111]\n"
    "v_cvt_pk_bf16_f32 v137, v138, v139\n"
    "v_mfma_f32_16x16x32_bf16 v[108:111], a[4:7], v[172:175], v[108:111]\n"
    "v_mfma_f32_16x16x32_bf16 v[108:111], a[8:11], v[180:183], v[108:111]\n"
    "v_mfma_f32_16x16x32_bf16 v[108:111], a[12:15], v[188:191], v[108:111]\n"
    "v_mfma_f32_16x16x32_bf16 v[108:111], a[64:67], v[204:207], v[108:111]\n"
    "v_mfma_f32_16x16x32_bf16 v[108:111], a[68:71], v[220:223], v[108:111]\n"
    "v_mul_f32 v152, v10, v152\n"
    "v_mul_f32 v153, v10, v153\n"
    "v_mfma_f32_16x16x32_bf16 v[108:111], a[72:75], v[236:239], v[108:111]\n"
    "v_mul_f32 v154, v10, v154\n"
    "v_mul_f32 v155, v10, v155\n"
    "v_mfma_f32_16x16x32_bf16 v[108:111], a[76:79], v[252:255], v[108:111]\n"
    "v_cvt_pk_bf16_f32 v138, v152, v153\n"
    "v_cvt_pk_bf16_f32 v139, v154, v155\n"
    "v_mfma_f32_16x16x32_bf16 v[124:127], a[16:19], v[164:167], v[124:127]\n"
    "v_permlane16_swap_b32 v136, v138\n"
    "v_mfma_f32_16x16x32_bf16 v[124:127], a[20:23], v[172:175], v[124:127]\n"
    "v_permlane16_swap_b32 v137, v139\n"
    "v_mfma_f32_16x16x32_bf16 v[124:127], a[24:27], v[180:183], v[124:127]\n"
    "v_mov_b32_dpp v104, v136 row_ror:8 row_mask:0xf bank_mask:0xc\n"
    "v_mov_b32_dpp v105, v137 row_ror:8 row_mask:0xf bank_mask:0xc\n"
    "v_mfma_f32_16x16x32_bf16 v[124:127], a[28:31], v[188:191], v[124:127]\n"
    "v_mov_b32_dpp v106, v138 row_ror:8 row_mask:0xf bank_mask:0xc\n"
    "v_mov_b32_dpp v107, v139 row_ror:8 row_mask:0xf bank_mask:0xc\n"
    "v_mfma_f32_16x16x32_bf16 v[124:127], a[80:83], v[204:207], v[124:127]\n"
    "buffer_store_dwordx4 v[104:107], v6, s[48:51], s68 offen nt sc1\n"
    "v_mfma_f32_16x16x32_bf16 v[124:127], a[84:87], v[220:223], v[124:127]\n"
    "v_mov_b32_dpp v136, v120 row_ror:8 row_mask:0xf bank_mask:0x3\n"
    "v_mov_b32_dpp v137, v121 row_ror:8 row_mask:0xf bank_mask:0x3\n"
    "v_mfma_f32_16x16x32_bf16 v[124:127], a[88:91], v[236:239], v[124:127]\n"
    "v_mov_b32_dpp v138, v122 row_ror:8 row_mask:0xf bank_mask:0x3\n"
    "v_mov_b32_dpp v139, v123 row_ror:8 row_mask:0xf bank_mask:0x3\n"
    "v_mfma_f32_16x16x32_bf16 v[124:127], a[92:95], v[252:255], v[124:127]\n"
    "v_mul_f32 v108, v11, v108\n"
    "v_mul_f32 v109, v11, v109\n"
    "v_mfma_f32_16x16x32_bf16 v[140:143], a[32:35], v[164:167], v[140:143]\n"
    "v_mul_f32 v110, v11, v110\n"
    "v_mul_f32 v111, v11, v111\n"
    "v_mfma_f32_16x16x32_bf16 v[140:143], a[36:39], v[172:175], v[140:143]\n"
    "v_cvt_pk_bf16_f32 v108, v108, v109\n"
    "v_mfma_f32_16x16x32_bf16 v[140:143], a[40:43], v[180:183], v[140:143]\n"
    "v_cvt_pk_bf16_f32 v109, v110, v111\n"
    "v_mfma_f32_16x16x32_bf16 v[140:143], a[44:47], v[188:191], v[140:143]\n"
    "v_mfma_f32_16x16x32_bf16 v[140:143], a[96:99], v[204:207], v[140:143]\n"
    "v_mfma_f32_16x16x32_bf16 v[140:143], a[100:103], v[220:223], v[140:143]\n"
    "v_mul_f32 v124, v11, v124\n"
    "v_mul_f32 v125, v11, v125\n"
    "v_mfma_f32_16x16x32_bf16 v[140:143], a[104:107], v[236:239], v[140:143]\n"
    "buffer_store_dwordx4 v[136:139], v6, s[48:51], s69 offen nt sc1\n"
    "v_mfma_f32_16x16x32_bf16 v[140:143], a[108:111], v[252:255], v[140:143]\n"
    "v_mul_f32 v126, v11, v126\n"
    "v_mul_f32 v127, v11, v127\n"
    "v_mfma_f32_16x16x32_bf16 v[156:159], a[48:51], v[164:167], v[156:159]\n"
    "v_cvt_pk_bf16_f32 v110, v124, v125\n"
    "v_cvt_pk_bf16_f32 v111, v126, v127\n"
    "v_mfma_f32_16x16x32_bf16 v[156:159], a[52:55], v[172:175], v[156:159]\n"
    "v_permlane16_swap_b32 v108, v110\n"
    "v_mfma_f32_16x16x32_bf16 v[156:159], a[56:59], v[180:183], v[156:159]\n"
    "v_permlane16_swap_b32 v109, v111\n"
    "v_mfma_f32_16x16x32_bf16 v[156:159], a[60:63], v[188:191], v[156:159]\n"
    "v_mov_b32 v124, v108\n"
    "v_mov_b32 v125, v109\n"
    "v_mfma_f32_16x16x32_bf16 v[156:159], a[112:115], v[204:207], v[156:159]\n"
    "v_mov_b32 v126, v110\n"
    "v_mov_b32 v127, v111\n"
    "v_mfma_f32_16x16x32_bf16 v[156:159], a[116:119], v[220:223], v[156:159]\n"
    "v_mul_f32 v140, v11, v140\n"
    "v_mul_f32 v141, v11, v141\n"
    "v_mfma_f32_16x16x32_bf16 v[156:159], a[120:123], v[236:239], v[156:159]\n"
    "v_mul_f32 v142, v11, v142\n"
    "v_mul_f32 v143, v11, v143\n"
    "v_mfma_f32_16x16x32_bf16 v[156:159], a[124:127], v[252:255], v[156:159]\n"
    "v_cvt_pk_bf16_f32 v140, v140, v141\n"
    "s_nop 6\n"
    "v_mul_f32 v156, v11, v156\n"
    "v_mul_f32 v157, v11, v157\n"
    "v_mul_f32 v158, v11, v158\n"
    "v_mul_f32 v159, v11, v159\n"
    "v_cvt_pk_bf16_f32 v141, v142, v143\n"
    "v_cvt_pk_bf16_f32 v142, v156, v157\n"
    "v_cvt_pk_bf16_f32 v143, v158, v159\n"
    "s_nop 0\n"
    "v_permlane16_swap_b32 v140, v142\n"
    "v_permlane16_swap_b32 v141, v143\n"
    "s_nop 0\n"
    "v_mov_b32_dpp v108, v140 row_ror:8 row_mask:0xf bank_mask:0xc\n"
    "v_mov_b32_dpp v109, v141 row_ror:8 row_mask:0xf bank_mask:0xc\n"
    "v_mov_b32_dpp v110, v142 row_ror:8 row_mask:0xf bank_mask:0xc\n"
    "v_mov_b32_dpp v111, v143 row_ror:8 row_mask:0xf bank_mask:0xc\n"
    "v_mov_b32_dpp v140, v124 row_ror:8 row_mask:0xf bank_mask:0x3\n"
    "v_mov_b32_dpp v141, v125 row_ror:8 row_mask:0xf bank_mask:0x3\n"
    "v_mov_b32_dpp v142, v126 row_ror:8 row_mask:0xf bank_mask:0x3\n"
    "buffer_store_dwordx4 v[108:111], v6, s[48:51], s70 offen nt sc1\n"
    "v_mov_b32_dpp v143, v127 row_ror:8 row_mask:0xf bank_mask:0x3\n"
    "buffer_store_dwordx4 v[140:143], v6, s[48:51], s71 offen nt sc1\n"
    "s_lshl_b32 s85, s84, 2\n"
    "s_mov_b32 s94, 0x24000\n"
    "s_add_u32 s85, s85, s94\n"
    "v_mov_b32 v16, s85\n"
    "v_mov_b32 v17, s59\n"
    "v_mov_b32 v18, s94\n"
    "ds_write_b32 v16, v17\n"
    "s_waitcnt lgkmcnt(0)\n"
    "s_barrier\n"
    "ds_read_b128 v[20:23], v18\n"
    "s_waitcnt lgkmcnt(0)\n"
    "v_or3_b32 v16, v20, v21, v22\n"
    "v_or_b32 v16, v16, v23\n"
    "s_nop 1\n"
    "v_readfirstlane_b32 s59, v16\n"
    "s_nop 3\n"
    "s_cmp_lg_u32 s59, 0\n"
    "s_cbranch_scc1 L_end_%=\n"
    "s_endpgm\n"
    "L_end_%=:\n"
    "s_waitcnt vmcnt(0) lgkmcnt(0)\n"
    "s_barrier\n"
    "s_mov_b32 %0, s59\n"
    : "=s"(fail), "+{s[64:71]}"(ka)
    : "n"(0), "n"(0), "n"(0), "s"(wid), "s"(vpf),
      "s"(kv[0]), "s"(kv[1]), "s"(kv[2]), "s"(qo[0]), "s"(qo[1]), "s"(qo[2]), "s"(lim[0]), "s"(lim[1]), "s"(lim[2])
    : "memory", "vcc", "scc", "v0", "v1", "v2", "v3", "v4", "v5", "v6", "v7", "v8", "v9", "v10", "v11", "v12", "v13", "v14", "v15", "v16", "v17", "v18", "v19", "v20", "v21", "v22", "v23", "v24", "v25", "v26", "v27", "v28", "v29", "v30", "v31", "v32", "v33", "v34", "v35", "v36", "v37", "v38", "v39", "v40", "v41", "v42", "v43", "v44", "v45", "v46", "v47", "v48", "v49", "v50", "v51", "v52", "v53", "v54", "v55", "v56", "v57", "v58", "v59", "v60", "v61", "v62", "v63", "v64", "v65", "v66", "v67", "v68", "v69", "v70", "v71", "v72", "v73", "v74", "v75", "v76", "v77", "v78", "v79", "v80", "v81", "v82", "v83", "v84", "v85", "v86", "v87", "v88", "v89", "v90", "v91", "v92", "v93", "v94", "v95", "v96", "v97", "v98", "v99", "v100", "v101", "v102", "v103", "v104", "v105", "v106", "v107", "v108", "v109", "v110", "v111", "v112", "v113", "v114", "v115", "v116", "v117", "v118", "v119", "v120", "v121", "v122", "v123", "v124", "v125", "v126", "v127", "v128", "v129", "v130", "v131", "v132", "v133", "v134", "v135", "v136", "v137", "v138", "v139", "v140", "v141", "v142", "v143", "v144", "v145", "v146", "v147", "v148", "v149", "v150", "v151", "v152", "v153", "v154", "v155", "v156", "v157", "v158", "v159", "v160", "v161", "v162", "v163", "v164", "v165", "v166", "v167", "v168", "v169", "v170", "v171", "v172", "v173", "v174", "v175", "v176", "v177", "v178", "v179", "v180", "v181", "v182", "v183", "v184", "v185", "v186", "v187", "v188", "v189", "v190", "v191", "v192", "v193", "v194", "v195", "v196", "v197", "v198", "v199", "v200", "v201", "v202", "v203", "v204", "v205", "v206", "v207", "v208", "v209", "v210", "v211", "v212", "v213", "v214", "v215", "v216", "v217", "v218", "v219", "v220", "v221", "v222", "v223", "v224", "v225", "v226", "v227", "v228", "v229", "v230", "v231", "v232", "v233", "v234", "v235", "v236", "v237", "v238", "v239", "v240", "v241", "v242", "v243", "v244", "v245", "v246", "v247", "v248", "v249", "v250", "v251", "v252", "v253", "v254", "v255", "a0", "a1", "a2", "a3", "a4", "a5", "a6", "a7", "a8", "a9", "a10", "a11", "a12", "a13", "a14", "a15", "a16", "a17", "a18", "a19", "a20", "a21", "a22", "a23", "a24", "a25", "a26", "a27", "a28", "a29", "a30", "a31", "a32", "a33", "a34", "a35", "a36", "a37", "a38", "a39", "a40", "a41", "a42", "a43", "a44", "a45", "a46", "a47", "a48", "a49", "a50", "a51", "a52", "a53", "a54", "a55", "a56", "a57", "a58", "a59", "a60", "a61", "a62", "a63", "a64", "a65", "a66", "a67", "a68", "a69", "a70", "a71", "a72", "a73", "a74", "a75", "a76", "a77", "a78", "a79", "a80", "a81", "a82", "a83", "a84", "a85", "a86", "a87", "a88", "a89", "a90", "a91", "a92", "a93", "a94", "a95", "a96", "a97", "a98", "a99", "a100", "a101", "a102", "a103", "a104", "a105", "a106", "a107", "a108", "a109", "a110", "a111", "a112", "a113", "a114", "a115", "a116", "a117", "a118", "a119", "a120", "a121", "a122", "a123", "a124", "a125", "a126", "a127", "a128", "a129", "a130", "a131", "a132", "a133", "a134", "a135", "a136", "a137", "a138", "a139", "a140", "a141", "a142", "a143", "a144", "a145", "a146", "a147", "a148", "a149", "a150", "a151", "a152", "a153", "a154", "a155", "a156", "a157", "a158", "a159", "a160", "a161", "a162", "a163", "a164", "a165", "a166", "a167", "a168", "a169", "a170", "a171", "a172", "a173", "a174", "a175", "a176", "a177", "a178", "a179", "a180", "a181", "a182", "a183", "a184", "a185", "a186", "a187", "a188", "a189", "a190", "a191", "a192", "a193", "a194", "a195", "a196", "a197", "a198", "a199", "a200", "a201", "a202", "a203", "a204", "a205", "a206", "a207", "a208", "a209", "a210", "a211", "a212", "a213", "a214", "a215", "a216", "a217", "a218", "a219", "a220", "a221", "a222", "a223", "a224", "a225", "a226", "a227", "a228", "a229", "a230", "a231", "a232", "a233", "a234", "a235", "a236", "a237", "a238", "a239", "a240", "a241", "a242", "a243", "a244", "a245", "a246", "a247", "a248", "a249", "a250", "a251", "a252", "a253", "a254", "a255", "s35", "s36", "s37", "s38", "s39", "s40", "s41", "s42", "s43", "s44", "s45", "s46", "s47", "s48", "s49", "s50", "s51", "s52", "s53", "s54", "s55", "s56", "s57", "s58", "s59", "s60", "s61", "s62", "s63", "s72", "s73", "s74", "s75", "s76", "s77", "s78", "s79", "s80", "s81", "s82", "s83", "s84", "s85", "s86", "s87", "s88", "s89", "s90", "s91", "s92", "s93", "s94", "s95", "s96", "s97", "s98", "s99");
  const int tid = wid * 64 + __builtin_amdgcn_mbcnt_hi(~0u, __builtin_amdgcn_mbcnt_lo(~0u, 0u));
  const unsigned any = fail;
  if (any) {
    for (int r = 0; r < 6; ++r) {
      if (!((any >> (r >> 1)) & 1u)) continue;
      unsigned id = id0 + 32u * (r >> 1), head = id / 3u, j = id - 3u * head;
      safe_rows(Q, K, V, O, head, j * 256 + (r & 1) * 128, lds, tid);
    }
  }
}

extern "C" void kernel_launch(void* const* d_in, const int* in_sizes, int n_in, void* d_out, int out_size, void* d_ws, size_t ws_size,
                              hipStream_t stream) {
  mha_fwd<<<256, 256, 0, stream>>>((const unsigned short*)d_in[0], (const unsigned short*)d_in[1], (const unsigned short*)d_in[2],
                                   (unsigned short*)d_out);
}
```
